```python
import torch

def _softmax(x, axis=-1):
    return torch.softmax(x, dim=axis)
def _gelu(x):
    return torch.nn.functional.gelu(x, approximate="tanh")
def _silu(x):
    return torch.nn.functional.silu(x)
def _sigmoid(x):
    return torch.sigmoid(x)
def _layernorm(x, axis=-1, eps=1e-5):
    m = x.mean(dim=axis, keepdim=True)
    v = x.var(dim=axis, keepdim=True, unbiased=False)
    return (x - m) / torch.sqrt(v + eps)
def _scatter_add(tgt, idx, src, axis=0):
    return tgt.clone().index_add_(axis, idx, src)
def _segment_sum(src, seg, n):
    out = torch.zeros((n,) + src.shape[1:], dtype=src.dtype)
    return out.index_add_(0, seg, src)
def _pad(x, pw):
    flat = tuple(v for pair in reversed(pw) for v in pair)
    return torch.nn.functional.pad(x, flat)

kernel_name = 'recurrent_scan__linear_kernel_hedgehog__dense_activation_mish__prompt'

def setup_inputs():

    _w = (lambda a: (lambda t: t.to(torch.bfloat16) if t.is_floating_point() else t)(torch.as_tensor(a)))
    def _A_setup():
        B, S, D, H = 1, 2048, 576, 32
        Dh = D // H
        return {
            "x": _w((lambda *s: torch.randn(*s, dtype=torch.bfloat16) * 0.05)(B, S, D)),
            "wqkv": _w((lambda *s: torch.randn(*s, dtype=torch.bfloat16) * 0.4)(D, 3 * D)),
            "w_phi": _w((lambda *s: torch.randn(*s, dtype=torch.bfloat16) * 0.4)(H, Dh, Dh) * (Dh ** -0.5)),
            "wo": _w((lambda *s: torch.randn(*s, dtype=torch.bfloat16) * 0.2)(D, D)),
        }
    _A_in = _A_setup()

    def _B_setup():
        B, S, D, F = 1, 2048, 576, 4608
        return {
            "x": _w((lambda *s: torch.randn(*s, dtype=torch.bfloat16) * 0.05)(B, S, D)),
            "w1": _w((lambda *s: torch.randn(*s, dtype=torch.bfloat16) * 0.4)(D, F) * (D ** -0.5)),
            "b1": _w((lambda *s: torch.randn(*s, dtype=torch.bfloat16) * 0.4)(F)),
            "w2": _w((lambda *s: torch.randn(*s, dtype=torch.bfloat16) * 0.1)(F, D) * (F ** -0.5)),
        }
    _B_in = _B_setup()

    return {
        "A_x": _A_in["x"],
        "A_wqkv": _A_in["wqkv"],
        "A_w_phi": _A_in["w_phi"],
        "A_wo": _A_in["wo"],
        "B_x": _B_in["x"],
        "B_w1": _B_in["w1"],
        "B_b1": _B_in["b1"],
        "B_w2": _B_in["w2"],
    }

def reference(A_x, A_wqkv, A_w_phi, A_wo, B_x, B_w1, B_b1, B_w2):
    def _A_ref(x, wqkv, w_phi, wo):
        B, S, D = x.shape
        H = 32; Dh = D // H
        qkv = (lambda x, *d: x.permute(*d))((x @ wqkv).reshape(B, S, 3, H, Dh), 2, 0, 3, 1, 4)
        q, k, v = qkv[0], qkv[1], qkv[2]
        pq = torch.einsum("bhsd,hdf->bhsf", q, w_phi)
        pk = torch.einsum("bhsd,hdf->bhsf", k, w_phi)
        phi_q = torch.concatenate([_softmax(pq, axis=-1), _softmax(-pq, axis=-1)], axis=-1)
        phi_k = torch.concatenate([_softmax(pk, axis=-1), _softmax(-pk, axis=-1)], axis=-1)
        kv = torch.einsum("bhsf,bhsd->bhfd", phi_k, v)
        z  = torch.einsum("bhsf->bhf", phi_k)
        num = torch.einsum("bhqf,bhfd->bhqd", phi_q, kv)
        den = torch.einsum("bhqf,bhf->bhq", phi_q, z)[..., None] + 1e-6
        o = num / den
        return (lambda x, *d: x.permute(*d))(o, 0, 2, 1, 3).reshape(B, S, D) @ wo

    def _B_ref(x, w1, b1, w2):
        h = torch.einsum("bsd,df->bsf", x, w1) + b1
        sp = torch.log1p(torch.exp(-torch.abs(h))) + torch.where(h > 0, h, torch.zeros_like(h))
        act = h * torch.tanh(sp)
        return torch.einsum("bsf,fd->bsd", act, w2)

    x = A_x
    xn = _layernorm(x, axis=-1)
    s = _A_ref(xn, A_wqkv, A_w_phi, A_wo)
    g = _silu(_B_ref(xn, B_w1, B_b1, B_w2))
    return x + s * g
```

```cpp
#include <hip/hip_runtime.h>
#include <hip/hip_bf16.h>
typedef unsigned short u16;
typedef unsigned int u32;
typedef short bf16x8 __attribute__((ext_vector_type(8)));
typedef short bf16x4 __attribute__((ext_vector_type(4)));
typedef float f32x16 __attribute__((ext_vector_type(16)));
typedef float f32x4 __attribute__((ext_vector_type(4)));
typedef float f32x2 __attribute__((ext_vector_type(2)));
typedef u32 u32x4 __attribute__((ext_vector_type(4)));
typedef int i32x4 __attribute__((ext_vector_type(4)));
typedef __bf16 bf2_t __attribute__((ext_vector_type(2)));
#define LAS __attribute__((address_space(3)))
constexpr float LOG2E = 1.4426950408889634f;
constexpr size_t WS_XN = 0;
constexpr size_t WS_WF = 2359296;
constexpr size_t WS_KV = WS_WF + 4 * 720 * 1024;
constexpr size_t WS_CNT = WS_KV + 32 * 19 * 40 * 4;
constexpr size_t WS_PART = WS_CNT + 3072;
constexpr size_t PART_B = 2359296;
constexpr int LDS_B = 149504;

__device__ __forceinline__ float bfl(u32 w) { return __uint_as_float(w << 16); }
__device__ __forceinline__ float bfh(u32 w) { return __uint_as_float(w & 0xffff0000u); }
__device__ __forceinline__ float bf1(u16 w) { return __uint_as_float((u32)w << 16); }
__device__ __forceinline__ u32 pk2(float a, float b) {
  f32x2 v = {a, b};
  bf2_t r = __builtin_convertvector(v, bf2_t);
  return __builtin_bit_cast(u32, r);
}

constexpr int NB_LN = 128, NB_FOLD = 432, NB_WO = 130, NB_PF = 264;
typedef u32 u32x2 __attribute__((ext_vector_type(2)));
__global__ __launch_bounds__(320) void prep_kernel(const u16* __restrict__ x, const u16* __restrict__ wqkv, const u16* __restrict__ wphi,
                                                   const u16* __restrict__ wo, char* __restrict__ ws, const char* __restrict__ w1,
                                                   const char* __restrict__ w2, const char* __restrict__ out) {
  __shared__ __attribute__((aligned(16))) float sw[144 * 20];
  __shared__ __attribute__((aligned(16))) float sp[8 * 324];
  int b = blockIdx.x, tid = threadIdx.x;
  if (b >= NB_LN + NB_FOLD + NB_WO + 6) {
    b -= NB_LN + NB_FOLD + NB_WO + 6;
    int xc = b & 7, i = (b >> 3) * 320 + tid, f0 = 1152 * (xc >> 1) + 192 * (xc & 1);
    if (i < 10368) {
      int j = i / 3456, r = i % 3456;
      u32 a = *(const u32*)(w1 + (size_t)(r / 6) * 9216 + (f0 + 384 * j) * 2 + (r % 6) * 64);
      u32 c = *(const u32*)(w2 + (size_t)(f0 + 384 * j) * 1152 + r * 64);
      u32 d = i < 576 ? *(const u32*)(ws + WS_PART + (size_t)i * 65536) : i < 612 ? *(const u32*)(out + (size_t)(i - 576) * 65536) : 0;
      asm volatile("" :: "v"(a), "v"(c), "v"(d));
    }
    return;
  }
  if (b >= NB_LN && b < NB_LN + NB_FOLD) {
    b -= NB_LN;
    int hq = b / 108, ks = (b / 3) % 36, g = b % 3;
    int t = tid < 288 ? tid : 287, k = t / 36, c4 = t % 36, seg = c4 / 9, w = (c4 % 9) * 4;
    int col = g == 2 ? 144 * hq + 4 * c4 : 576 * (1 + (seg & 1)) + (8 * hq + 4 * (seg >> 1) + 2 * g) * 18 + w;
    const u16* src = wqkv + (16 * ks + k) * 1728 + col;
    u32x2 a0 = *(const u32x2*)src, a1 = *(const u32x2*)(src + 8 * 1728);
    const u32x2* pp = (const u32x2*)(wphi + hq * 2592);
    u32x2 p0 = pp[tid], p1 = pp[tid + 320], p2 = pp[tid < 8 ? tid + 640 : 647];
#pragma unroll
    for (int e = 0; e < 4; e++) {
      int we = w + e, c = g == 2 ? 4 * c4 + e : (seg >> 1) * 72 + (we >= 18 ? 18 + we : we) + (seg & 1) * 18;
      sw[c * 20 + k] = (e & 1) ? bfh(a0[e >> 1]) : bfl(a0[e >> 1]);
      sw[c * 20 + k + 8] = (e & 1) ? bfh(a1[e >> 1]) : bfl(a1[e >> 1]);
    }
    *(f32x4*)&sp[tid * 4] = f32x4{bfl(p0[0]), bfh(p0[0]), bfl(p0[1]), bfh(p0[1])};
    *(f32x4*)&sp[(tid + 320) * 4] = f32x4{bfl(p1[0]), bfh(p1[0]), bfl(p1[1]), bfh(p1[1])};
    if (tid < 8) *(f32x4*)&sp[(tid + 640) * 4] = f32x4{bfl(p2[0]), bfh(p2[0]), bfl(p2[1]), bfh(p2[1])};
    __syncthreads();
    int tile = tid >> 6, l = tid & 63, r = l & 31, h = l >> 5, hp = (r >> 2) & 1, slot = 16 * tile + 4 * (r >> 3) + (r & 3);
    float o[8] = {0, 0, 0, 0, 0, 0, 0, 0};
    if (slot < 72) {
      int blk = slot / 18, f = slot % 18, cb = hp * 72 + blk * 18;
      if (g < 2 && (blk & 1)) {
        const f32x4* p = (const f32x4*)&sw[(cb + f) * 20 + 8 * h];
        f32x4 a = p[0], c = p[1];
        for (int j = 0; j < 4; j++) { o[j] = a[j]; o[4 + j] = c[j]; }
      } else {
        int hl = 4 * hp + (g == 2 ? blk : 2 * g + (blk >> 1));
#pragma unroll
        for (int d = 0; d < 18; d++) {
          const f32x4* p = (const f32x4*)&sw[(cb + d) * 20 + 8 * h];
          f32x4 a = p[0], c = p[1];
          float w = sp[hl * 324 + d * 18 + f];
          for (int j = 0; j < 4; j++) { o[j] += a[j] * w; o[4 + j] += c[j] * w; }
        }
      }
    }
    u32x4 v = {pk2(o[0], o[1]), pk2(o[2], o[3]), pk2(o[4], o[5]), pk2(o[6], o[7])};
    *(u32x4*)(ws + WS_WF + (size_t)(hq * 720 + (g * 5 + tile) * 36 + ks) * 1024 + l * 16) = v;
    return;
  }
  if (b < NB_LN) {
    if (tid >= 256) {
      if (tid < 305) *(u32x4*)(ws + WS_KV + (size_t)(b * 49 + tid - 256) * 16) = u32x4{0, 0, 0, 0};
      return;
    }
    int l = tid & 63, t = b * 16 + (tid >> 6) * 4 + (l & 3), sub = l >> 2;
    const bool last = sub < 8;
    u32x4 v[5];
#pragma unroll
    for (int i = 0; i < 4; i++) v[i] = *(const u32x4*)(x + t * 576 + (sub + 16 * i) * 8);
    v[4] = *(const u32x4*)(x + t * 576 + ((sub & 7) + 64) * 8);
    float e[40], s0 = 0, s1 = 0, s2 = 0;
#pragma unroll
    for (int i = 0; i < 20; i++) { e[2 * i] = bfl(v[i >> 2][i & 3]); e[2 * i + 1] = bfh(v[i >> 2][i & 3]); }
#pragma unroll
    for (int i = 0; i < 16; i++) { s0 += e[2 * i]; s1 += e[2 * i + 1]; }
#pragma unroll
    for (int i = 32; i < 40; i++) s2 += e[i];
    float s = s0 + s1 + (last ? s2 : 0.f);
    s += __shfl_xor(s, 4); s += __shfl_xor(s, 8); s += __shfl_xor(s, 16); s += __shfl_xor(s, 32);
    float m = s * (1.0f / 576.0f);
    s0 = s1 = s2 = 0;
#pragma unroll
    for (int i = 0; i < 40; i++) e[i] -= m;
#pragma unroll
    for (int i = 0; i < 16; i++) { s0 += e[2 * i] * e[2 * i]; s1 += e[2 * i + 1] * e[2 * i + 1]; }
#pragma unroll
    for (int i = 32; i < 40; i++) s2 += e[i] * e[i];
    float q = s0 + s1 + (last ? s2 : 0.f);
    q += __shfl_xor(q, 4); q += __shfl_xor(q, 8); q += __shfl_xor(q, 16); q += __shfl_xor(q, 32);
    float rs = LOG2E / sqrtf(q * (1.0f / 576.0f) + 1e-5f);
#pragma unroll
    for (int i = 0; i < 5; i++) {
      u32x4 o; int c = sub + 16 * i;
      for (int j = 0; j < 4; j++) o[j] = pk2(e[8 * i + 2 * j] * rs, e[8 * i + 2 * j + 1] * rs);
      if (i < 4 || last) *(u32x4*)(ws + WS_XN + (size_t)(((t >> 5) * 36 + (c >> 1)) * 64 + (t & 31) + 32 * (c & 1)) * 16) = o;
    }
    return;
  }
  b -= NB_LN + NB_FOLD;
  int pc = b * 5 + (tid >> 6);
  if (pc < 648) {
    int hq = pc / 162, p = pc % 162, T = p / 9, ks = p % 9, l = tid & 63, r = l & 31, h = l >> 5;
    const u16* src = wo + (144 * hq + 72 * h + 8 * ks) * 576 + 32 * T + 16 * ((r >> 2) & 1) + 4 * (r >> 3) + (r & 3);
    u32x4 v;
#pragma unroll
    for (int j = 0; j < 4; j++) v[j] = (u32)src[(2 * j) * 576] | ((u32)src[(2 * j + 1) * 576] << 16);
    *(u32x4*)(ws + WS_WF + (size_t)(hq * 720 + 540 + p) * 1024 + l * 16) = v;
  }
}

__global__ __launch_bounds__(256, 1) void main_kernel(const u16* __restrict__ w1, const u16* __restrict__ b1, const u16* __restrict__ w2,
                                                      char* __restrict__ ws, const u16* __restrict__ x, u16* __restrict__ out) {
  const int xq = (blockIdx.x & 7) >> 1, rj = (blockIdx.x >> 3) & 3, role = rj == 3 ? 12 + xq : 3 * xq + rj, mt = (blockIdx.x & 1) * 8 + (blockIdx.x >> 5);
  const int tid = threadIdx.x, l = tid & 63, wv = __builtin_amdgcn_readfirstlane(tid >> 6);
  const size_t row0 = (size_t)(mt * 128 + (role >> 2) * 32) * 576;
#define TAIL_OPS "s"(ws + WS_PART + (size_t)(mt * 4 + (role >> 2)) * 36864), "s"(x + row0), "s"(out + row0), "s"(ws + WS_CNT + 1024 + mt * 128), "s"(role & 3)
  if (role >= 12) {
    const int hq = role - 12, h = l >> 5, r = l & 31, hs = wv >> 1, nt = wv & 1;
    int img_w = 110592 + (wv * 32 + r) * 240 + h * 112;
    int img_r = 110592 + (8 * h + ((l >> 2) & 3)) * 240 + hs * 112 + ((l >> 4) & 1) * 32 + (l & 3) * 8;
    int atv = (hs * 4 * 760 + 4 * h * 40 + nt * 32 + r) * 4;
    asm volatile(
"s_nop 4\n"
"s_getpc_b64 s[40:41]\n"
"CST_%=:\n"
"v_mbcnt_lo_u32_b32 v250, -1, 0\n"
"v_mbcnt_hi_u32_b32 v250, -1, v250\n"
"v_lshlrev_b32 v250, 6, v250\n"
"s_lshl_b32 s42, %10, 12\n"
"v_add_u32 v250, s42, v250\n"
"s_mov_b32 s42, CEN_%=-CST_%=-4\n"
"v_min_u32 v251, s42, v250\n"
"global_load_dword v253, v251, s[40:41]\n"
"v_add_u32 v250, 0x4000, v250\n"
"s_nop 1\n"
"v_min_u32 v251, s42, v250\n"
"global_load_dword v253, v251, s[40:41]\n"
"v_add_u32 v250, 0x4000, v250\n"
"s_nop 1\n"
"v_min_u32 v251, s42, v250\n"
"global_load_dword v253, v251, s[40:41]\n"
"v_add_u32 v250, 0x4000, v250\n"
"s_nop 1\n"
"v_mov_b32 v8, %0\n"
"v_mov_b32 v9, %1\n"
"v_mov_b32 v10, %2\n"
"v_mov_b32 v11, %3\n"
"v_mov_b32 v12, %4\n"
"v_mov_b32 v0, v8\n"
"v_add_u32 v1, 0x10000, v8\n"
"v_mov_b32 v2, v9\n"
"v_add_u32 v3, 72, v10\n"
"v_mov_b32 v5, v11\n"
"v_mov_b32 v6, v12\n"
"s_mov_b64 s[40:41], %5\n"
"s_mov_b32 s42, 718848\n"
"s_mov_b32 s43, 0x20000\n"
"s_mul_i32 s45, %10, 9216\n"
"s_mov_b64 s[46:47], %7\n"
"s_mov_b64 s[50:51], %6\n"
"s_and_b32 s56, %10, 1\n"
"s_lshl_b32 s57, s56, 6\n"
"v_add_u32 v4, s57, v10\n"
"s_cmp_eq_u32 s56, 0\n"
"s_cselect_b32 s48, -1, 15\n"
"s_mov_b32 s49, s48\n"
"s_mov_b64 s[52:53], %7\n"
"s_mov_b32 s54, 24320\n"
"s_mov_b32 s55, 0x20000\n"
"s_add_u32 m0, s45, 0\n"
"s_add_u32 s44, s45, 0\n"
"buffer_load_dwordx4 v0, s[40:43], s44 offen offset:0 lds\n"
"global_load_dwordx4 a[0:3], v0, s[50:51] offset:0\n"
"global_load_dwordx4 a[4:7], v0, s[50:51] offset:1024\n"
"global_load_dwordx4 a[8:11], v0, s[50:51] offset:2048\n"
"global_load_dwordx4 a[12:15], v0, s[50:51] offset:3072\n"
"s_add_u32 s50, s50, 4096\n"
"s_addc_u32 s51, s51, 0\n"
"buffer_load_dwordx4 v0, s[40:43], s44 offen offset:1024 lds\n"
"global_load_dwordx4 a[16:19], v0, s[50:51] offset:0\n"
"global_load_dwordx4 a[20:23], v0, s[50:51] offset:1024\n"
"global_load_dwordx4 a[24:27], v0, s[50:51] offset:2048\n"
"global_load_dwordx4 a[28:31], v0, s[50:51] offset:3072\n"
"s_add_u32 s50, s50, 4096\n"
"s_addc_u32 s51, s51, 0\n"
"buffer_load_dwordx4 v0, s[40:43], s44 offen offset:2048 lds\n"
"global_load_dwordx4 a[32:35], v0, s[50:51] offset:0\n"
"global_load_dwordx4 a[36:39], v0, s[50:51] offset:1024\n"
"global_load_dwordx4 a[40:43], v0, s[50:51] offset:2048\n"
"global_load_dwordx4 a[44:47], v0, s[50:51] offset:3072\n"
"s_add_u32 s50, s50, 4096\n"
"s_addc_u32 s51, s51, 0\n"
"buffer_load_dwordx4 v0, s[40:43], s44 offen offset:3072 lds\n"
"global_load_dwordx4 a[48:51], v0, s[50:51] offset:0\n"
"global_load_dwordx4 a[52:55], v0, s[50:51] offset:1024\n"
"global_load_dwordx4 a[56:59], v0, s[50:51] offset:2048\n"
"global_load_dwordx4 a[60:63], v0, s[50:51] offset:3072\n"
"s_add_u32 s50, s50, 4096\n"
"s_addc_u32 s51, s51, 0\n"
"s_add_u32 m0, s45, 4096\n"
"s_add_u32 s44, s45, 4096\n"
"buffer_load_dwordx4 v0, s[40:43], s44 offen offset:0 lds\n"
"global_load_dwordx4 a[64:67], v0, s[50:51] offset:0\n"
"global_load_dwordx4 a[68:71], v0, s[50:51] offset:1024\n"
"global_load_dwordx4 a[72:75], v0, s[50:51] offset:2048\n"
"global_load_dwordx4 a[76:79], v0, s[50:51] offset:3072\n"
"s_add_u32 s50, s50, 4096\n"
"s_addc_u32 s51, s51, 0\n"
"buffer_load_dwordx4 v0, s[40:43], s44 offen offset:1024 lds\n"
"global_load_dwordx4 a[80:83], v0, s[50:51] offset:0\n"
"global_load_dwordx4 a[84:87], v0, s[50:51] offset:1024\n"
"global_load_dwordx4 a[88:91], v0, s[50:51] offset:2048\n"
"global_load_dwordx4 a[92:95], v0, s[50:51] offset:3072\n"
"s_add_u32 s50, s50, 4096\n"
"s_addc_u32 s51, s51, 0\n"
"buffer_load_dwordx4 v0, s[40:43], s44 offen offset:2048 lds\n"
"global_load_dwordx4 a[96:99], v0, s[50:51] offset:0\n"
"global_load_dwordx4 a[100:103], v0, s[50:51] offset:1024\n"
"global_load_dwordx4 a[104:107], v0, s[50:51] offset:2048\n"
"global_load_dwordx4 a[108:111], v0, s[50:51] offset:3072\n"
"s_add_u32 s50, s50, 4096\n"
"s_addc_u32 s51, s51, 0\n"
"buffer_load_dwordx4 v0, s[40:43], s44 offen offset:3072 lds\n"
"global_load_dwordx4 a[112:115], v0, s[50:51] offset:0\n"
"global_load_dwordx4 a[116:119], v0, s[50:51] offset:1024\n"
"global_load_dwordx4 a[120:123], v0, s[50:51] offset:2048\n"
"global_load_dwordx4 a[124:127], v0, s[50:51] offset:3072\n"
"s_add_u32 s50, s50, 4096\n"
"s_addc_u32 s51, s51, 0\n"
"s_add_u32 m0, s45, 8192\n"
"s_add_u32 s44, s45, 8192\n"
"buffer_load_dwordx4 v0, s[40:43], s44 offen offset:0 lds\n"
"global_load_dwordx4 a[128:131], v0, s[50:51] offset:0\n"
"global_load_dwordx4 a[132:135], v0, s[50:51] offset:1024\n"
"global_load_dwordx4 a[136:139], v0, s[50:51] offset:2048\n"
"global_load_dwordx4 a[140:143], v0, s[50:51] offset:3072\n"
"s_add_u32 s50, s50, 4096\n"
"s_addc_u32 s51, s51, 0\n"
"s_add_u32 m0, s45, 36864\n"
"s_add_u32 s44, s45, 36864\n"
"buffer_load_dwordx4 v0, s[40:43], s44 offen offset:0 lds\n"
"buffer_load_dwordx4 v0, s[40:43], s44 offen offset:1024 lds\n"
"buffer_load_dwordx4 v0, s[40:43], s44 offen offset:2048 lds\n"
"buffer_load_dwordx4 v0, s[40:43], s44 offen offset:3072 lds\n"
"s_add_u32 m0, s45, 40960\n"
"s_add_u32 s44, s45, 40960\n"
"buffer_load_dwordx4 v0, s[40:43], s44 offen offset:0 lds\n"
"buffer_load_dwordx4 v0, s[40:43], s44 offen offset:1024 lds\n"
"buffer_load_dwordx4 v0, s[40:43], s44 offen offset:2048 lds\n"
"buffer_load_dwordx4 v0, s[40:43], s44 offen offset:3072 lds\n"
"s_add_u32 m0, s45, 45056\n"
"s_add_u32 s44, s45, 45056\n"
"buffer_load_dwordx4 v0, s[40:43], s44 offen offset:0 lds\n"
"v_mov_b32 v59, 0x3f80\n"
"s_waitcnt vmcnt(9)\n"
"s_barrier\n"
"ds_read_b128 v[8:11], v0 offset:0\n"
"ds_read_b128 v[12:15], v0 offset:1024\n"
"ds_read_b128 v[16:19], v0 offset:2048\n"
"ds_read_b128 v[20:23], v0 offset:3072\n"
"ds_read_b128 v[24:27], v0 offset:4096\n"
"ds_read_b128 v[28:31], v0 offset:5120\n"
"s_waitcnt lgkmcnt(4)\n"
"v_mfma_f32_32x32x16_bf16 v[96:111], v[8:11], a[0:3], 0\n"
"ds_read_b128 v[8:11], v0 offset:6144\n"
"v_mfma_f32_32x32x16_bf16 v[96:111], v[12:15], a[4:7], v[96:111]\n"
"ds_read_b128 v[12:15], v0 offset:7168\n"
"s_waitcnt lgkmcnt(4)\n"
"v_mfma_f32_32x32x16_bf16 v[96:111], v[16:19], a[8:11], v[96:111]\n"
"ds_read_b128 v[16:19], v0 offset:8192\n"
"s_add_u32 m0, s45, 73728\n"
"s_add_u32 s44, s45, 73728\n"
"buffer_load_dwordx4 v0, s[40:43], s44 offen offset:0 lds\n"
"v_mfma_f32_32x32x16_bf16 v[96:111], v[20:23], a[12:15], v[96:111]\n"
"ds_read_b128 v[20:23], v0 offset:9216\n"
"s_waitcnt lgkmcnt(4)\n"
"v_mfma_f32_32x32x16_bf16 v[96:111], v[24:27], a[16:19], v[96:111]\n"
"ds_read_b128 v[24:27], v0 offset:10240\n"
"v_mfma_f32_32x32x16_bf16 v[96:111], v[28:31], a[20:23], v[96:111]\n"
"ds_read_b128 v[28:31], v0 offset:11264\n"
"buffer_load_dwordx4 v0, s[40:43], s44 offen offset:1024 lds\n"
"s_waitcnt lgkmcnt(4)\n"
"v_mfma_f32_32x32x16_bf16 v[96:111], v[8:11], a[24:27], v[96:111]\n"
"ds_read_b128 v[8:11], v0 offset:12288\n"
"v_mfma_f32_32x32x16_bf16 v[96:111], v[12:15], a[28:31], v[96:111]\n"
"ds_read_b128 v[12:15], v0 offset:13312\n"
"s_waitcnt lgkmcnt(4)\n"
"v_mfma_f32_32x32x16_bf16 v[96:111], v[16:19], a[32:35], v[96:111]\n"
"ds_read_b128 v[16:19], v0 offset:14336\n"
"buffer_load_dwordx4 v0, s[40:43], s44 offen offset:2048 lds\n"
"v_mfma_f32_32x32x16_bf16 v[96:111], v[20:23], a[36:39], v[96:111]\n"
"ds_read_b128 v[20:23], v0 offset:15360\n"
"s_waitcnt lgkmcnt(4)\n"
"v_mfma_f32_32x32x16_bf16 v[96:111], v[24:27], a[40:43], v[96:111]\n"
"ds_read_b128 v[24:27], v0 offset:16384\n"
"v_mfma_f32_32x32x16_bf16 v[96:111], v[28:31], a[44:47], v[96:111]\n"
"ds_read_b128 v[28:31], v0 offset:17408\n"
"buffer_load_dwordx4 v0, s[40:43], s44 offen offset:3072 lds\n"
"s_waitcnt lgkmcnt(4)\n"
"v_mfma_f32_32x32x16_bf16 v[96:111], v[8:11], a[48:51], v[96:111]\n"
"ds_read_b128 v[8:11], v0 offset:18432\n"
"v_mfma_f32_32x32x16_bf16 v[96:111], v[12:15], a[52:55], v[96:111]\n"
"ds_read_b128 v[12:15], v0 offset:19456\n"
"s_waitcnt lgkmcnt(4)\n"
"v_mfma_f32_32x32x16_bf16 v[96:111], v[16:19], a[56:59], v[96:111]\n"
"ds_read_b128 v[16:19], v0 offset:20480\n"
"s_add_u32 m0, s45, 77824\n"
"s_add_u32 s44, s45, 77824\n"
"buffer_load_dwordx4 v0, s[40:43], s44 offen offset:0 lds\n"
"v_mfma_f32_32x32x16_bf16 v[96:111], v[20:23], a[60:63], v[96:111]\n"
"ds_read_b128 v[20:23], v0 offset:21504\n"
"s_waitcnt lgkmcnt(4)\n"
"v_mfma_f32_32x32x16_bf16 v[96:111], v[24:27], a[64:67], v[96:111]\n"
"ds_read_b128 v[24:27], v0 offset:22528\n"
"v_mfma_f32_32x32x16_bf16 v[96:111], v[28:31], a[68:71], v[96:111]\n"
"ds_read_b128 v[28:31], v0 offset:23552\n"
"buffer_load_dwordx4 v0, s[40:43], s44 offen offset:1024 lds\n"
"s_waitcnt lgkmcnt(4)\n"
"v_mfma_f32_32x32x16_bf16 v[96:111], v[8:11], a[72:75], v[96:111]\n"
"ds_read_b128 v[8:11], v0 offset:24576\n"
"v_mfma_f32_32x32x16_bf16 v[96:111], v[12:15], a[76:79], v[96:111]\n"
"ds_read_b128 v[12:15], v0 offset:25600\n"
"s_waitcnt lgkmcnt(4)\n"
"v_mfma_f32_32x32x16_bf16 v[96:111], v[16:19], a[80:83], v[96:111]\n"
"ds_read_b128 v[16:19], v0 offset:26624\n"
"buffer_load_dwordx4 v0, s[40:43], s44 offen offset:2048 lds\n"
"v_mfma_f32_32x32x16_bf16 v[96:111], v[20:23], a[84:87], v[96:111]\n"
"ds_read_b128 v[20:23], v0 offset:27648\n"
"s_waitcnt lgkmcnt(4)\n"
"v_mfma_f32_32x32x16_bf16 v[96:111], v[24:27], a[88:91], v[96:111]\n"
"ds_read_b128 v[24:27], v0 offset:28672\n"
"v_mfma_f32_32x32x16_bf16 v[96:111], v[28:31], a[92:95], v[96:111]\n"
"ds_read_b128 v[28:31], v0 offset:29696\n"
"buffer_load_dwordx4 v0, s[40:43], s44 offen offset:3072 lds\n"
"s_waitcnt lgkmcnt(4)\n"
"v_mfma_f32_32x32x16_bf16 v[96:111], v[8:11], a[96:99], v[96:111]\n"
"ds_read_b128 v[8:11], v0 offset:30720\n"
"v_mfma_f32_32x32x16_bf16 v[96:111], v[12:15], a[100:103], v[96:111]\n"
"ds_read_b128 v[12:15], v0 offset:31744\n"
"s_waitcnt lgkmcnt(4)\n"
"v_mfma_f32_32x32x16_bf16 v[96:111], v[16:19], a[104:107], v[96:111]\n"
"ds_read_b128 v[16:19], v0 offset:32768\n"
"s_add_u32 m0, s45, 81920\n"
"s_add_u32 s44, s45, 81920\n"
"buffer_load_dwordx4 v0, s[40:43], s44 offen offset:0 lds\n"
"v_mfma_f32_32x32x16_bf16 v[96:111], v[20:23], a[108:111], v[96:111]\n"
"ds_read_b128 v[20:23], v0 offset:33792\n"
"s_waitcnt lgkmcnt(4)\n"
"v_mfma_f32_32x32x16_bf16 v[96:111], v[24:27], a[112:115], v[96:111]\n"
"ds_read_b128 v[24:27], v0 offset:34816\n"
"v_mfma_f32_32x32x16_bf16 v[96:111], v[28:31], a[116:119], v[96:111]\n"
"ds_read_b128 v[28:31], v0 offset:35840\n"
"s_waitcnt lgkmcnt(4)\n"
"v_mfma_f32_32x32x16_bf16 v[96:111], v[8:11], a[120:123], v[96:111]\n"
"s_waitcnt vmcnt(9)\n"
"s_barrier\n"
"ds_read_b128 v[8:11], v0 offset:36864\n"
"v_mfma_f32_32x32x16_bf16 v[96:111], v[12:15], a[124:127], v[96:111]\n"
"ds_read_b128 v[12:15], v0 offset:37888\n"
"s_waitcnt lgkmcnt(4)\n"
"v_mfma_f32_32x32x16_bf16 v[96:111], v[16:19], a[128:131], v[96:111]\n"
"ds_read_b128 v[16:19], v0 offset:38912\n"
"v_mfma_f32_32x32x16_bf16 v[96:111], v[20:23], a[132:135], v[96:111]\n"
"ds_read_b128 v[20:23], v0 offset:39936\n"
"s_waitcnt lgkmcnt(4)\n"
"v_mfma_f32_32x32x16_bf16 v[96:111], v[24:27], a[136:139], v[96:111]\n"
"ds_read_b128 v[24:27], v0 offset:40960\n"
"v_mfma_f32_32x32x16_bf16 v[96:111], v[28:31], a[140:143], v[96:111]\n"
"ds_read_b128 v[28:31], v0 offset:41984\n"
"s_waitcnt lgkmcnt(4)\n"
"v_mfma_f32_32x32x16_bf16 v[112:127], v[8:11], a[0:3], 0\n"
"ds_read_b128 v[8:11], v0 offset:43008\n"
"v_mfma_f32_32x32x16_bf16 v[112:127], v[12:15], a[4:7], v[112:127]\n"
"ds_read_b128 v[12:15], v0 offset:44032\n"
"s_waitcnt lgkmcnt(4)\n"
"v_mfma_f32_32x32x16_bf16 v[112:127], v[16:19], a[8:11], v[112:127]\n"
"ds_read_b128 v[16:19], v0 offset:45056\n"
"s_add_u32 m0, s45, 0\n"
"s_add_u32 s44, s45, 110592\n"
"buffer_load_dwordx4 v0, s[40:43], s44 offen offset:0 lds\n"
"v_mfma_f32_32x32x16_bf16 v[112:127], v[20:23], a[12:15], v[112:127]\n"
"ds_read_b128 v[20:23], v0 offset:46080\n"
"s_waitcnt lgkmcnt(4)\n"
"v_mfma_f32_32x32x16_bf16 v[112:127], v[24:27], a[16:19], v[112:127]\n"
"ds_read_b128 v[24:27], v0 offset:47104\n"
"v_mfma_f32_32x32x16_bf16 v[112:127], v[28:31], a[20:23], v[112:127]\n"
"ds_read_b128 v[28:31], v0 offset:48128\n"
"buffer_load_dwordx4 v0, s[40:43], s44 offen offset:1024 lds\n"
"s_waitcnt lgkmcnt(4)\n"
"v_mfma_f32_32x32x16_bf16 v[112:127], v[8:11], a[24:27], v[112:127]\n"
"ds_read_b128 v[8:11], v0 offset:49152\n"
"v_mfma_f32_32x32x16_bf16 v[112:127], v[12:15], a[28:31], v[112:127]\n"
"ds_read_b128 v[12:15], v0 offset:50176\n"
"s_waitcnt lgkmcnt(4)\n"
"v_mfma_f32_32x32x16_bf16 v[112:127], v[16:19], a[32:35], v[112:127]\n"
"ds_read_b128 v[16:19], v0 offset:51200\n"
"buffer_load_dwordx4 v0, s[40:43], s44 offen offset:2048 lds\n"
"v_mfma_f32_32x32x16_bf16 v[112:127], v[20:23], a[36:39], v[112:127]\n"
"ds_read_b128 v[20:23], v0 offset:52224\n"
"s_waitcnt lgkmcnt(4)\n"
"v_mfma_f32_32x32x16_bf16 v[112:127], v[24:27], a[40:43], v[112:127]\n"
"ds_read_b128 v[24:27], v0 offset:53248\n"
"v_mfma_f32_32x32x16_bf16 v[112:127], v[28:31], a[44:47], v[112:127]\n"
"ds_read_b128 v[28:31], v0 offset:54272\n"
"buffer_load_dwordx4 v0, s[40:43], s44 offen offset:3072 lds\n"
"s_waitcnt lgkmcnt(4)\n"
"v_mfma_f32_32x32x16_bf16 v[112:127], v[8:11], a[48:51], v[112:127]\n"
"ds_read_b128 v[8:11], v0 offset:55296\n"
"v_mfma_f32_32x32x16_bf16 v[112:127], v[12:15], a[52:55], v[112:127]\n"
"ds_read_b128 v[12:15], v0 offset:56320\n"
"s_waitcnt lgkmcnt(4)\n"
"v_mfma_f32_32x32x16_bf16 v[112:127], v[16:19], a[56:59], v[112:127]\n"
"ds_read_b128 v[16:19], v0 offset:57344\n"
"s_add_u32 m0, s45, 4096\n"
"s_add_u32 s44, s45, 114688\n"
"buffer_load_dwordx4 v0, s[40:43], s44 offen offset:0 lds\n"
"v_mfma_f32_32x32x16_bf16 v[112:127], v[20:23], a[60:63], v[112:127]\n"
"ds_read_b128 v[20:23], v0 offset:58368\n"
"s_waitcnt lgkmcnt(4)\n"
"v_mfma_f32_32x32x16_bf16 v[112:127], v[24:27], a[64:67], v[112:127]\n"
"ds_read_b128 v[24:27], v0 offset:59392\n"
"v_mfma_f32_32x32x16_bf16 v[112:127], v[28:31], a[68:71], v[112:127]\n"
"ds_read_b128 v[28:31], v0 offset:60416\n"
"buffer_load_dwordx4 v0, s[40:43], s44 offen offset:1024 lds\n"
"s_waitcnt lgkmcnt(4)\n"
"v_mfma_f32_32x32x16_bf16 v[112:127], v[8:11], a[72:75], v[112:127]\n"
"ds_read_b128 v[8:11], v0 offset:61440\n"
"v_mfma_f32_32x32x16_bf16 v[112:127], v[12:15], a[76:79], v[112:127]\n"
"ds_read_b128 v[12:15], v0 offset:62464\n"
"s_waitcnt lgkmcnt(4)\n"
"v_mfma_f32_32x32x16_bf16 v[112:127], v[16:19], a[80:83], v[112:127]\n"
"ds_read_b128 v[16:19], v0 offset:63488\n"
"buffer_load_dwordx4 v0, s[40:43], s44 offen offset:2048 lds\n"
"v_mfma_f32_32x32x16_bf16 v[112:127], v[20:23], a[84:87], v[112:127]\n"
"ds_read_b128 v[20:23], v0 offset:64512\n"
"s_waitcnt lgkmcnt(4)\n"
"v_mfma_f32_32x32x16_bf16 v[112:127], v[24:27], a[88:91], v[112:127]\n"
"ds_read_b128 v[24:27], v1 offset:0\n"
"v_mfma_f32_32x32x16_bf16 v[112:127], v[28:31], a[92:95], v[112:127]\n"
"ds_read_b128 v[28:31], v1 offset:1024\n"
"buffer_load_dwordx4 v0, s[40:43], s44 offen offset:3072 lds\n"
"s_waitcnt lgkmcnt(4)\n"
"v_mfma_f32_32x32x16_bf16 v[112:127], v[8:11], a[96:99], v[112:127]\n"
"ds_read_b128 v[8:11], v1 offset:2048\n"
"v_mfma_f32_32x32x16_bf16 v[112:127], v[12:15], a[100:103], v[112:127]\n"
"ds_read_b128 v[12:15], v1 offset:3072\n"
"s_waitcnt lgkmcnt(4)\n"
"v_mfma_f32_32x32x16_bf16 v[112:127], v[16:19], a[104:107], v[112:127]\n"
"ds_read_b128 v[16:19], v1 offset:4096\n"
"s_add_u32 m0, s45, 8192\n"
"s_add_u32 s44, s45, 118784\n"
"buffer_load_dwordx4 v0, s[40:43], s44 offen offset:0 lds\n"
"v_mfma_f32_32x32x16_bf16 v[112:127], v[20:23], a[108:111], v[112:127]\n"
"ds_read_b128 v[20:23], v1 offset:5120\n"
"s_waitcnt lgkmcnt(4)\n"
"v_mfma_f32_32x32x16_bf16 v[112:127], v[24:27], a[112:115], v[112:127]\n"
"ds_read_b128 v[24:27], v1 offset:6144\n"
"v_mfma_f32_32x32x16_bf16 v[112:127], v[28:31], a[116:119], v[112:127]\n"
"ds_read_b128 v[28:31], v1 offset:7168\n"
"s_waitcnt lgkmcnt(4)\n"
"v_mfma_f32_32x32x16_bf16 v[112:127], v[8:11], a[120:123], v[112:127]\n"
"s_waitcnt vmcnt(9)\n"
"s_barrier\n"
"ds_read_b128 v[8:11], v1 offset:8192\n"
"v_mfma_f32_32x32x16_bf16 v[112:127], v[12:15], a[124:127], v[112:127]\n"
"ds_read_b128 v[12:15], v1 offset:9216\n"
"s_waitcnt lgkmcnt(4)\n"
"v_mfma_f32_32x32x16_bf16 v[112:127], v[16:19], a[128:131], v[112:127]\n"
"ds_read_b128 v[16:19], v1 offset:10240\n"
"v_mfma_f32_32x32x16_bf16 v[112:127], v[20:23], a[132:135], v[112:127]\n"
"ds_read_b128 v[20:23], v1 offset:11264\n"
"s_waitcnt lgkmcnt(4)\n"
"v_mfma_f32_32x32x16_bf16 v[112:127], v[24:27], a[136:139], v[112:127]\n"
"ds_read_b128 v[24:27], v1 offset:12288\n"
"v_mfma_f32_32x32x16_bf16 v[112:127], v[28:31], a[140:143], v[112:127]\n"
"ds_read_b128 v[28:31], v1 offset:13312\n"
"s_waitcnt lgkmcnt(4)\n"
"v_mfma_f32_32x32x16_bf16 v[128:143], v[8:11], a[0:3], 0\n"
"ds_read_b128 v[8:11], v1 offset:14336\n"
"v_mfma_f32_32x32x16_bf16 v[128:143], v[12:15], a[4:7], v[128:143]\n"
"ds_read_b128 v[12:15], v1 offset:15360\n"
"s_waitcnt lgkmcnt(4)\n"
"v_mfma_f32_32x32x16_bf16 v[128:143], v[16:19], a[8:11], v[128:143]\n"
"ds_read_b128 v[16:19], v1 offset:16384\n"
"s_add_u32 m0, s45, 36864\n"
"s_add_u32 s44, s45, 147456\n"
"buffer_load_dwordx4 v0, s[40:43], s44 offen offset:0 lds\n"
"v_mfma_f32_32x32x16_bf16 v[128:143], v[20:23], a[12:15], v[128:143]\n"
"ds_read_b128 v[20:23], v1 offset:17408\n"
"s_waitcnt lgkmcnt(4)\n"
"v_mfma_f32_32x32x16_bf16 v[128:143], v[24:27], a[16:19], v[128:143]\n"
"ds_read_b128 v[24:27], v1 offset:18432\n"
"v_mfma_f32_32x32x16_bf16 v[128:143], v[28:31], a[20:23], v[128:143]\n"
"ds_read_b128 v[28:31], v1 offset:19456\n"
"buffer_load_dwordx4 v0, s[40:43], s44 offen offset:1024 lds\n"
"s_waitcnt lgkmcnt(4)\n"
"v_mfma_f32_32x32x16_bf16 v[128:143], v[8:11], a[24:27], v[128:143]\n"
"ds_read_b128 v[8:11], v1 offset:20480\n"
"v_mfma_f32_32x32x16_bf16 v[128:143], v[12:15], a[28:31], v[128:143]\n"
"ds_read_b128 v[12:15], v1 offset:21504\n"
"s_waitcnt lgkmcnt(4)\n"
"v_mfma_f32_32x32x16_bf16 v[128:143], v[16:19], a[32:35], v[128:143]\n"
"ds_read_b128 v[16:19], v1 offset:22528\n"
"buffer_load_dwordx4 v0, s[40:43], s44 offen offset:2048 lds\n"
"v_mfma_f32_32x32x16_bf16 v[128:143], v[20:23], a[36:39], v[128:143]\n"
"ds_read_b128 v[20:23], v1 offset:23552\n"
"s_waitcnt lgkmcnt(4)\n"
"v_mfma_f32_32x32x16_bf16 v[128:143], v[24:27], a[40:43], v[128:143]\n"
"ds_read_b128 v[24:27], v1 offset:24576\n"
"v_mfma_f32_32x32x16_bf16 v[128:143], v[28:31], a[44:47], v[128:143]\n"
"ds_read_b128 v[28:31], v1 offset:25600\n"
"buffer_load_dwordx4 v0, s[40:43], s44 offen offset:3072 lds\n"
"s_waitcnt lgkmcnt(4)\n"
"v_mfma_f32_32x32x16_bf16 v[128:143], v[8:11], a[48:51], v[128:143]\n"
"ds_read_b128 v[8:11], v1 offset:26624\n"
"v_mfma_f32_32x32x16_bf16 v[128:143], v[12:15], a[52:55], v[128:143]\n"
"ds_read_b128 v[12:15], v1 offset:27648\n"
"s_waitcnt lgkmcnt(4)\n"
"v_mfma_f32_32x32x16_bf16 v[128:143], v[16:19], a[56:59], v[128:143]\n"
"ds_read_b128 v[16:19], v1 offset:28672\n"
"s_add_u32 m0, s45, 40960\n"
"s_add_u32 s44, s45, 151552\n"
"buffer_load_dwordx4 v0, s[40:43], s44 offen offset:0 lds\n"
"v_mfma_f32_32x32x16_bf16 v[128:143], v[20:23], a[60:63], v[128:143]\n"
"ds_read_b128 v[20:23], v1 offset:29696\n"
"s_waitcnt lgkmcnt(4)\n"
"v_mfma_f32_32x32x16_bf16 v[128:143], v[24:27], a[64:67], v[128:143]\n"
"ds_read_b128 v[24:27], v1 offset:30720\n"
"v_mfma_f32_32x32x16_bf16 v[128:143], v[28:31], a[68:71], v[128:143]\n"
"ds_read_b128 v[28:31], v1 offset:31744\n"
"buffer_load_dwordx4 v0, s[40:43], s44 offen offset:1024 lds\n"
"s_waitcnt lgkmcnt(4)\n"
"v_mfma_f32_32x32x16_bf16 v[128:143], v[8:11], a[72:75], v[128:143]\n"
"ds_read_b128 v[8:11], v1 offset:32768\n"
"v_mfma_f32_32x32x16_bf16 v[128:143], v[12:15], a[76:79], v[128:143]\n"
"ds_read_b128 v[12:15], v1 offset:33792\n"
"s_waitcnt lgkmcnt(4)\n"
"v_mfma_f32_32x32x16_bf16 v[128:143], v[16:19], a[80:83], v[128:143]\n"
"ds_read_b128 v[16:19], v1 offset:34816\n"
"buffer_load_dwordx4 v0, s[40:43], s44 offen offset:2048 lds\n"
"v_mfma_f32_32x32x16_bf16 v[128:143], v[20:23], a[84:87], v[128:143]\n"
"ds_read_b128 v[20:23], v1 offset:35840\n"
"s_waitcnt lgkmcnt(4)\n"
"v_mfma_f32_32x32x16_bf16 v[128:143], v[24:27], a[88:91], v[128:143]\n"
"ds_read_b128 v[24:27], v1 offset:36864\n"
"v_mfma_f32_32x32x16_bf16 v[128:143], v[28:31], a[92:95], v[128:143]\n"
"ds_read_b128 v[28:31], v1 offset:37888\n"
"buffer_load_dwordx4 v0, s[40:43], s44 offen offset:3072 lds\n"
"s_waitcnt lgkmcnt(4)\n"
"v_mfma_f32_32x32x16_bf16 v[128:143], v[8:11], a[96:99], v[128:143]\n"
"ds_read_b128 v[8:11], v1 offset:38912\n"
"v_mfma_f32_32x32x16_bf16 v[128:143], v[12:15], a[100:103], v[128:143]\n"
"ds_read_b128 v[12:15], v1 offset:39936\n"
"s_waitcnt lgkmcnt(4)\n"
"v_mfma_f32_32x32x16_bf16 v[128:143], v[16:19], a[104:107], v[128:143]\n"
"ds_read_b128 v[16:19], v1 offset:40960\n"
"s_add_u32 m0, s45, 45056\n"
"s_add_u32 s44, s45, 155648\n"
"buffer_load_dwordx4 v0, s[40:43], s44 offen offset:0 lds\n"
"v_mfma_f32_32x32x16_bf16 v[128:143], v[20:23], a[108:111], v[128:143]\n"
"ds_read_b128 v[20:23], v1 offset:41984\n"
"s_waitcnt lgkmcnt(4)\n"
"v_mfma_f32_32x32x16_bf16 v[128:143], v[24:27], a[112:115], v[128:143]\n"
"ds_read_b128 v[24:27], v1 offset:43008\n"
"v_mfma_f32_32x32x16_bf16 v[128:143], v[28:31], a[116:119], v[128:143]\n"
"ds_read_b128 v[28:31], v1 offset:44032\n"
"s_waitcnt lgkmcnt(4)\n"
"v_mfma_f32_32x32x16_bf16 v[128:143], v[8:11], a[120:123], v[128:143]\n"
"s_waitcnt vmcnt(9)\n"
"s_barrier\n"
"ds_read_b128 v[8:11], v0 offset:0\n"
"v_mfma_f32_32x32x16_bf16 v[128:143], v[12:15], a[124:127], v[128:143]\n"
"ds_read_b128 v[12:15], v0 offset:1024\n"
"s_waitcnt lgkmcnt(4)\n"
"v_mfma_f32_32x32x16_bf16 v[128:143], v[16:19], a[128:131], v[128:143]\n"
"ds_read_b128 v[16:19], v0 offset:2048\n"
"v_mfma_f32_32x32x16_bf16 v[128:143], v[20:23], a[132:135], v[128:143]\n"
"ds_read_b128 v[20:23], v0 offset:3072\n"
"s_waitcnt lgkmcnt(4)\n"
"v_mfma_f32_32x32x16_bf16 v[128:143], v[24:27], a[136:139], v[128:143]\n"
"ds_read_b128 v[24:27], v0 offset:4096\n"
"v_mfma_f32_32x32x16_bf16 v[128:143], v[28:31], a[140:143], v[128:143]\n"
"ds_read_b128 v[28:31], v0 offset:5120\n"
"s_waitcnt lgkmcnt(4)\n"
"v_mfma_f32_32x32x16_bf16 v[144:159], v[8:11], a[0:3], 0\n"
"ds_read_b128 v[8:11], v0 offset:6144\n"
"v_mfma_f32_32x32x16_bf16 v[144:159], v[12:15], a[4:7], v[144:159]\n"
"ds_read_b128 v[12:15], v0 offset:7168\n"
"s_waitcnt lgkmcnt(4)\n"
"v_mfma_f32_32x32x16_bf16 v[144:159], v[16:19], a[8:11], v[144:159]\n"
"ds_read_b128 v[16:19], v0 offset:8192\n"
"s_add_u32 m0, s45, 73728\n"
"s_add_u32 s44, s45, 184320\n"
"buffer_load_dwordx4 v0, s[40:43], s44 offen offset:0 lds\n"
"v_mfma_f32_32x32x16_bf16 v[144:159], v[20:23], a[12:15], v[144:159]\n"
"ds_read_b128 v[20:23], v0 offset:9216\n"
"s_waitcnt lgkmcnt(4)\n"
"v_mfma_f32_32x32x16_bf16 v[144:159], v[24:27], a[16:19], v[144:159]\n"
"ds_read_b128 v[24:27], v0 offset:10240\n"
"v_mfma_f32_32x32x16_bf16 v[144:159], v[28:31], a[20:23], v[144:159]\n"
"ds_read_b128 v[28:31], v0 offset:11264\n"
"buffer_load_dwordx4 v0, s[40:43], s44 offen offset:1024 lds\n"
"s_waitcnt lgkmcnt(4)\n"
"v_mfma_f32_32x32x16_bf16 v[144:159], v[8:11], a[24:27], v[144:159]\n"
"ds_read_b128 v[8:11], v0 offset:12288\n"
"v_mfma_f32_32x32x16_bf16 v[144:159], v[12:15], a[28:31], v[144:159]\n"
"ds_read_b128 v[12:15], v0 offset:13312\n"
"s_waitcnt lgkmcnt(4)\n"
"v_mfma_f32_32x32x16_bf16 v[144:159], v[16:19], a[32:35], v[144:159]\n"
"ds_read_b128 v[16:19], v0 offset:14336\n"
"buffer_load_dwordx4 v0, s[40:43], s44 offen offset:2048 lds\n"
"v_mfma_f32_32x32x16_bf16 v[144:159], v[20:23], a[36:39], v[144:159]\n"
"ds_read_b128 v[20:23], v0 offset:15360\n"
"s_waitcnt lgkmcnt(4)\n"
"v_mfma_f32_32x32x16_bf16 v[144:159], v[24:27], a[40:43], v[144:159]\n"
"ds_read_b128 v[24:27], v0 offset:16384\n"
"v_mfma_f32_32x32x16_bf16 v[144:159], v[28:31], a[44:47], v[144:159]\n"
"ds_read_b128 v[28:31], v0 offset:17408\n"
"buffer_load_dwordx4 v0, s[40:43], s44 offen offset:3072 lds\n"
"s_waitcnt lgkmcnt(4)\n"
"v_mfma_f32_32x32x16_bf16 v[144:159], v[8:11], a[48:51], v[144:159]\n"
"ds_read_b128 v[8:11], v0 offset:18432\n"
"v_mfma_f32_32x32x16_bf16 v[144:159], v[12:15], a[52:55], v[144:159]\n"
"ds_read_b128 v[12:15], v0 offset:19456\n"
"s_waitcnt lgkmcnt(4)\n"
"v_mfma_f32_32x32x16_bf16 v[144:159], v[16:19], a[56:59], v[144:159]\n"
"ds_read_b128 v[16:19], v0 offset:20480\n"
"s_add_u32 m0, s45, 77824\n"
"s_add_u32 s44, s45, 188416\n"
"buffer_load_dwordx4 v0, s[40:43], s44 offen offset:0 lds\n"
"v_mfma_f32_32x32x16_bf16 v[144:159], v[20:23], a[60:63], v[144:159]\n"
"ds_read_b128 v[20:23], v0 offset:21504\n"
"s_waitcnt lgkmcnt(4)\n"
"v_mfma_f32_32x32x16_bf16 v[144:159], v[24:27], a[64:67], v[144:159]\n"
"ds_read_b128 v[24:27], v0 offset:22528\n"
"v_mfma_f32_32x32x16_bf16 v[144:159], v[28:31], a[68:71], v[144:159]\n"
"ds_read_b128 v[28:31], v0 offset:23552\n"
"buffer_load_dwordx4 v0, s[40:43], s44 offen offset:1024 lds\n"
"s_waitcnt lgkmcnt(4)\n"
"v_mfma_f32_32x32x16_bf16 v[144:159], v[8:11], a[72:75], v[144:159]\n"
"ds_read_b128 v[8:11], v0 offset:24576\n"
"v_mfma_f32_32x32x16_bf16 v[144:159], v[12:15], a[76:79], v[144:159]\n"
"ds_read_b128 v[12:15], v0 offset:25600\n"
"s_waitcnt lgkmcnt(4)\n"
"v_mfma_f32_32x32x16_bf16 v[144:159], v[16:19], a[80:83], v[144:159]\n"
"ds_read_b128 v[16:19], v0 offset:26624\n"
"buffer_load_dwordx4 v0, s[40:43], s44 offen offset:2048 lds\n"
"v_mfma_f32_32x32x16_bf16 v[144:159], v[20:23], a[84:87], v[144:159]\n"
"ds_read_b128 v[20:23], v0 offset:27648\n"
"s_waitcnt lgkmcnt(4)\n"
"v_mfma_f32_32x32x16_bf16 v[144:159], v[24:27], a[88:91], v[144:159]\n"
"ds_read_b128 v[24:27], v0 offset:28672\n"
"v_mfma_f32_32x32x16_bf16 v[144:159], v[28:31], a[92:95], v[144:159]\n"
"ds_read_b128 v[28:31], v0 offset:29696\n"
"buffer_load_dwordx4 v0, s[40:43], s44 offen offset:3072 lds\n"
"s_waitcnt lgkmcnt(4)\n"
"v_mfma_f32_32x32x16_bf16 v[144:159], v[8:11], a[96:99], v[144:159]\n"
"ds_read_b128 v[8:11], v0 offset:30720\n"
"v_mfma_f32_32x32x16_bf16 v[144:159], v[12:15], a[100:103], v[144:159]\n"
"ds_read_b128 v[12:15], v0 offset:31744\n"
"s_waitcnt lgkmcnt(4)\n"
"v_mfma_f32_32x32x16_bf16 v[144:159], v[16:19], a[104:107], v[144:159]\n"
"ds_read_b128 v[16:19], v0 offset:32768\n"
"s_add_u32 m0, s45, 81920\n"
"s_add_u32 s44, s45, 192512\n"
"buffer_load_dwordx4 v0, s[40:43], s44 offen offset:0 lds\n"
"v_mfma_f32_32x32x16_bf16 v[144:159], v[20:23], a[108:111], v[144:159]\n"
"ds_read_b128 v[20:23], v0 offset:33792\n"
"s_waitcnt lgkmcnt(4)\n"
"v_mfma_f32_32x32x16_bf16 v[144:159], v[24:27], a[112:115], v[144:159]\n"
"ds_read_b128 v[24:27], v0 offset:34816\n"
"v_mfma_f32_32x32x16_bf16 v[144:159], v[28:31], a[116:119], v[144:159]\n"
"ds_read_b128 v[28:31], v0 offset:35840\n"
"s_waitcnt lgkmcnt(4)\n"
"v_mfma_f32_32x32x16_bf16 v[144:159], v[8:11], a[120:123], v[144:159]\n"
"s_waitcnt vmcnt(9)\n"
"s_barrier\n"
"ds_read_b128 v[8:11], v0 offset:36864\n"
"v_mfma_f32_32x32x16_bf16 v[144:159], v[12:15], a[124:127], v[144:159]\n"
"ds_read_b128 v[12:15], v0 offset:37888\n"
"s_waitcnt lgkmcnt(4)\n"
"v_mfma_f32_32x32x16_bf16 v[144:159], v[16:19], a[128:131], v[144:159]\n"
"ds_read_b128 v[16:19], v0 offset:38912\n"
"v_mfma_f32_32x32x16_bf16 v[144:159], v[20:23], a[132:135], v[144:159]\n"
"ds_read_b128 v[20:23], v0 offset:39936\n"
"s_waitcnt lgkmcnt(4)\n"
"v_mfma_f32_32x32x16_bf16 v[144:159], v[24:27], a[136:139], v[144:159]\n"
"ds_read_b128 v[24:27], v0 offset:40960\n"
"v_mfma_f32_32x32x16_bf16 v[144:159], v[28:31], a[140:143], v[144:159]\n"
"ds_read_b128 v[28:31], v0 offset:41984\n"
"s_waitcnt lgkmcnt(4)\n"
"v_mfma_f32_32x32x16_bf16 v[160:175], v[8:11], a[0:3], 0\n"
"ds_read_b128 v[8:11], v0 offset:43008\n"
"v_mfma_f32_32x32x16_bf16 v[160:175], v[12:15], a[4:7], v[160:175]\n"
"ds_read_b128 v[12:15], v0 offset:44032\n"
"s_waitcnt lgkmcnt(4)\n"
"v_mfma_f32_32x32x16_bf16 v[160:175], v[16:19], a[8:11], v[160:175]\n"
"ds_read_b128 v[16:19], v0 offset:45056\n"
"s_add_u32 m0, s45, 0\n"
"s_add_u32 s44, s45, 221184\n"
"buffer_load_dwordx4 v0, s[40:43], s44 offen offset:0 lds\n"
"v_mfma_f32_32x32x16_bf16 v[160:175], v[20:23], a[12:15], v[160:175]\n"
"ds_read_b128 v[20:23], v0 offset:46080\n"
"s_waitcnt lgkmcnt(4)\n"
"v_mfma_f32_32x32x16_bf16 v[160:175], v[24:27], a[16:19], v[160:175]\n"
"ds_read_b128 v[24:27], v0 offset:47104\n"
"v_mfma_f32_32x32x16_bf16 v[160:175], v[28:31], a[20:23], v[160:175]\n"
"ds_read_b128 v[28:31], v0 offset:48128\n"
"buffer_load_dwordx4 v0, s[40:43], s44 offen offset:1024 lds\n"
"s_waitcnt lgkmcnt(4)\n"
"v_mfma_f32_32x32x16_bf16 v[160:175], v[8:11], a[24:27], v[160:175]\n"
"ds_read_b128 v[8:11], v0 offset:49152\n"
"v_mfma_f32_32x32x16_bf16 v[160:175], v[12:15], a[28:31], v[160:175]\n"
"ds_read_b128 v[12:15], v0 offset:50176\n"
"s_waitcnt lgkmcnt(4)\n"
"v_mfma_f32_32x32x16_bf16 v[160:175], v[16:19], a[32:35], v[160:175]\n"
"ds_read_b128 v[16:19], v0 offset:51200\n"
"buffer_load_dwordx4 v0, s[40:43], s44 offen offset:2048 lds\n"
"v_mfma_f32_32x32x16_bf16 v[160:175], v[20:23], a[36:39], v[160:175]\n"
"ds_read_b128 v[20:23], v0 offset:52224\n"
"s_waitcnt lgkmcnt(4)\n"
"v_mfma_f32_32x32x16_bf16 v[160:175], v[24:27], a[40:43], v[160:175]\n"
"ds_read_b128 v[24:27], v0 offset:53248\n"
"v_mfma_f32_32x32x16_bf16 v[160:175], v[28:31], a[44:47], v[160:175]\n"
"ds_read_b128 v[28:31], v0 offset:54272\n"
"buffer_load_dwordx4 v0, s[40:43], s44 offen offset:3072 lds\n"
"s_waitcnt lgkmcnt(4)\n"
"v_mfma_f32_32x32x16_bf16 v[160:175], v[8:11], a[48:51], v[160:175]\n"
"ds_read_b128 v[8:11], v0 offset:55296\n"
"v_mfma_f32_32x32x16_bf16 v[160:175], v[12:15], a[52:55], v[160:175]\n"
"ds_read_b128 v[12:15], v0 offset:56320\n"
"s_waitcnt lgkmcnt(4)\n"
"v_mfma_f32_32x32x16_bf16 v[160:175], v[16:19], a[56:59], v[160:175]\n"
"ds_read_b128 v[16:19], v0 offset:57344\n"
"s_add_u32 m0, s45, 4096\n"
"s_add_u32 s44, s45, 225280\n"
"buffer_load_dwordx4 v0, s[40:43], s44 offen offset:0 lds\n"
"v_mfma_f32_32x32x16_bf16 v[160:175], v[20:23], a[60:63], v[160:175]\n"
"ds_read_b128 v[20:23], v0 offset:58368\n"
"s_waitcnt lgkmcnt(4)\n"
"v_mfma_f32_32x32x16_bf16 v[160:175], v[24:27], a[64:67], v[160:175]\n"
"ds_read_b128 v[24:27], v0 offset:59392\n"
"v_mfma_f32_32x32x16_bf16 v[160:175], v[28:31], a[68:71], v[160:175]\n"
"ds_read_b128 v[28:31], v0 offset:60416\n"
"buffer_load_dwordx4 v0, s[40:43], s44 offen offset:1024 lds\n"
"s_waitcnt lgkmcnt(4)\n"
"v_mfma_f32_32x32x16_bf16 v[160:175], v[8:11], a[72:75], v[160:175]\n"
"ds_read_b128 v[8:11], v0 offset:61440\n"
"v_mfma_f32_32x32x16_bf16 v[160:175], v[12:15], a[76:79], v[160:175]\n"
"ds_read_b128 v[12:15], v0 offset:62464\n"
"s_waitcnt lgkmcnt(4)\n"
"v_mfma_f32_32x32x16_bf16 v[160:175], v[16:19], a[80:83], v[160:175]\n"
"ds_read_b128 v[16:19], v0 offset:63488\n"
"buffer_load_dwordx4 v0, s[40:43], s44 offen offset:2048 lds\n"
"v_mfma_f32_32x32x16_bf16 v[160:175], v[20:23], a[84:87], v[160:175]\n"
"ds_read_b128 v[20:23], v0 offset:64512\n"
"s_waitcnt lgkmcnt(4)\n"
"v_mfma_f32_32x32x16_bf16 v[160:175], v[24:27], a[88:91], v[160:175]\n"
"ds_read_b128 v[24:27], v1 offset:0\n"
"v_mfma_f32_32x32x16_bf16 v[160:175], v[28:31], a[92:95], v[160:175]\n"
"ds_read_b128 v[28:31], v1 offset:1024\n"
"buffer_load_dwordx4 v0, s[40:43], s44 offen offset:3072 lds\n"
"s_waitcnt lgkmcnt(4)\n"
"v_mfma_f32_32x32x16_bf16 v[160:175], v[8:11], a[96:99], v[160:175]\n"
"ds_read_b128 v[8:11], v1 offset:2048\n"
"v_mfma_f32_32x32x16_bf16 v[160:175], v[12:15], a[100:103], v[160:175]\n"
"ds_read_b128 v[12:15], v1 offset:3072\n"
"s_waitcnt lgkmcnt(4)\n"
"v_mfma_f32_32x32x16_bf16 v[160:175], v[16:19], a[104:107], v[160:175]\n"
"ds_read_b128 v[16:19], v1 offset:4096\n"
"s_add_u32 m0, s45, 8192\n"
"s_add_u32 s44, s45, 229376\n"
"buffer_load_dwordx4 v0, s[40:43], s44 offen offset:0 lds\n"
"v_mfma_f32_32x32x16_bf16 v[160:175], v[20:23], a[108:111], v[160:175]\n"
"ds_read_b128 v[20:23], v1 offset:5120\n"
"s_waitcnt lgkmcnt(4)\n"
"v_mfma_f32_32x32x16_bf16 v[160:175], v[24:27], a[112:115], v[160:175]\n"
"ds_read_b128 v[24:27], v1 offset:6144\n"
"v_mfma_f32_32x32x16_bf16 v[160:175], v[28:31], a[116:119], v[160:175]\n"
"ds_read_b128 v[28:31], v1 offset:7168\n"
"s_waitcnt lgkmcnt(4)\n"
"v_mfma_f32_32x32x16_bf16 v[160:175], v[8:11], a[120:123], v[160:175]\n"
"s_waitcnt vmcnt(9)\n"
"s_barrier\n"
"ds_read_b128 v[8:11], v1 offset:8192\n"
"v_mfma_f32_32x32x16_bf16 v[160:175], v[12:15], a[124:127], v[160:175]\n"
"ds_read_b128 v[12:15], v1 offset:9216\n"
"s_waitcnt lgkmcnt(4)\n"
"v_mfma_f32_32x32x16_bf16 v[160:175], v[16:19], a[128:131], v[160:175]\n"
"ds_read_b128 v[16:19], v1 offset:10240\n"
"v_mfma_f32_32x32x16_bf16 v[160:175], v[20:23], a[132:135], v[160:175]\n"
"ds_read_b128 v[20:23], v1 offset:11264\n"
"s_waitcnt lgkmcnt(4)\n"
"v_mfma_f32_32x32x16_bf16 v[160:175], v[24:27], a[136:139], v[160:175]\n"
"ds_read_b128 v[24:27], v1 offset:12288\n"
"v_mfma_f32_32x32x16_bf16 v[160:175], v[28:31], a[140:143], v[160:175]\n"
"ds_read_b128 v[28:31], v1 offset:13312\n"
"s_waitcnt lgkmcnt(4)\n"
"v_mfma_f32_32x32x16_bf16 v[176:191], v[8:11], a[0:3], 0\n"
"ds_read_b128 v[8:11], v1 offset:14336\n"
"v_mfma_f32_32x32x16_bf16 v[176:191], v[12:15], a[4:7], v[176:191]\n"
"ds_read_b128 v[12:15], v1 offset:15360\n"
"s_waitcnt lgkmcnt(4)\n"
"v_mfma_f32_32x32x16_bf16 v[176:191], v[16:19], a[8:11], v[176:191]\n"
"ds_read_b128 v[16:19], v1 offset:16384\n"
"s_add_u32 m0, s45, 36864\n"
"s_add_u32 s44, s45, 258048\n"
"buffer_load_dwordx4 v0, s[40:43], s44 offen offset:0 lds\n"
"v_mfma_f32_32x32x16_bf16 v[176:191], v[20:23], a[12:15], v[176:191]\n"
"ds_read_b128 v[20:23], v1 offset:17408\n"
"s_waitcnt lgkmcnt(4)\n"
"v_mfma_f32_32x32x16_bf16 v[176:191], v[24:27], a[16:19], v[176:191]\n"
"ds_read_b128 v[24:27], v1 offset:18432\n"
"v_max3_f32 v78, v96, v97, v98\n"
"v_max3_f32 v78, v78, v99, v100\n"
"v_max3_f32 v78, v78, v101, v102\n"
"v_mfma_f32_32x32x16_bf16 v[176:191], v[28:31], a[20:23], v[176:191]\n"
"ds_read_b128 v[28:31], v1 offset:19456\n"
"buffer_load_dwordx4 v0, s[40:43], s44 offen offset:1024 lds\n"
"v_max3_f32 v78, v78, v103, v104\n"
"v_max3_f32 v78, v78, v105, v106\n"
"v_max3_f32 v78, v78, v107, v108\n"
"v_max3_f32 v78, v78, v109, v110\n"
"s_waitcnt lgkmcnt(4)\n"
"v_mfma_f32_32x32x16_bf16 v[176:191], v[8:11], a[24:27], v[176:191]\n"
"ds_read_b128 v[8:11], v1 offset:20480\n"
"v_max3_f32 v78, v78, v111, v112\n"
"v_max_f32 v78, v78, v113\n"
"v_sub_f32 v60, v96, v78\n"
"v_mfma_f32_32x32x16_bf16 v[176:191], v[12:15], a[28:31], v[176:191]\n"
"ds_read_b128 v[12:15], v1 offset:21504\n"
"v_sub_f32 v61, v97, v78\n"
"v_exp_f32 v60, v60\n"
"v_sub_f32 v62, v98, v78\n"
"s_waitcnt lgkmcnt(4)\n"
"v_mfma_f32_32x32x16_bf16 v[176:191], v[16:19], a[32:35], v[176:191]\n"
"ds_read_b128 v[16:19], v1 offset:22528\n"
"buffer_load_dwordx4 v0, s[40:43], s44 offen offset:2048 lds\n"
"v_exp_f32 v61, v61\n"
"v_sub_f32 v63, v99, v78\n"
"v_mfma_f32_32x32x16_bf16 v[176:191], v[20:23], a[36:39], v[176:191]\n"
"ds_read_b128 v[20:23], v1 offset:23552\n"
"v_exp_f32 v62, v62\n"
"v_sub_f32 v64, v100, v78\n"
"v_add_f32 v79, v60, v61\n"
"s_waitcnt lgkmcnt(4)\n"
"v_mfma_f32_32x32x16_bf16 v[176:191], v[24:27], a[40:43], v[176:191]\n"
"ds_read_b128 v[24:27], v1 offset:24576\n"
"v_exp_f32 v63, v63\n"
"v_sub_f32 v65, v101, v78\n"
"v_mfma_f32_32x32x16_bf16 v[176:191], v[28:31], a[44:47], v[176:191]\n"
"ds_read_b128 v[28:31], v1 offset:25600\n"
"buffer_load_dwordx4 v0, s[40:43], s44 offen offset:3072 lds\n"
"v_add_f32 v79, v79, v62\n"
"v_exp_f32 v64, v64\n"
"v_sub_f32 v66, v102, v78\n"
"s_waitcnt lgkmcnt(4)\n"
"v_mfma_f32_32x32x16_bf16 v[176:191], v[8:11], a[48:51], v[176:191]\n"
"ds_read_b128 v[8:11], v1 offset:26624\n"
"v_add_f32 v79, v79, v63\n"
"v_exp_f32 v65, v65\n"
"v_mfma_f32_32x32x16_bf16 v[176:191], v[12:15], a[52:55], v[176:191]\n"
"ds_read_b128 v[12:15], v1 offset:27648\n"
"v_sub_f32 v67, v103, v78\n"
"v_add_f32 v79, v79, v64\n"
"v_exp_f32 v66, v66\n"
"s_waitcnt lgkmcnt(4)\n"
"v_mfma_f32_32x32x16_bf16 v[176:191], v[16:19], a[56:59], v[176:191]\n"
"ds_read_b128 v[16:19], v1 offset:28672\n"
"s_add_u32 m0, s45, 40960\n"
"s_add_u32 s44, s45, 262144\n"
"buffer_load_dwordx4 v0, s[40:43], s44 offen offset:0 lds\n"
"v_sub_f32 v68, v104, v78\n"
"v_add_f32 v79, v79, v65\n"
"v_mfma_f32_32x32x16_bf16 v[176:191], v[20:23], a[60:63], v[176:191]\n"
"ds_read_b128 v[20:23], v1 offset:29696\n"
"v_exp_f32 v67, v67\n"
"v_sub_f32 v69, v105, v78\n"
"v_add_f32 v79, v79, v66\n"
"s_waitcnt lgkmcnt(4)\n"
"v_mfma_f32_32x32x16_bf16 v[176:191], v[24:27], a[64:67], v[176:191]\n"
"ds_read_b128 v[24:27], v1 offset:30720\n"
"v_exp_f32 v68, v68\n"
"v_sub_f32 v70, v106, v78\n"
"v_add_f32 v79, v79, v67\n"
"v_mfma_f32_32x32x16_bf16 v[176:191], v[28:31], a[68:71], v[176:191]\n"
"ds_read_b128 v[28:31], v1 offset:31744\n"
"buffer_load_dwordx4 v0, s[40:43], s44 offen offset:1024 lds\n"
"v_exp_f32 v69, v69\n"
"v_sub_f32 v71, v107, v78\n"
"s_waitcnt lgkmcnt(4)\n"
"v_mfma_f32_32x32x16_bf16 v[176:191], v[8:11], a[72:75], v[176:191]\n"
"ds_read_b128 v[8:11], v1 offset:32768\n"
"v_add_f32 v79, v79, v68\n"
"v_exp_f32 v70, v70\n"
"v_sub_f32 v72, v108, v78\n"
"v_mfma_f32_32x32x16_bf16 v[176:191], v[12:15], a[76:79], v[176:191]\n"
"ds_read_b128 v[12:15], v1 offset:33792\n"
"v_add_f32 v79, v79, v69\n"
"v_exp_f32 v71, v71\n"
"s_waitcnt lgkmcnt(4)\n"
"v_mfma_f32_32x32x16_bf16 v[176:191], v[16:19], a[80:83], v[176:191]\n"
"ds_read_b128 v[16:19], v1 offset:34816\n"
"buffer_load_dwordx4 v0, s[40:43], s44 offen offset:2048 lds\n"
"v_sub_f32 v73, v109, v78\n"
"v_add_f32 v79, v79, v70\n"
"v_exp_f32 v72, v72\n"
"v_mfma_f32_32x32x16_bf16 v[176:191], v[20:23], a[84:87], v[176:191]\n"
"ds_read_b128 v[20:23], v1 offset:35840\n"
"v_sub_f32 v74, v110, v78\n"
"v_add_f32 v79, v79, v71\n"
"s_waitcnt lgkmcnt(4)\n"
"v_mfma_f32_32x32x16_bf16 v[176:191], v[24:27], a[88:91], v[176:191]\n"
"ds_read_b128 v[24:27], v1 offset:36864\n"
"v_exp_f32 v73, v73\n"
"v_sub_f32 v75, v111, v78\n"
"v_add_f32 v79, v79, v72\n"
"v_mfma_f32_32x32x16_bf16 v[176:191], v[28:31], a[92:95], v[176:191]\n"
"ds_read_b128 v[28:31], v1 offset:37888\n"
"buffer_load_dwordx4 v0, s[40:43], s44 offen offset:3072 lds\n"
"v_exp_f32 v74, v74\n"
"v_sub_f32 v76, v112, v78\n"
"v_add_f32 v79, v79, v73\n"
"s_waitcnt lgkmcnt(4)\n"
"v_mfma_f32_32x32x16_bf16 v[176:191], v[8:11], a[96:99], v[176:191]\n"
"ds_read_b128 v[8:11], v1 offset:38912\n"
"v_exp_f32 v75, v75\n"
"v_sub_f32 v77, v113, v78\n"
"v_mfma_f32_32x32x16_bf16 v[176:191], v[12:15], a[100:103], v[176:191]\n"
"ds_read_b128 v[12:15], v1 offset:39936\n"
"v_add_f32 v79, v79, v74\n"
"v_exp_f32 v76, v76\n"
"v_add_f32 v79, v79, v75\n"
"s_waitcnt lgkmcnt(4)\n"
"v_mfma_f32_32x32x16_bf16 v[176:191], v[16:19], a[104:107], v[176:191]\n"
"ds_read_b128 v[16:19], v1 offset:40960\n"
"s_add_u32 m0, s45, 45056\n"
"s_add_u32 s44, s45, 266240\n"
"buffer_load_dwordx4 v0, s[40:43], s44 offen offset:0 lds\n"
"v_exp_f32 v77, v77\n"
"v_add_f32 v79, v79, v76\n"
"v_mfma_f32_32x32x16_bf16 v[176:191], v[20:23], a[108:111], v[176:191]\n"
"ds_read_b128 v[20:23], v1 offset:41984\n"
"v_add_f32 v79, v79, v77\n"
"v_rcp_f32 v79, v79\n"
"s_nop 0\n"
"s_waitcnt lgkmcnt(4)\n"
"v_mfma_f32_32x32x16_bf16 v[176:191], v[24:27], a[112:115], v[176:191]\n"
"ds_read_b128 v[24:27], v1 offset:43008\n"
"v_mul_f32 v60, v60, v79\n"
"v_mul_f32 v61, v61, v79\n"
"v_mul_f32 v62, v62, v79\n"
"v_mfma_f32_32x32x16_bf16 v[176:191], v[28:31], a[116:119], v[176:191]\n"
"ds_read_b128 v[28:31], v1 offset:44032\n"
"v_mul_f32 v63, v63, v79\n"
"v_cvt_pk_bf16_f32 v32, v60, v61\n"
"v_mul_f32 v64, v64, v79\n"
"s_waitcnt lgkmcnt(4)\n"
"v_mfma_f32_32x32x16_bf16 v[176:191], v[8:11], a[120:123], v[176:191]\n"
"s_waitcnt vmcnt(9)\n"
"s_barrier\n"
"ds_read_b128 v[8:11], v0 offset:0\n"
"v_mul_f32 v65, v65, v79\n"
"v_cvt_pk_bf16_f32 v33, v62, v63\n"
"v_mul_f32 v66, v66, v79\n"
"v_mul_f32 v67, v67, v79\n"
"v_mfma_f32_32x32x16_bf16 v[176:191], v[12:15], a[124:127], v[176:191]\n"
"ds_read_b128 v[12:15], v0 offset:1024\n"
"v_cvt_pk_bf16_f32 v34, v64, v65\n"
"v_mul_f32 v68, v68, v79\n"
"v_mul_f32 v69, v69, v79\n"
"s_waitcnt lgkmcnt(4)\n"
"v_mfma_f32_32x32x16_bf16 v[176:191], v[16:19], a[128:131], v[176:191]\n"
"ds_read_b128 v[16:19], v0 offset:2048\n"
"v_cvt_pk_bf16_f32 v35, v66, v67\n"
"v_mul_f32 v70, v70, v79\n"
"v_mul_f32 v71, v71, v79\n"
"v_cvt_pk_bf16_f32 v36, v68, v69\n"
"v_mfma_f32_32x32x16_bf16 v[176:191], v[20:23], a[132:135], v[176:191]\n"
"ds_read_b128 v[20:23], v0 offset:3072\n"
"v_mul_f32 v72, v72, v79\n"
"v_mul_f32 v73, v73, v79\n"
"v_cvt_pk_bf16_f32 v37, v70, v71\n"
"s_waitcnt lgkmcnt(4)\n"
"v_mfma_f32_32x32x16_bf16 v[176:191], v[24:27], a[136:139], v[176:191]\n"
"ds_read_b128 v[24:27], v0 offset:4096\n"
"v_mul_f32 v74, v74, v79\n"
"v_mul_f32 v75, v75, v79\n"
"v_cvt_pk_bf16_f32 v38, v72, v73\n"
"v_mul_f32 v76, v76, v79\n"
"v_mfma_f32_32x32x16_bf16 v[176:191], v[28:31], a[140:143], v[176:191]\n"
"ds_read_b128 v[28:31], v0 offset:5120\n"
"v_mul_f32 v77, v77, v79\n"
"v_cvt_pk_bf16_f32 v39, v74, v75\n"
"v_cvt_pk_bf16_f32 v40, v76, v77\n"
"s_waitcnt lgkmcnt(4)\n"
"v_mfma_f32_32x32x16_bf16 v[192:207], v[8:11], a[0:3], 0\n"
"ds_read_b128 v[8:11], v0 offset:6144\n"
"v_cvt_pk_bf16_f32 v50, v114, v115\n"
"v_cvt_pk_bf16_f32 v51, v116, v117\n"
"v_cvt_pk_bf16_f32 v52, v118, v119\n"
"v_cvt_pk_bf16_f32 v53, v120, v121\n"
"v_cvt_pk_bf16_f32 v54, v122, v123\n"
"v_mfma_f32_32x32x16_bf16 v[192:207], v[12:15], a[4:7], v[192:207]\n"
"ds_read_b128 v[12:15], v0 offset:7168\n"
"v_cvt_pk_bf16_f32 v55, v124, v125\n"
"v_cvt_pk_bf16_f32 v56, v126, v127\n"
"v_cvt_pk_bf16_f32 v57, v128, v129\n"
"v_cvt_pk_bf16_f32 v58, v130, v131\n"
"s_waitcnt lgkmcnt(4)\n"
"v_mfma_f32_32x32x16_bf16 v[192:207], v[16:19], a[8:11], v[192:207]\n"
"ds_read_b128 v[16:19], v0 offset:8192\n"
"s_add_u32 m0, s45, 73728\n"
"s_add_u32 s44, s45, 294912\n"
"buffer_load_dwordx4 v0, s[40:43], s44 offen offset:0 lds\n"
"ds_write_b128 v2, v[32:35] offset:0\n"
"ds_write_b128 v2, v[36:39] offset:16\n"
"ds_write_b128 v2, v[52:55] offset:80\n"
"ds_write_b128 v2, v[56:59] offset:96\n"
"v_min3_f32 v78, v96, v97, v98\n"
"v_mfma_f32_32x32x16_bf16 v[192:207], v[20:23], a[12:15], v[192:207]\n"
"ds_read_b128 v[20:23], v0 offset:9216\n"
"v_min3_f32 v78, v78, v99, v100\n"
"v_min3_f32 v78, v78, v101, v102\n"
"v_min3_f32 v78, v78, v103, v104\n"
"v_min3_f32 v78, v78, v105, v106\n"
"s_waitcnt lgkmcnt(8)\n"
"v_mfma_f32_32x32x16_bf16 v[192:207], v[24:27], a[16:19], v[192:207]\n"
"ds_read_b128 v[24:27], v0 offset:10240\n"
"v_min3_f32 v78, v78, v107, v108\n"
"v_min3_f32 v78, v78, v109, v110\n"
"v_min3_f32 v78, v78, v111, v112\n"
"v_min_f32 v78, v78, v113\n"
"v_sub_f32 v60, v78, v96\n"
"v_mfma_f32_32x32x16_bf16 v[192:207], v[28:31], a[20:23], v[192:207]\n"
"ds_read_b128 v[28:31], v0 offset:11264\n"
"buffer_load_dwordx4 v0, s[40:43], s44 offen offset:1024 lds\n"
"v_sub_f32 v61, v78, v97\n"
"v_exp_f32 v60, v60\n"
"v_sub_f32 v62, v78, v98\n"
"s_waitcnt lgkmcnt(8)\n"
"v_mfma_f32_32x32x16_bf16 v[192:207], v[8:11], a[24:27], v[192:207]\n"
"ds_read_b128 v[8:11], v0 offset:12288\n"
"v_exp_f32 v61, v61\n"
"v_sub_f32 v63, v78, v99\n"
"v_exp_f32 v62, v62\n"
"v_mfma_f32_32x32x16_bf16 v[192:207], v[12:15], a[28:31], v[192:207]\n"
"ds_read_b128 v[12:15], v0 offset:13312\n"
"v_sub_f32 v64, v78, v100\n"
"v_add_f32 v79, v60, v61\n"
"v_exp_f32 v63, v63\n"
"s_waitcnt lgkmcnt(4)\n"
"v_mfma_f32_32x32x16_bf16 v[192:207], v[16:19], a[32:35], v[192:207]\n"
"ds_read_b128 v[16:19], v0 offset:14336\n"
"buffer_load_dwordx4 v0, s[40:43], s44 offen offset:2048 lds\n"
"v_sub_f32 v65, v78, v101\n"
"v_add_f32 v79, v79, v62\n"
"v_exp_f32 v64, v64\n"
"v_sub_f32 v66, v78, v102\n"
"v_mfma_f32_32x32x16_bf16 v[192:207], v[20:23], a[36:39], v[192:207]\n"
"ds_read_b128 v[20:23], v0 offset:15360\n"
"v_add_f32 v79, v79, v63\n"
"v_exp_f32 v65, v65\n"
"v_sub_f32 v67, v78, v103\n"
"s_waitcnt lgkmcnt(4)\n"
"v_mfma_f32_32x32x16_bf16 v[192:207], v[24:27], a[40:43], v[192:207]\n"
"ds_read_b128 v[24:27], v0 offset:16384\n"
"v_add_f32 v79, v79, v64\n"
"v_exp_f32 v66, v66\n"
"v_sub_f32 v68, v78, v104\n"
"v_add_f32 v79, v79, v65\n"
"v_mfma_f32_32x32x16_bf16 v[192:207], v[28:31], a[44:47], v[192:207]\n"
"ds_read_b128 v[28:31], v0 offset:17408\n"
"buffer_load_dwordx4 v0, s[40:43], s44 offen offset:3072 lds\n"
"v_exp_f32 v67, v67\n"
"v_sub_f32 v69, v78, v105\n"
"v_add_f32 v79, v79, v66\n"
"s_waitcnt lgkmcnt(4)\n"
"v_mfma_f32_32x32x16_bf16 v[192:207], v[8:11], a[48:51], v[192:207]\n"
"ds_read_b128 v[8:11], v0 offset:18432\n"
"v_exp_f32 v68, v68\n"
"v_sub_f32 v70, v78, v106\n"
"v_add_f32 v79, v79, v67\n"
"v_mfma_f32_32x32x16_bf16 v[192:207], v[12:15], a[52:55], v[192:207]\n"
"ds_read_b128 v[12:15], v0 offset:19456\n"
"v_exp_f32 v69, v69\n"
"v_sub_f32 v71, v78, v107\n"
"v_add_f32 v79, v79, v68\n"
"v_exp_f32 v70, v70\n"
"s_waitcnt lgkmcnt(4)\n"
"v_mfma_f32_32x32x16_bf16 v[192:207], v[16:19], a[56:59], v[192:207]\n"
"ds_read_b128 v[16:19], v0 offset:20480\n"
"s_add_u32 m0, s45, 77824\n"
"s_add_u32 s44, s45, 299008\n"
"buffer_load_dwordx4 v0, s[40:43], s44 offen offset:0 lds\n"
"v_sub_f32 v72, v78, v108\n"
"v_add_f32 v79, v79, v69\n"
"v_exp_f32 v71, v71\n"
"v_mfma_f32_32x32x16_bf16 v[192:207], v[20:23], a[60:63], v[192:207]\n"
"ds_read_b128 v[20:23], v0 offset:21504\n"
"v_sub_f32 v73, v78, v109\n"
"v_add_f32 v79, v79, v70\n"
"v_exp_f32 v72, v72\n"
"s_waitcnt lgkmcnt(4)\n"
"v_mfma_f32_32x32x16_bf16 v[192:207], v[24:27], a[64:67], v[192:207]\n"
"ds_read_b128 v[24:27], v0 offset:22528\n"
"v_sub_f32 v74, v78, v110\n"
"v_add_f32 v79, v79, v71\n"
"v_exp_f32 v73, v73\n"
"v_sub_f32 v75, v78, v111\n"
"v_mfma_f32_32x32x16_bf16 v[192:207], v[28:31], a[68:71], v[192:207]\n"
"ds_read_b128 v[28:31], v0 offset:23552\n"
"buffer_load_dwordx4 v0, s[40:43], s44 offen offset:1024 lds\n"
"v_add_f32 v79, v79, v72\n"
"v_exp_f32 v74, v74\n"
"v_sub_f32 v76, v78, v112\n"
"s_waitcnt lgkmcnt(4)\n"
"v_mfma_f32_32x32x16_bf16 v[192:207], v[8:11], a[72:75], v[192:207]\n"
"ds_read_b128 v[8:11], v0 offset:24576\n"
"v_add_f32 v79, v79, v73\n"
"v_exp_f32 v75, v75\n"
"v_sub_f32 v77, v78, v113\n"
"v_add_f32 v79, v79, v74\n"
"v_mfma_f32_32x32x16_bf16 v[192:207], v[12:15], a[76:79], v[192:207]\n"
"ds_read_b128 v[12:15], v0 offset:25600\n"
"v_exp_f32 v76, v76\n"
"v_add_f32 v79, v79, v75\n"
"v_exp_f32 v77, v77\n"
"s_waitcnt lgkmcnt(4)\n"
"v_mfma_f32_32x32x16_bf16 v[192:207], v[16:19], a[80:83], v[192:207]\n"
"ds_read_b128 v[16:19], v0 offset:26624\n"
"buffer_load_dwordx4 v0, s[40:43], s44 offen offset:2048 lds\n"
"v_add_f32 v79, v79, v76\n"
"v_add_f32 v79, v79, v77\n"
"v_rcp_f32 v79, v79\n"
"v_mfma_f32_32x32x16_bf16 v[192:207], v[20:23], a[84:87], v[192:207]\n"
"ds_read_b128 v[20:23], v0 offset:27648\n"
"s_nop 0\n"
"v_mul_f32 v60, v60, v79\n"
"v_mul_f32 v61, v61, v79\n"
"v_mul_f32 v62, v62, v79\n"
"s_waitcnt lgkmcnt(4)\n"
"v_mfma_f32_32x32x16_bf16 v[192:207], v[24:27], a[88:91], v[192:207]\n"
"ds_read_b128 v[24:27], v0 offset:28672\n"
"v_mul_f32 v63, v63, v79\n"
"v_cvt_pk_bf16_f32 v41, v60, v61\n"
"v_mul_f32 v64, v64, v79\n"
"v_mul_f32 v65, v65, v79\n"
"v_cvt_pk_bf16_f32 v42, v62, v63\n"
"v_mfma_f32_32x32x16_bf16 v[192:207], v[28:31], a[92:95], v[192:207]\n"
"ds_read_b128 v[28:31], v0 offset:29696\n"
"buffer_load_dwordx4 v0, s[40:43], s44 offen offset:3072 lds\n"
"v_mul_f32 v66, v66, v79\n"
"v_mul_f32 v67, v67, v79\n"
"v_cvt_pk_bf16_f32 v43, v64, v65\n"
"v_mul_f32 v68, v68, v79\n"
"s_waitcnt lgkmcnt(4)\n"
"v_mfma_f32_32x32x16_bf16 v[192:207], v[8:11], a[96:99], v[192:207]\n"
"ds_read_b128 v[8:11], v0 offset:30720\n"
"v_mul_f32 v69, v69, v79\n"
"v_cvt_pk_bf16_f32 v44, v66, v67\n"
"v_mul_f32 v70, v70, v79\n"
"v_mul_f32 v71, v71, v79\n"
"v_cvt_pk_bf16_f32 v45, v68, v69\n"
"v_mfma_f32_32x32x16_bf16 v[192:207], v[12:15], a[100:103], v[192:207]\n"
"ds_read_b128 v[12:15], v0 offset:31744\n"
"v_mul_f32 v72, v72, v79\n"
"v_mul_f32 v73, v73, v79\n"
"v_cvt_pk_bf16_f32 v46, v70, v71\n"
"v_mul_f32 v74, v74, v79\n"
"s_waitcnt lgkmcnt(4)\n"
"v_mfma_f32_32x32x16_bf16 v[192:207], v[16:19], a[104:107], v[192:207]\n"
"ds_read_b128 v[16:19], v0 offset:32768\n"
"s_add_u32 m0, s45, 81920\n"
"s_add_u32 s44, s45, 303104\n"
"buffer_load_dwordx4 v0, s[40:43], s44 offen offset:0 lds\n"
"v_mul_f32 v75, v75, v79\n"
"v_cvt_pk_bf16_f32 v47, v72, v73\n"
"v_mul_f32 v76, v76, v79\n"
"v_mul_f32 v77, v77, v79\n"
"v_cvt_pk_bf16_f32 v48, v74, v75\n"
"v_mfma_f32_32x32x16_bf16 v[192:207], v[20:23], a[108:111], v[192:207]\n"
"ds_read_b128 v[20:23], v0 offset:33792\n"
"v_cvt_pk_bf16_f32 v49, v76, v77\n"
"ds_write_b128 v2, v[40:43] offset:32\n"
"ds_write_b128 v2, v[44:47] offset:48\n"
"ds_write_b128 v2, v[48:51] offset:64\n"
"s_waitcnt lgkmcnt(7)\n"
"v_mfma_f32_32x32x16_bf16 v[192:207], v[24:27], a[112:115], v[192:207]\n"
"ds_read_b128 v[24:27], v0 offset:34816\n"
"v_mfma_f32_32x32x16_bf16 v[192:207], v[28:31], a[116:119], v[192:207]\n"
"ds_read_b128 v[28:31], v0 offset:35840\n"
"s_waitcnt lgkmcnt(7)\n"
"v_mfma_f32_32x32x16_bf16 v[192:207], v[8:11], a[120:123], v[192:207]\n"
"s_waitcnt lgkmcnt(2)\n"
"s_waitcnt vmcnt(9)\n"
"s_barrier\n"
"ds_read_b128 v[8:11], v0 offset:36864\n"
"v_mfma_f32_32x32x16_bf16 v[192:207], v[12:15], a[124:127], v[192:207]\n"
"ds_read_b128 v[12:15], v0 offset:37888\n"
"v_mfma_f32_32x32x16_bf16 v[192:207], v[16:19], a[128:131], v[192:207]\n"
"ds_read_b128 v[16:19], v0 offset:38912\n"
"v_mfma_f32_32x32x16_bf16 v[192:207], v[20:23], a[132:135], v[192:207]\n"
"ds_read_b128 v[20:23], v0 offset:39936\n"
"s_waitcnt lgkmcnt(4)\n"
"v_mfma_f32_32x32x16_bf16 v[192:207], v[24:27], a[136:139], v[192:207]\n"
"ds_read_b128 v[24:27], v0 offset:40960\n"
"v_mfma_f32_32x32x16_bf16 v[192:207], v[28:31], a[140:143], v[192:207]\n"
"ds_read_b128 v[28:31], v0 offset:41984\n"
"s_waitcnt lgkmcnt(4)\n"
"v_mfma_f32_32x32x16_bf16 v[208:223], v[8:11], a[0:3], 0\n"
"ds_read_b128 v[8:11], v0 offset:43008\n"
"ds_read_b64_tr_b16 a[144:145], v3 offset:0\n"
"ds_read_b64_tr_b16 a[146:147], v3 offset:960\n"
"ds_read_b64_tr_b16 a[148:149], v4 offset:0\n"
"ds_read_b64_tr_b16 a[150:151], v4 offset:960\n"
"v_mfma_f32_32x32x16_bf16 v[208:223], v[12:15], a[4:7], v[208:223]\n"
"ds_read_b128 v[12:15], v0 offset:44032\n"
"ds_read_b64_tr_b16 a[152:153], v3 offset:3840\n"
"ds_read_b64_tr_b16 a[154:155], v3 offset:4800\n"
"ds_read_b64_tr_b16 a[156:157], v4 offset:3840\n"
"ds_read_b64_tr_b16 a[158:159], v4 offset:4800\n"
"s_waitcnt lgkmcnt(12)\n"
"v_mfma_f32_32x32x16_bf16 v[208:223], v[16:19], a[8:11], v[208:223]\n"
"ds_read_b128 v[16:19], v0 offset:45056\n"
"s_add_u32 m0, s45, 0\n"
"s_add_u32 s44, s45, 331776\n"
"buffer_load_dwordx4 v0, s[40:43], s44 offen offset:0 lds\n"
"ds_read_b64_tr_b16 a[160:161], v3 offset:7680\n"
"ds_read_b64_tr_b16 a[162:163], v3 offset:8640\n"
"ds_read_b64_tr_b16 a[164:165], v4 offset:7680\n"
"ds_read_b64_tr_b16 a[166:167], v4 offset:8640\n"
"v_mfma_f32_32x32x16_bf16 v[208:223], v[20:23], a[12:15], v[208:223]\n"
"ds_read_b128 v[20:23], v0 offset:46080\n"
"ds_read_b64_tr_b16 a[168:169], v3 offset:11520\n"
"ds_read_b64_tr_b16 a[170:171], v3 offset:12480\n"
"ds_read_b64_tr_b16 a[172:173], v4 offset:11520\n"
"ds_read_b64_tr_b16 a[174:175], v4 offset:12480\n"
"s_waitcnt lgkmcnt(15)\n"
"v_mfma_f32_32x32x16_bf16 v[208:223], v[24:27], a[16:19], v[208:223]\n"
"ds_read_b128 v[24:27], v0 offset:47104\n"
"v_mfma_f32_32x32x16_bf16 v[80:95], a[144:147], a[148:151], 0\n"
"ds_read_b64_tr_b16 a[176:177], v3 offset:15360\n"
"ds_read_b64_tr_b16 a[178:179], v3 offset:16320\n"
"ds_read_b64_tr_b16 a[180:181], v4 offset:15360\n"
"ds_read_b64_tr_b16 a[182:183], v4 offset:16320\n"
"v_mfma_f32_32x32x16_bf16 v[208:223], v[28:31], a[20:23], v[208:223]\n"
"ds_read_b128 v[28:31], v0 offset:48128\n"
"buffer_load_dwordx4 v0, s[40:43], s44 offen offset:1024 lds\n"
"s_waitcnt lgkmcnt(15)\n"
"v_mfma_f32_32x32x16_bf16 v[80:95], a[152:155], a[156:159], v[80:95]\n"
"ds_read_b64_tr_b16 a[184:185], v3 offset:19200\n"
"ds_read_b64_tr_b16 a[186:187], v3 offset:20160\n"
"ds_read_b64_tr_b16 a[188:189], v4 offset:19200\n"
"ds_read_b64_tr_b16 a[190:191], v4 offset:20160\n"
"v_mfma_f32_32x32x16_bf16 v[208:223], v[8:11], a[24:27], v[208:223]\n"
"ds_read_b128 v[8:11], v0 offset:49152\n"
"s_waitcnt lgkmcnt(15)\n"
"v_mfma_f32_32x32x16_bf16 v[80:95], a[160:163], a[164:167], v[80:95]\n"
"ds_read_b64_tr_b16 a[192:193], v3 offset:23040\n"
"ds_read_b64_tr_b16 a[194:195], v3 offset:24000\n"
"ds_read_b64_tr_b16 a[196:197], v4 offset:23040\n"
"ds_read_b64_tr_b16 a[198:199], v4 offset:24000\n"
"v_mfma_f32_32x32x16_bf16 v[208:223], v[12:15], a[28:31], v[208:223]\n"
"ds_read_b128 v[12:15], v0 offset:50176\n"
"s_waitcnt lgkmcnt(15)\n"
"v_mfma_f32_32x32x16_bf16 v[80:95], a[168:171], a[172:175], v[80:95]\n"
"ds_read_b64_tr_b16 a[200:201], v3 offset:26880\n"
"ds_read_b64_tr_b16 a[202:203], v3 offset:27840\n"
"ds_read_b64_tr_b16 a[204:205], v4 offset:26880\n"
"ds_read_b64_tr_b16 a[206:207], v4 offset:27840\n"
"v_mfma_f32_32x32x16_bf16 v[208:223], v[16:19], a[32:35], v[208:223]\n"
"ds_read_b128 v[16:19], v0 offset:51200\n"
"buffer_load_dwordx4 v0, s[40:43], s44 offen offset:2048 lds\n"
"s_waitcnt lgkmcnt(15)\n"
"v_mfma_f32_32x32x16_bf16 v[80:95], a[176:179], a[180:183], v[80:95]\n"
"v_max3_f32 v78, v132, v133, v134\n"
"v_max3_f32 v78, v78, v135, v136\n"
"v_max3_f32 v78, v78, v137, v138\n"
"v_max3_f32 v78, v78, v139, v140\n"
"v_mfma_f32_32x32x16_bf16 v[208:223], v[20:23], a[36:39], v[208:223]\n"
"ds_read_b128 v[20:23], v0 offset:52224\n"
"s_waitcnt lgkmcnt(12)\n"
"v_mfma_f32_32x32x16_bf16 v[80:95], a[184:187], a[188:191], v[80:95]\n"
"v_max3_f32 v78, v78, v141, v142\n"
"v_max3_f32 v78, v78, v143, v144\n"
"v_max3_f32 v78, v78, v145, v146\n"
"v_max3_f32 v78, v78, v147, v148\n"
"v_mfma_f32_32x32x16_bf16 v[208:223], v[24:27], a[40:43], v[208:223]\n"
"ds_read_b128 v[24:27], v0 offset:53248\n"
"s_waitcnt lgkmcnt(8)\n"
"v_mfma_f32_32x32x16_bf16 v[80:95], a[192:195], a[196:199], v[80:95]\n"
"v_max_f32 v78, v78, v149\n"
"v_sub_f32 v60, v132, v78\n"
"v_sub_f32 v61, v133, v78\n"
"v_mfma_f32_32x32x16_bf16 v[208:223], v[28:31], a[44:47], v[208:223]\n"
"ds_read_b128 v[28:31], v0 offset:54272\n"
"buffer_load_dwordx4 v0, s[40:43], s44 offen offset:3072 lds\n"
"s_waitcnt lgkmcnt(4)\n"
"v_mfma_f32_32x32x16_bf16 v[80:95], a[200:203], a[204:207], v[80:95]\n"
"v_exp_f32 v60, v60\n"
"v_sub_f32 v62, v134, v78\n"
"v_exp_f32 v61, v61\n"
"v_mfma_f32_32x32x16_bf16 v[208:223], v[8:11], a[48:51], v[208:223]\n"
"ds_read_b128 v[8:11], v0 offset:55296\n"
"v_sub_f32 v63, v135, v78\n"
"v_exp_f32 v62, v62\n"
"v_sub_f32 v64, v136, v78\n"
"v_mfma_f32_32x32x16_bf16 v[208:223], v[12:15], a[52:55], v[208:223]\n"
"ds_read_b128 v[12:15], v0 offset:56320\n"
"v_add_f32 v79, v60, v61\n"
"v_exp_f32 v63, v63\n"
"v_sub_f32 v65, v137, v78\n"
"s_waitcnt lgkmcnt(4)\n"
"v_mfma_f32_32x32x16_bf16 v[208:223], v[16:19], a[56:59], v[208:223]\n"
"ds_read_b128 v[16:19], v0 offset:57344\n"
"s_add_u32 m0, s45, 4096\n"
"s_add_u32 s44, s45, 335872\n"
"buffer_load_dwordx4 v0, s[40:43], s44 offen offset:0 lds\n"
"v_add_f32 v79, v79, v62\n"
"v_exp_f32 v64, v64\n"
"v_sub_f32 v66, v138, v78\n"
"v_mfma_f32_32x32x16_bf16 v[208:223], v[20:23], a[60:63], v[208:223]\n"
"ds_read_b128 v[20:23], v0 offset:58368\n"
"v_add_f32 v79, v79, v63\n"
"v_exp_f32 v65, v65\n"
"s_waitcnt lgkmcnt(4)\n"
"v_mfma_f32_32x32x16_bf16 v[208:223], v[24:27], a[64:67], v[208:223]\n"
"ds_read_b128 v[24:27], v0 offset:59392\n"
"v_sub_f32 v67, v139, v78\n"
"v_add_f32 v79, v79, v64\n"
"v_exp_f32 v66, v66\n"
"v_mfma_f32_32x32x16_bf16 v[208:223], v[28:31], a[68:71], v[208:223]\n"
"ds_read_b128 v[28:31], v0 offset:60416\n"
"buffer_load_dwordx4 v0, s[40:43], s44 offen offset:1024 lds\n"
"v_sub_f32 v68, v140, v78\n"
"v_add_f32 v79, v79, v65\n"
"v_exp_f32 v67, v67\n"
"s_waitcnt lgkmcnt(4)\n"
"v_mfma_f32_32x32x16_bf16 v[208:223], v[8:11], a[72:75], v[208:223]\n"
"ds_read_b128 v[8:11], v0 offset:61440\n"
"s_mov_b64 exec, s[48:49]\n"
"global_atomic_add_f32 v5, v80, s[46:47] offset:0\n"
"global_atomic_add_f32 v5, v81, s[46:47] offset:160\n"
"global_atomic_add_f32 v5, v82, s[46:47] offset:320\n"
"global_atomic_add_f32 v5, v83, s[46:47] offset:480\n"
"s_mov_b64 exec, -1\n"
"v_sub_f32 v69, v141, v78\n"
"v_add_f32 v79, v79, v66\n"
"v_exp_f32 v68, v68\n"
"v_mfma_f32_32x32x16_bf16 v[208:223], v[12:15], a[76:79], v[208:223]\n"
"ds_read_b128 v[12:15], v0 offset:62464\n"
"v_sub_f32 v70, v142, v78\n"
"v_add_f32 v79, v79, v67\n"
"v_exp_f32 v69, v69\n"
"s_waitcnt lgkmcnt(4)\n"
"v_mfma_f32_32x32x16_bf16 v[208:223], v[16:19], a[80:83], v[208:223]\n"
"ds_read_b128 v[16:19], v0 offset:63488\n"
"buffer_load_dwordx4 v0, s[40:43], s44 offen offset:2048 lds\n"
"v_sub_f32 v71, v143, v78\n"
"v_add_f32 v79, v79, v68\n"
"v_exp_f32 v70, v70\n"
"v_mfma_f32_32x32x16_bf16 v[208:223], v[20:23], a[84:87], v[208:223]\n"
"ds_read_b128 v[20:23], v0 offset:64512\n"
"s_mov_b64 exec, s[48:49]\n"
"global_atomic_add_f32 v5, v84, s[46:47] offset:1280\n"
"global_atomic_add_f32 v5, v85, s[46:47] offset:1440\n"
"global_atomic_add_f32 v5, v86, s[46:47] offset:1600\n"
"global_atomic_add_f32 v5, v87, s[46:47] offset:1760\n"
"s_mov_b64 exec, -1\n"
"v_sub_f32 v72, v144, v78\n"
"v_add_f32 v79, v79, v69\n"
"v_exp_f32 v71, v71\n"
"s_waitcnt lgkmcnt(4)\n"
"v_mfma_f32_32x32x16_bf16 v[208:223], v[24:27], a[88:91], v[208:223]\n"
"ds_read_b128 v[24:27], v1 offset:0\n"
"v_sub_f32 v73, v145, v78\n"
"v_add_f32 v79, v79, v70\n"
"v_exp_f32 v72, v72\n"
"v_mfma_f32_32x32x16_bf16 v[208:223], v[28:31], a[92:95], v[208:223]\n"
"ds_read_b128 v[28:31], v1 offset:1024\n"
"buffer_load_dwordx4 v0, s[40:43], s44 offen offset:3072 lds\n"
"v_sub_f32 v74, v146, v78\n"
"v_add_f32 v79, v79, v71\n"
"v_exp_f32 v73, v73\n"
"s_waitcnt lgkmcnt(4)\n"
"v_mfma_f32_32x32x16_bf16 v[208:223], v[8:11], a[96:99], v[208:223]\n"
"ds_read_b128 v[8:11], v1 offset:2048\n"
"s_mov_b64 exec, s[48:49]\n"
"s_mov_b32 exec_hi, 0\n"
"global_atomic_add_f32 v5, v88, s[46:47] offset:2560\n"
"global_atomic_add_f32 v5, v89, s[46:47] offset:2720\n"
"global_atomic_add_f32 v5, v90, s[46:47] offset:2880\n"
"s_mov_b64 exec, -1\n"
"s_add_u32 s46, s46, 3040\n"
"s_addc_u32 s47, s47, 0\n"
"v_sub_f32 v75, v147, v78\n"
"v_add_f32 v79, v79, v72\n"
"v_exp_f32 v74, v74\n"
"v_mfma_f32_32x32x16_bf16 v[208:223], v[12:15], a[100:103], v[208:223]\n"
"ds_read_b128 v[12:15], v1 offset:3072\n"
"v_sub_f32 v76, v148, v78\n"
"v_add_f32 v79, v79, v73\n"
"v_exp_f32 v75, v75\n"
"s_waitcnt lgkmcnt(4)\n"
"v_mfma_f32_32x32x16_bf16 v[208:223], v[16:19], a[104:107], v[208:223]\n"
"ds_read_b128 v[16:19], v1 offset:4096\n"
"s_add_u32 m0, s45, 8192\n"
"s_add_u32 s44, s45, 339968\n"
"buffer_load_dwordx4 v0, s[40:43], s44 offen offset:0 lds\n"
"v_sub_f32 v77, v149, v78\n"
"v_add_f32 v79, v79, v74\n"
"v_exp_f32 v76, v76\n"
"v_mfma_f32_32x32x16_bf16 v[208:223], v[20:23], a[108:111], v[208:223]\n"
"ds_read_b128 v[20:23], v1 offset:5120\n"
"v_add_f32 v79, v79, v75\n"
"v_exp_f32 v77, v77\n"
"v_add_f32 v79, v79, v76\n"
"s_waitcnt lgkmcnt(4)\n"
"v_mfma_f32_32x32x16_bf16 v[208:223], v[24:27], a[112:115], v[208:223]\n"
"ds_read_b128 v[24:27], v1 offset:6144\n"
"v_add_f32 v79, v79, v77\n"
"v_rcp_f32 v79, v79\n"
"s_nop 0\n"
"v_mfma_f32_32x32x16_bf16 v[208:223], v[28:31], a[116:119], v[208:223]\n"
"ds_read_b128 v[28:31], v1 offset:7168\n"
"v_mul_f32 v60, v60, v79\n"
"v_mul_f32 v61, v61, v79\n"
"v_mul_f32 v62, v62, v79\n"
"s_waitcnt lgkmcnt(4)\n"
"v_mfma_f32_32x32x16_bf16 v[208:223], v[8:11], a[120:123], v[208:223]\n"
"s_waitcnt vmcnt(20)\n"
"s_barrier\n"
"ds_read_b128 v[8:11], v1 offset:8192\n"
"v_mul_f32 v63, v63, v79\n"
"v_cvt_pk_bf16_f32 v32, v60, v61\n"
"v_mul_f32 v64, v64, v79\n"
"v_mul_f32 v65, v65, v79\n"
"v_mfma_f32_32x32x16_bf16 v[208:223], v[12:15], a[124:127], v[208:223]\n"
"ds_read_b128 v[12:15], v1 offset:9216\n"
"v_cvt_pk_bf16_f32 v33, v62, v63\n"
"v_mul_f32 v66, v66, v79\n"
"v_mul_f32 v67, v67, v79\n"
"v_cvt_pk_bf16_f32 v34, v64, v65\n"
"s_waitcnt lgkmcnt(4)\n"
"v_mfma_f32_32x32x16_bf16 v[208:223], v[16:19], a[128:131], v[208:223]\n"
"ds_read_b128 v[16:19], v1 offset:10240\n"
"v_mul_f32 v68, v68, v79\n"
"v_mul_f32 v69, v69, v79\n"
"v_cvt_pk_bf16_f32 v35, v66, v67\n"
"v_mul_f32 v70, v70, v79\n"
"v_mfma_f32_32x32x16_bf16 v[208:223], v[20:23], a[132:135], v[208:223]\n"
"ds_read_b128 v[20:23], v1 offset:11264\n"
"v_mul_f32 v71, v71, v79\n"
"v_cvt_pk_bf16_f32 v36, v68, v69\n"
"v_mul_f32 v72, v72, v79\n"
"v_mul_f32 v73, v73, v79\n"
"s_waitcnt lgkmcnt(4)\n"
"v_mfma_f32_32x32x16_bf16 v[208:223], v[24:27], a[136:139], v[208:223]\n"
"ds_read_b128 v[24:27], v1 offset:12288\n"
"v_cvt_pk_bf16_f32 v37, v70, v71\n"
"v_mul_f32 v74, v74, v79\n"
"v_mul_f32 v75, v75, v79\n"
"v_cvt_pk_bf16_f32 v38, v72, v73\n"
"v_mfma_f32_32x32x16_bf16 v[208:223], v[28:31], a[140:143], v[208:223]\n"
"ds_read_b128 v[28:31], v1 offset:13312\n"
"v_mul_f32 v76, v76, v79\n"
"v_mul_f32 v77, v77, v79\n"
"v_cvt_pk_bf16_f32 v39, v74, v75\n"
"v_cvt_pk_bf16_f32 v40, v76, v77\n"
"s_waitcnt lgkmcnt(4)\n"
"v_mfma_f32_32x32x16_bf16 v[224:239], v[8:11], a[0:3], 0\n"
"ds_read_b128 v[8:11], v1 offset:14336\n"
"v_cvt_pk_bf16_f32 v50, v150, v151\n"
"v_cvt_pk_bf16_f32 v51, v152, v153\n"
"v_cvt_pk_bf16_f32 v52, v154, v155\n"
"v_cvt_pk_bf16_f32 v53, v156, v157\n"
"v_cvt_pk_bf16_f32 v54, v158, v159\n"
"v_mfma_f32_32x32x16_bf16 v[224:239], v[12:15], a[4:7], v[224:239]\n"
"ds_read_b128 v[12:15], v1 offset:15360\n"
"v_cvt_pk_bf16_f32 v55, v160, v161\n"
"v_cvt_pk_bf16_f32 v56, v162, v163\n"
"v_cvt_pk_bf16_f32 v57, v164, v165\n"
"v_cvt_pk_bf16_f32 v58, v166, v167\n"
"s_waitcnt lgkmcnt(4)\n"
"v_mfma_f32_32x32x16_bf16 v[224:239], v[16:19], a[8:11], v[224:239]\n"
"ds_read_b128 v[16:19], v1 offset:16384\n"
"s_add_u32 m0, s45, 36864\n"
"s_add_u32 s44, s45, 368640\n"
"buffer_load_dwordx4 v0, s[40:43], s44 offen offset:0 lds\n"
"ds_write_b128 v2, v[32:35] offset:0\n"
"ds_write_b128 v2, v[36:39] offset:16\n"
"ds_write_b128 v2, v[52:55] offset:80\n"
"ds_write_b128 v2, v[56:59] offset:96\n"
"v_min3_f32 v78, v132, v133, v134\n"
"v_mfma_f32_32x32x16_bf16 v[224:239], v[20:23], a[12:15], v[224:239]\n"
"ds_read_b128 v[20:23], v1 offset:17408\n"
"v_min3_f32 v78, v78, v135, v136\n"
"v_min3_f32 v78, v78, v137, v138\n"
"v_min3_f32 v78, v78, v139, v140\n"
"v_min3_f32 v78, v78, v141, v142\n"
"s_waitcnt lgkmcnt(8)\n"
"v_mfma_f32_32x32x16_bf16 v[224:239], v[24:27], a[16:19], v[224:239]\n"
"ds_read_b128 v[24:27], v1 offset:18432\n"
"v_min3_f32 v78, v78, v143, v144\n"
"v_min3_f32 v78, v78, v145, v146\n"
"v_min3_f32 v78, v78, v147, v148\n"
"v_min_f32 v78, v78, v149\n"
"v_sub_f32 v60, v78, v132\n"
"v_mfma_f32_32x32x16_bf16 v[224:239], v[28:31], a[20:23], v[224:239]\n"
"ds_read_b128 v[28:31], v1 offset:19456\n"
"buffer_load_dwordx4 v0, s[40:43], s44 offen offset:1024 lds\n"
"v_sub_f32 v61, v78, v133\n"
"v_exp_f32 v60, v60\n"
"v_sub_f32 v62, v78, v134\n"
"s_waitcnt lgkmcnt(8)\n"
"v_mfma_f32_32x32x16_bf16 v[224:239], v[8:11], a[24:27], v[224:239]\n"
"ds_read_b128 v[8:11], v1 offset:20480\n"
"v_exp_f32 v61, v61\n"
"v_sub_f32 v63, v78, v135\n"
"v_exp_f32 v62, v62\n"
"v_mfma_f32_32x32x16_bf16 v[224:239], v[12:15], a[28:31], v[224:239]\n"
"ds_read_b128 v[12:15], v1 offset:21504\n"
"v_sub_f32 v64, v78, v136\n"
"v_add_f32 v79, v60, v61\n"
"v_exp_f32 v63, v63\n"
"s_waitcnt lgkmcnt(4)\n"
"v_mfma_f32_32x32x16_bf16 v[224:239], v[16:19], a[32:35], v[224:239]\n"
"ds_read_b128 v[16:19], v1 offset:22528\n"
"buffer_load_dwordx4 v0, s[40:43], s44 offen offset:2048 lds\n"
"v_sub_f32 v65, v78, v137\n"
"v_add_f32 v79, v79, v62\n"
"v_exp_f32 v64, v64\n"
"v_sub_f32 v66, v78, v138\n"
"v_mfma_f32_32x32x16_bf16 v[224:239], v[20:23], a[36:39], v[224:239]\n"
"ds_read_b128 v[20:23], v1 offset:23552\n"
"v_add_f32 v79, v79, v63\n"
"v_exp_f32 v65, v65\n"
"v_sub_f32 v67, v78, v139\n"
"s_waitcnt lgkmcnt(4)\n"
"v_mfma_f32_32x32x16_bf16 v[224:239], v[24:27], a[40:43], v[224:239]\n"
"ds_read_b128 v[24:27], v1 offset:24576\n"
"v_add_f32 v79, v79, v64\n"
"v_exp_f32 v66, v66\n"
"v_sub_f32 v68, v78, v140\n"
"v_add_f32 v79, v79, v65\n"
"v_mfma_f32_32x32x16_bf16 v[224:239], v[28:31], a[44:47], v[224:239]\n"
"ds_read_b128 v[28:31], v1 offset:25600\n"
"buffer_load_dwordx4 v0, s[40:43], s44 offen offset:3072 lds\n"
"v_exp_f32 v67, v67\n"
"v_sub_f32 v69, v78, v141\n"
"v_add_f32 v79, v79, v66\n"
"s_waitcnt lgkmcnt(4)\n"
"v_mfma_f32_32x32x16_bf16 v[224:239], v[8:11], a[48:51], v[224:239]\n"
"ds_read_b128 v[8:11], v1 offset:26624\n"
"v_exp_f32 v68, v68\n"
"v_sub_f32 v70, v78, v142\n"
"v_add_f32 v79, v79, v67\n"
"v_mfma_f32_32x32x16_bf16 v[224:239], v[12:15], a[52:55], v[224:239]\n"
"ds_read_b128 v[12:15], v1 offset:27648\n"
"v_exp_f32 v69, v69\n"
"v_sub_f32 v71, v78, v143\n"
"v_add_f32 v79, v79, v68\n"
"v_exp_f32 v70, v70\n"
"s_waitcnt lgkmcnt(4)\n"
"v_mfma_f32_32x32x16_bf16 v[224:239], v[16:19], a[56:59], v[224:239]\n"
"ds_read_b128 v[16:19], v1 offset:28672\n"
"s_add_u32 m0, s45, 40960\n"
"s_add_u32 s44, s45, 372736\n"
"buffer_load_dwordx4 v0, s[40:43], s44 offen offset:0 lds\n"
"v_sub_f32 v72, v78, v144\n"
"v_add_f32 v79, v79, v69\n"
"v_exp_f32 v71, v71\n"
"v_mfma_f32_32x32x16_bf16 v[224:239], v[20:23], a[60:63], v[224:239]\n"
"ds_read_b128 v[20:23], v1 offset:29696\n"
"v_sub_f32 v73, v78, v145\n"
"v_add_f32 v79, v79, v70\n"
"v_exp_f32 v72, v72\n"
"s_waitcnt lgkmcnt(4)\n"
"v_mfma_f32_32x32x16_bf16 v[224:239], v[24:27], a[64:67], v[224:239]\n"
"ds_read_b128 v[24:27], v1 offset:30720\n"
"v_sub_f32 v74, v78, v146\n"
"v_add_f32 v79, v79, v71\n"
"v_exp_f32 v73, v73\n"
"v_sub_f32 v75, v78, v147\n"
"v_mfma_f32_32x32x16_bf16 v[224:239], v[28:31], a[68:71], v[224:239]\n"
"ds_read_b128 v[28:31], v1 offset:31744\n"
"buffer_load_dwordx4 v0, s[40:43], s44 offen offset:1024 lds\n"
"v_add_f32 v79, v79, v72\n"
"v_exp_f32 v74, v74\n"
"v_sub_f32 v76, v78, v148\n"
"s_waitcnt lgkmcnt(4)\n"
"v_mfma_f32_32x32x16_bf16 v[224:239], v[8:11], a[72:75], v[224:239]\n"
"ds_read_b128 v[8:11], v1 offset:32768\n"
"v_add_f32 v79, v79, v73\n"
"v_exp_f32 v75, v75\n"
"v_sub_f32 v77, v78, v149\n"
"v_add_f32 v79, v79, v74\n"
"v_mfma_f32_32x32x16_bf16 v[224:239], v[12:15], a[76:79], v[224:239]\n"
"ds_read_b128 v[12:15], v1 offset:33792\n"
"v_exp_f32 v76, v76\n"
"v_add_f32 v79, v79, v75\n"
"v_exp_f32 v77, v77\n"
"s_waitcnt lgkmcnt(4)\n"
"v_mfma_f32_32x32x16_bf16 v[224:239], v[16:19], a[80:83], v[224:239]\n"
"ds_read_b128 v[16:19], v1 offset:34816\n"
"buffer_load_dwordx4 v0, s[40:43], s44 offen offset:2048 lds\n"
"v_add_f32 v79, v79, v76\n"
"v_add_f32 v79, v79, v77\n"
"v_rcp_f32 v79, v79\n"
"v_mfma_f32_32x32x16_bf16 v[224:239], v[20:23], a[84:87], v[224:239]\n"
"ds_read_b128 v[20:23], v1 offset:35840\n"
"s_nop 0\n"
"v_mul_f32 v60, v60, v79\n"
"v_mul_f32 v61, v61, v79\n"
"v_mul_f32 v62, v62, v79\n"
"s_waitcnt lgkmcnt(4)\n"
"v_mfma_f32_32x32x16_bf16 v[224:239], v[24:27], a[88:91], v[224:239]\n"
"ds_read_b128 v[24:27], v1 offset:36864\n"
"v_mul_f32 v63, v63, v79\n"
"v_cvt_pk_bf16_f32 v41, v60, v61\n"
"v_mul_f32 v64, v64, v79\n"
"v_mul_f32 v65, v65, v79\n"
"v_cvt_pk_bf16_f32 v42, v62, v63\n"
"v_mfma_f32_32x32x16_bf16 v[224:239], v[28:31], a[92:95], v[224:239]\n"
"ds_read_b128 v[28:31], v1 offset:37888\n"
"buffer_load_dwordx4 v0, s[40:43], s44 offen offset:3072 lds\n"
"v_mul_f32 v66, v66, v79\n"
"v_mul_f32 v67, v67, v79\n"
"v_cvt_pk_bf16_f32 v43, v64, v65\n"
"v_mul_f32 v68, v68, v79\n"
"s_waitcnt lgkmcnt(4)\n"
"v_mfma_f32_32x32x16_bf16 v[224:239], v[8:11], a[96:99], v[224:239]\n"
"ds_read_b128 v[8:11], v1 offset:38912\n"
"v_mul_f32 v69, v69, v79\n"
"v_cvt_pk_bf16_f32 v44, v66, v67\n"
"v_mul_f32 v70, v70, v79\n"
"v_mul_f32 v71, v71, v79\n"
"v_cvt_pk_bf16_f32 v45, v68, v69\n"
"v_mfma_f32_32x32x16_bf16 v[224:239], v[12:15], a[100:103], v[224:239]\n"
"ds_read_b128 v[12:15], v1 offset:39936\n"
"v_mul_f32 v72, v72, v79\n"
"v_mul_f32 v73, v73, v79\n"
"v_cvt_pk_bf16_f32 v46, v70, v71\n"
"v_mul_f32 v74, v74, v79\n"
"s_waitcnt lgkmcnt(4)\n"
"v_mfma_f32_32x32x16_bf16 v[224:239], v[16:19], a[104:107], v[224:239]\n"
"ds_read_b128 v[16:19], v1 offset:40960\n"
"s_add_u32 m0, s45, 45056\n"
"s_add_u32 s44, s45, 376832\n"
"buffer_load_dwordx4 v0, s[40:43], s44 offen offset:0 lds\n"
"v_mul_f32 v75, v75, v79\n"
"v_cvt_pk_bf16_f32 v47, v72, v73\n"
"v_mul_f32 v76, v76, v79\n"
"v_mul_f32 v77, v77, v79\n"
"v_cvt_pk_bf16_f32 v48, v74, v75\n"
"v_mfma_f32_32x32x16_bf16 v[224:239], v[20:23], a[108:111], v[224:239]\n"
"ds_read_b128 v[20:23], v1 offset:41984\n"
"v_cvt_pk_bf16_f32 v49, v76, v77\n"
"ds_write_b128 v2, v[40:43] offset:32\n"
"ds_write_b128 v2, v[44:47] offset:48\n"
"ds_write_b128 v2, v[48:51] offset:64\n"
"s_waitcnt lgkmcnt(7)\n"
"v_mfma_f32_32x32x16_bf16 v[224:239], v[24:27], a[112:115], v[224:239]\n"
"ds_read_b128 v[24:27], v1 offset:43008\n"
"v_mfma_f32_32x32x16_bf16 v[224:239], v[28:31], a[116:119], v[224:239]\n"
"ds_read_b128 v[28:31], v1 offset:44032\n"
"s_waitcnt lgkmcnt(7)\n"
"v_mfma_f32_32x32x16_bf16 v[224:239], v[8:11], a[120:123], v[224:239]\n"
"s_waitcnt lgkmcnt(2)\n"
"s_waitcnt vmcnt(9)\n"
"s_barrier\n"
"ds_read_b128 v[8:11], v0 offset:0\n"
"v_mfma_f32_32x32x16_bf16 v[224:239], v[12:15], a[124:127], v[224:239]\n"
"ds_read_b128 v[12:15], v0 offset:1024\n"
"v_mfma_f32_32x32x16_bf16 v[224:239], v[16:19], a[128:131], v[224:239]\n"
"ds_read_b128 v[16:19], v0 offset:2048\n"
"v_mfma_f32_32x32x16_bf16 v[224:239], v[20:23], a[132:135], v[224:239]\n"
"ds_read_b128 v[20:23], v0 offset:3072\n"
"s_waitcnt lgkmcnt(4)\n"
"v_mfma_f32_32x32x16_bf16 v[224:239], v[24:27], a[136:139], v[224:239]\n"
"ds_read_b128 v[24:27], v0 offset:4096\n"
"v_mfma_f32_32x32x16_bf16 v[224:239], v[28:31], a[140:143], v[224:239]\n"
"ds_read_b128 v[28:31], v0 offset:5120\n"
"s_waitcnt lgkmcnt(4)\n"
"v_mfma_f32_32x32x16_bf16 v[240:255], v[8:11], a[0:3], 0\n"
"ds_read_b128 v[8:11], v0 offset:6144\n"
"ds_read_b64_tr_b16 a[144:145], v3 offset:0\n"
"ds_read_b64_tr_b16 a[146:147], v3 offset:960\n"
"ds_read_b64_tr_b16 a[148:149], v4 offset:0\n"
"ds_read_b64_tr_b16 a[150:151], v4 offset:960\n"
"v_mfma_f32_32x32x16_bf16 v[240:255], v[12:15], a[4:7], v[240:255]\n"
"ds_read_b128 v[12:15], v0 offset:7168\n"
"ds_read_b64_tr_b16 a[152:153], v3 offset:3840\n"
"ds_read_b64_tr_b16 a[154:155], v3 offset:4800\n"
"ds_read_b64_tr_b16 a[156:157], v4 offset:3840\n"
"ds_read_b64_tr_b16 a[158:159], v4 offset:4800\n"
"s_waitcnt lgkmcnt(12)\n"
"v_mfma_f32_32x32x16_bf16 v[240:255], v[16:19], a[8:11], v[240:255]\n"
"ds_read_b128 v[16:19], v0 offset:8192\n"
"s_add_u32 m0, s45, 73728\n"
"s_add_u32 s44, s45, 405504\n"
"buffer_load_dwordx4 v0, s[40:43], s44 offen offset:0 lds\n"
"ds_read_b64_tr_b16 a[160:161], v3 offset:7680\n"
"ds_read_b64_tr_b16 a[162:163], v3 offset:8640\n"
"ds_read_b64_tr_b16 a[164:165], v4 offset:7680\n"
"ds_read_b64_tr_b16 a[166:167], v4 offset:8640\n"
"v_mfma_f32_32x32x16_bf16 v[240:255], v[20:23], a[12:15], v[240:255]\n"
"ds_read_b128 v[20:23], v0 offset:9216\n"
"ds_read_b64_tr_b16 a[168:169], v3 offset:11520\n"
"ds_read_b64_tr_b16 a[170:171], v3 offset:12480\n"
"ds_read_b64_tr_b16 a[172:173], v4 offset:11520\n"
"ds_read_b64_tr_b16 a[174:175], v4 offset:12480\n"
"s_waitcnt lgkmcnt(15)\n"
"v_mfma_f32_32x32x16_bf16 v[240:255], v[24:27], a[16:19], v[240:255]\n"
"ds_read_b128 v[24:27], v0 offset:10240\n"
"v_mfma_f32_32x32x16_bf16 v[80:95], a[144:147], a[148:151], 0\n"
"ds_read_b64_tr_b16 a[176:177], v3 offset:15360\n"
"ds_read_b64_tr_b16 a[178:179], v3 offset:16320\n"
"ds_read_b64_tr_b16 a[180:181], v4 offset:15360\n"
"ds_read_b64_tr_b16 a[182:183], v4 offset:16320\n"
"v_mfma_f32_32x32x16_bf16 v[240:255], v[28:31], a[20:23], v[240:255]\n"
"ds_read_b128 v[28:31], v0 offset:11264\n"
"buffer_load_dwordx4 v0, s[40:43], s44 offen offset:1024 lds\n"
"s_waitcnt lgkmcnt(15)\n"
"v_mfma_f32_32x32x16_bf16 v[80:95], a[152:155], a[156:159], v[80:95]\n"
"ds_read_b64_tr_b16 a[184:185], v3 offset:19200\n"
"ds_read_b64_tr_b16 a[186:187], v3 offset:20160\n"
"ds_read_b64_tr_b16 a[188:189], v4 offset:19200\n"
"ds_read_b64_tr_b16 a[190:191], v4 offset:20160\n"
"v_mfma_f32_32x32x16_bf16 v[240:255], v[8:11], a[24:27], v[240:255]\n"
"ds_read_b128 v[8:11], v0 offset:12288\n"
"s_waitcnt lgkmcnt(15)\n"
"v_mfma_f32_32x32x16_bf16 v[80:95], a[160:163], a[164:167], v[80:95]\n"
"ds_read_b64_tr_b16 a[192:193], v3 offset:23040\n"
"ds_read_b64_tr_b16 a[194:195], v3 offset:24000\n"
"ds_read_b64_tr_b16 a[196:197], v4 offset:23040\n"
"ds_read_b64_tr_b16 a[198:199], v4 offset:24000\n"
"v_mfma_f32_32x32x16_bf16 v[240:255], v[12:15], a[28:31], v[240:255]\n"
"ds_read_b128 v[12:15], v0 offset:13312\n"
"s_waitcnt lgkmcnt(15)\n"
"v_mfma_f32_32x32x16_bf16 v[80:95], a[168:171], a[172:175], v[80:95]\n"
"ds_read_b64_tr_b16 a[200:201], v3 offset:26880\n"
"ds_read_b64_tr_b16 a[202:203], v3 offset:27840\n"
"ds_read_b64_tr_b16 a[204:205], v4 offset:26880\n"
"ds_read_b64_tr_b16 a[206:207], v4 offset:27840\n"
"v_mfma_f32_32x32x16_bf16 v[240:255], v[16:19], a[32:35], v[240:255]\n"
"ds_read_b128 v[16:19], v0 offset:14336\n"
"buffer_load_dwordx4 v0, s[40:43], s44 offen offset:2048 lds\n"
"s_waitcnt lgkmcnt(15)\n"
"v_mfma_f32_32x32x16_bf16 v[80:95], a[176:179], a[180:183], v[80:95]\n"
"v_mfma_f32_32x32x16_bf16 v[240:255], v[20:23], a[36:39], v[240:255]\n"
"ds_read_b128 v[20:23], v0 offset:15360\n"
"s_waitcnt lgkmcnt(12)\n"
"v_mfma_f32_32x32x16_bf16 v[80:95], a[184:187], a[188:191], v[80:95]\n"
"v_mfma_f32_32x32x16_bf16 v[240:255], v[24:27], a[40:43], v[240:255]\n"
"ds_read_b128 v[24:27], v0 offset:16384\n"
"s_waitcnt lgkmcnt(8)\n"
"v_mfma_f32_32x32x16_bf16 v[80:95], a[192:195], a[196:199], v[80:95]\n"
"v_mfma_f32_32x32x16_bf16 v[240:255], v[28:31], a[44:47], v[240:255]\n"
"ds_read_b128 v[28:31], v0 offset:17408\n"
"buffer_load_dwordx4 v0, s[40:43], s44 offen offset:3072 lds\n"
"s_waitcnt lgkmcnt(4)\n"
"v_mfma_f32_32x32x16_bf16 v[80:95], a[200:203], a[204:207], v[80:95]\n"
"v_mfma_f32_32x32x16_bf16 v[240:255], v[8:11], a[48:51], v[240:255]\n"
"ds_read_b128 v[8:11], v0 offset:18432\n"
"v_mfma_f32_32x32x16_bf16 v[240:255], v[12:15], a[52:55], v[240:255]\n"
"ds_read_b128 v[12:15], v0 offset:19456\n"
"s_waitcnt lgkmcnt(4)\n"
"v_mfma_f32_32x32x16_bf16 v[240:255], v[16:19], a[56:59], v[240:255]\n"
"ds_read_b128 v[16:19], v0 offset:20480\n"
"s_add_u32 m0, s45, 77824\n"
"s_add_u32 s44, s45, 409600\n"
"buffer_load_dwordx4 v0, s[40:43], s44 offen offset:0 lds\n"
"v_mfma_f32_32x32x16_bf16 v[240:255], v[20:23], a[60:63], v[240:255]\n"
"ds_read_b128 v[20:23], v0 offset:21504\n"
"s_waitcnt lgkmcnt(4)\n"
"v_mfma_f32_32x32x16_bf16 v[240:255], v[24:27], a[64:67], v[240:255]\n"
"ds_read_b128 v[24:27], v0 offset:22528\n"
"v_mfma_f32_32x32x16_bf16 v[240:255], v[28:31], a[68:71], v[240:255]\n"
"ds_read_b128 v[28:31], v0 offset:23552\n"
"buffer_load_dwordx4 v0, s[40:43], s44 offen offset:1024 lds\n"
"s_waitcnt lgkmcnt(4)\n"
"v_mfma_f32_32x32x16_bf16 v[240:255], v[8:11], a[72:75], v[240:255]\n"
"ds_read_b128 v[8:11], v0 offset:24576\n"
"s_mov_b64 exec, s[48:49]\n"
"global_atomic_add_f32 v5, v80, s[46:47] offset:0\n"
"global_atomic_add_f32 v5, v81, s[46:47] offset:160\n"
"global_atomic_add_f32 v5, v82, s[46:47] offset:320\n"
"global_atomic_add_f32 v5, v83, s[46:47] offset:480\n"
"s_mov_b64 exec, -1\n"
"v_mfma_f32_32x32x16_bf16 v[240:255], v[12:15], a[76:79], v[240:255]\n"
"ds_read_b128 v[12:15], v0 offset:25600\n"
"s_waitcnt lgkmcnt(4)\n"
"v_mfma_f32_32x32x16_bf16 v[240:255], v[16:19], a[80:83], v[240:255]\n"
"ds_read_b128 v[16:19], v0 offset:26624\n"
"buffer_load_dwordx4 v0, s[40:43], s44 offen offset:2048 lds\n"
"v_mfma_f32_32x32x16_bf16 v[240:255], v[20:23], a[84:87], v[240:255]\n"
"ds_read_b128 v[20:23], v0 offset:27648\n"
"s_mov_b64 exec, s[48:49]\n"
"global_atomic_add_f32 v5, v84, s[46:47] offset:1280\n"
"global_atomic_add_f32 v5, v85, s[46:47] offset:1440\n"
"global_atomic_add_f32 v5, v86, s[46:47] offset:1600\n"
"global_atomic_add_f32 v5, v87, s[46:47] offset:1760\n"
"s_mov_b64 exec, -1\n"
"s_waitcnt lgkmcnt(4)\n"
"v_mfma_f32_32x32x16_bf16 v[240:255], v[24:27], a[88:91], v[240:255]\n"
"ds_read_b128 v[24:27], v0 offset:28672\n"
"v_mfma_f32_32x32x16_bf16 v[240:255], v[28:31], a[92:95], v[240:255]\n"
"ds_read_b128 v[28:31], v0 offset:29696\n"
"buffer_load_dwordx4 v0, s[40:43], s44 offen offset:3072 lds\n"
"s_waitcnt lgkmcnt(4)\n"
"v_mfma_f32_32x32x16_bf16 v[240:255], v[8:11], a[96:99], v[240:255]\n"
"ds_read_b128 v[8:11], v0 offset:30720\n"
"s_mov_b64 exec, s[48:49]\n"
"s_mov_b32 exec_hi, 0\n"
"global_atomic_add_f32 v5, v88, s[46:47] offset:2560\n"
"global_atomic_add_f32 v5, v89, s[46:47] offset:2720\n"
"global_atomic_add_f32 v5, v90, s[46:47] offset:2880\n"
"s_mov_b64 exec, -1\n"
"s_add_u32 s46, s46, 3040\n"
"s_addc_u32 s47, s47, 0\n"
"v_mfma_f32_32x32x16_bf16 v[240:255], v[12:15], a[100:103], v[240:255]\n"
"ds_read_b128 v[12:15], v0 offset:31744\n"
"s_waitcnt lgkmcnt(4)\n"
"v_mfma_f32_32x32x16_bf16 v[240:255], v[16:19], a[104:107], v[240:255]\n"
"ds_read_b128 v[16:19], v0 offset:32768\n"
"s_add_u32 m0, s45, 81920\n"
"s_add_u32 s44, s45, 413696\n"
"buffer_load_dwordx4 v0, s[40:43], s44 offen offset:0 lds\n"
"v_mfma_f32_32x32x16_bf16 v[240:255], v[20:23], a[108:111], v[240:255]\n"
"ds_read_b128 v[20:23], v0 offset:33792\n"
"s_waitcnt lgkmcnt(4)\n"
"v_mfma_f32_32x32x16_bf16 v[240:255], v[24:27], a[112:115], v[240:255]\n"
"ds_read_b128 v[24:27], v0 offset:34816\n"
"v_mfma_f32_32x32x16_bf16 v[240:255], v[28:31], a[116:119], v[240:255]\n"
"ds_read_b128 v[28:31], v0 offset:35840\n"
"s_waitcnt lgkmcnt(4)\n"
"v_mfma_f32_32x32x16_bf16 v[240:255], v[8:11], a[120:123], v[240:255]\n"
"s_waitcnt vmcnt(20)\n"
"s_barrier\n"
"ds_read_b128 v[8:11], v0 offset:36864\n"
"v_mfma_f32_32x32x16_bf16 v[240:255], v[12:15], a[124:127], v[240:255]\n"
"ds_read_b128 v[12:15], v0 offset:37888\n"
"s_waitcnt lgkmcnt(4)\n"
"v_mfma_f32_32x32x16_bf16 v[240:255], v[16:19], a[128:131], v[240:255]\n"
"ds_read_b128 v[16:19], v0 offset:38912\n"
"v_mfma_f32_32x32x16_bf16 v[240:255], v[20:23], a[132:135], v[240:255]\n"
"ds_read_b128 v[20:23], v0 offset:39936\n"
"s_waitcnt lgkmcnt(4)\n"
"v_mfma_f32_32x32x16_bf16 v[240:255], v[24:27], a[136:139], v[240:255]\n"
"ds_read_b128 v[24:27], v0 offset:40960\n"
"v_mfma_f32_32x32x16_bf16 v[240:255], v[28:31], a[140:143], v[240:255]\n"
"ds_read_b128 v[28:31], v0 offset:41984\n"
"s_waitcnt lgkmcnt(4)\n"
"v_mfma_f32_32x32x16_bf16 v[96:111], v[8:11], a[0:3], 0\n"
"ds_read_b128 v[8:11], v0 offset:43008\n"
"v_mfma_f32_32x32x16_bf16 v[96:111], v[12:15], a[4:7], v[96:111]\n"
"ds_read_b128 v[12:15], v0 offset:44032\n"
"s_waitcnt lgkmcnt(4)\n"
"v_mfma_f32_32x32x16_bf16 v[96:111], v[16:19], a[8:11], v[96:111]\n"
"ds_read_b128 v[16:19], v0 offset:45056\n"
"s_add_u32 m0, s45, 0\n"
"s_add_u32 s44, s45, 442368\n"
"buffer_load_dwordx4 v0, s[40:43], s44 offen offset:0 lds\n"
"v_mfma_f32_32x32x16_bf16 v[96:111], v[20:23], a[12:15], v[96:111]\n"
"ds_read_b128 v[20:23], v0 offset:46080\n"
"s_waitcnt lgkmcnt(4)\n"
"v_mfma_f32_32x32x16_bf16 v[96:111], v[24:27], a[16:19], v[96:111]\n"
"ds_read_b128 v[24:27], v0 offset:47104\n"
"v_max3_f32 v78, v176, v177, v178\n"
"v_max3_f32 v78, v78, v179, v180\n"
"v_max3_f32 v78, v78, v181, v182\n"
"v_mfma_f32_32x32x16_bf16 v[96:111], v[28:31], a[20:23], v[96:111]\n"
"ds_read_b128 v[28:31], v0 offset:48128\n"
"buffer_load_dwordx4 v0, s[40:43], s44 offen offset:1024 lds\n"
"v_max3_f32 v78, v78, v183, v184\n"
"v_max3_f32 v78, v78, v185, v186\n"
"v_max3_f32 v78, v78, v187, v188\n"
"v_max3_f32 v78, v78, v189, v190\n"
"s_waitcnt lgkmcnt(4)\n"
"v_mfma_f32_32x32x16_bf16 v[96:111], v[8:11], a[24:27], v[96:111]\n"
"ds_read_b128 v[8:11], v0 offset:49152\n"
"v_max3_f32 v78, v78, v191, v192\n"
"v_max_f32 v78, v78, v193\n"
"v_sub_f32 v60, v176, v78\n"
"v_mfma_f32_32x32x16_bf16 v[96:111], v[12:15], a[28:31], v[96:111]\n"
"ds_read_b128 v[12:15], v0 offset:50176\n"
"v_sub_f32 v61, v177, v78\n"
"v_exp_f32 v60, v60\n"
"v_sub_f32 v62, v178, v78\n"
"s_waitcnt lgkmcnt(4)\n"
"v_mfma_f32_32x32x16_bf16 v[96:111], v[16:19], a[32:35], v[96:111]\n"
"ds_read_b128 v[16:19], v0 offset:51200\n"
"buffer_load_dwordx4 v0, s[40:43], s44 offen offset:2048 lds\n"
"v_exp_f32 v61, v61\n"
"v_sub_f32 v63, v179, v78\n"
"v_mfma_f32_32x32x16_bf16 v[96:111], v[20:23], a[36:39], v[96:111]\n"
"ds_read_b128 v[20:23], v0 offset:52224\n"
"v_exp_f32 v62, v62\n"
"v_sub_f32 v64, v180, v78\n"
"v_add_f32 v79, v60, v61\n"
"s_waitcnt lgkmcnt(4)\n"
"v_mfma_f32_32x32x16_bf16 v[96:111], v[24:27], a[40:43], v[96:111]\n"
"ds_read_b128 v[24:27], v0 offset:53248\n"
"v_exp_f32 v63, v63\n"
"v_sub_f32 v65, v181, v78\n"
"v_mfma_f32_32x32x16_bf16 v[96:111], v[28:31], a[44:47], v[96:111]\n"
"ds_read_b128 v[28:31], v0 offset:54272\n"
"buffer_load_dwordx4 v0, s[40:43], s44 offen offset:3072 lds\n"
"v_add_f32 v79, v79, v62\n"
"v_exp_f32 v64, v64\n"
"v_sub_f32 v66, v182, v78\n"
"s_waitcnt lgkmcnt(4)\n"
"v_mfma_f32_32x32x16_bf16 v[96:111], v[8:11], a[48:51], v[96:111]\n"
"ds_read_b128 v[8:11], v0 offset:55296\n"
"v_add_f32 v79, v79, v63\n"
"v_exp_f32 v65, v65\n"
"v_mfma_f32_32x32x16_bf16 v[96:111], v[12:15], a[52:55], v[96:111]\n"
"ds_read_b128 v[12:15], v0 offset:56320\n"
"v_sub_f32 v67, v183, v78\n"
"v_add_f32 v79, v79, v64\n"
"v_exp_f32 v66, v66\n"
"s_waitcnt lgkmcnt(4)\n"
"v_mfma_f32_32x32x16_bf16 v[96:111], v[16:19], a[56:59], v[96:111]\n"
"ds_read_b128 v[16:19], v0 offset:57344\n"
"s_add_u32 m0, s45, 4096\n"
"s_add_u32 s44, s45, 446464\n"
"buffer_load_dwordx4 v0, s[40:43], s44 offen offset:0 lds\n"
"v_sub_f32 v68, v184, v78\n"
"v_add_f32 v79, v79, v65\n"
"v_mfma_f32_32x32x16_bf16 v[96:111], v[20:23], a[60:63], v[96:111]\n"
"ds_read_b128 v[20:23], v0 offset:58368\n"
"v_exp_f32 v67, v67\n"
"v_sub_f32 v69, v185, v78\n"
"v_add_f32 v79, v79, v66\n"
"s_waitcnt lgkmcnt(4)\n"
"v_mfma_f32_32x32x16_bf16 v[96:111], v[24:27], a[64:67], v[96:111]\n"
"ds_read_b128 v[24:27], v0 offset:59392\n"
"v_exp_f32 v68, v68\n"
"v_sub_f32 v70, v186, v78\n"
"v_add_f32 v79, v79, v67\n"
"v_mfma_f32_32x32x16_bf16 v[96:111], v[28:31], a[68:71], v[96:111]\n"
"ds_read_b128 v[28:31], v0 offset:60416\n"
"buffer_load_dwordx4 v0, s[40:43], s44 offen offset:1024 lds\n"
"v_exp_f32 v69, v69\n"
"v_sub_f32 v71, v187, v78\n"
"s_waitcnt lgkmcnt(4)\n"
"v_mfma_f32_32x32x16_bf16 v[96:111], v[8:11], a[72:75], v[96:111]\n"
"ds_read_b128 v[8:11], v0 offset:61440\n"
"v_add_f32 v79, v79, v68\n"
"v_exp_f32 v70, v70\n"
"v_sub_f32 v72, v188, v78\n"
"v_mfma_f32_32x32x16_bf16 v[96:111], v[12:15], a[76:79], v[96:111]\n"
"ds_read_b128 v[12:15], v0 offset:62464\n"
"v_add_f32 v79, v79, v69\n"
"v_exp_f32 v71, v71\n"
"s_waitcnt lgkmcnt(4)\n"
"v_mfma_f32_32x32x16_bf16 v[96:111], v[16:19], a[80:83], v[96:111]\n"
"ds_read_b128 v[16:19], v0 offset:63488\n"
"buffer_load_dwordx4 v0, s[40:43], s44 offen offset:2048 lds\n"
"v_sub_f32 v73, v189, v78\n"
"v_add_f32 v79, v79, v70\n"
"v_exp_f32 v72, v72\n"
"v_mfma_f32_32x32x16_bf16 v[96:111], v[20:23], a[84:87], v[96:111]\n"
"ds_read_b128 v[20:23], v0 offset:64512\n"
"v_sub_f32 v74, v190, v78\n"
"v_add_f32 v79, v79, v71\n"
"s_waitcnt lgkmcnt(4)\n"
"v_mfma_f32_32x32x16_bf16 v[96:111], v[24:27], a[88:91], v[96:111]\n"
"ds_read_b128 v[24:27], v1 offset:0\n"
"v_exp_f32 v73, v73\n"
"v_sub_f32 v75, v191, v78\n"
"v_add_f32 v79, v79, v72\n"
"v_mfma_f32_32x32x16_bf16 v[96:111], v[28:31], a[92:95], v[96:111]\n"
"ds_read_b128 v[28:31], v1 offset:1024\n"
"buffer_load_dwordx4 v0, s[40:43], s44 offen offset:3072 lds\n"
"v_exp_f32 v74, v74\n"
"v_sub_f32 v76, v192, v78\n"
"v_add_f32 v79, v79, v73\n"
"s_waitcnt lgkmcnt(4)\n"
"v_mfma_f32_32x32x16_bf16 v[96:111], v[8:11], a[96:99], v[96:111]\n"
"ds_read_b128 v[8:11], v1 offset:2048\n"
"v_exp_f32 v75, v75\n"
"v_sub_f32 v77, v193, v78\n"
"v_mfma_f32_32x32x16_bf16 v[96:111], v[12:15], a[100:103], v[96:111]\n"
"ds_read_b128 v[12:15], v1 offset:3072\n"
"v_add_f32 v79, v79, v74\n"
"v_exp_f32 v76, v76\n"
"v_add_f32 v79, v79, v75\n"
"s_waitcnt lgkmcnt(4)\n"
"v_mfma_f32_32x32x16_bf16 v[96:111], v[16:19], a[104:107], v[96:111]\n"
"ds_read_b128 v[16:19], v1 offset:4096\n"
"s_add_u32 m0, s45, 8192\n"
"s_add_u32 s44, s45, 450560\n"
"buffer_load_dwordx4 v0, s[40:43], s44 offen offset:0 lds\n"
"v_exp_f32 v77, v77\n"
"v_add_f32 v79, v79, v76\n"
"v_mfma_f32_32x32x16_bf16 v[96:111], v[20:23], a[108:111], v[96:111]\n"
"ds_read_b128 v[20:23], v1 offset:5120\n"
"v_add_f32 v79, v79, v77\n"
"v_rcp_f32 v79, v79\n"
"s_nop 0\n"
"s_waitcnt lgkmcnt(4)\n"
"v_mfma_f32_32x32x16_bf16 v[96:111], v[24:27], a[112:115], v[96:111]\n"
"ds_read_b128 v[24:27], v1 offset:6144\n"
"v_mul_f32 v60, v60, v79\n"
"v_mul_f32 v61, v61, v79\n"
"v_mul_f32 v62, v62, v79\n"
"v_mfma_f32_32x32x16_bf16 v[96:111], v[28:31], a[116:119], v[96:111]\n"
"ds_read_b128 v[28:31], v1 offset:7168\n"
"v_mul_f32 v63, v63, v79\n"
"v_cvt_pk_bf16_f32 v32, v60, v61\n"
"v_mul_f32 v64, v64, v79\n"
"s_waitcnt lgkmcnt(4)\n"
"v_mfma_f32_32x32x16_bf16 v[96:111], v[8:11], a[120:123], v[96:111]\n"
"s_waitcnt vmcnt(9)\n"
"s_barrier\n"
"ds_read_b128 v[8:11], v1 offset:8192\n"
"v_mul_f32 v65, v65, v79\n"
"v_cvt_pk_bf16_f32 v33, v62, v63\n"
"v_mul_f32 v66, v66, v79\n"
"v_mul_f32 v67, v67, v79\n"
"v_mfma_f32_32x32x16_bf16 v[96:111], v[12:15], a[124:127], v[96:111]\n"
"ds_read_b128 v[12:15], v1 offset:9216\n"
"v_cvt_pk_bf16_f32 v34, v64, v65\n"
"v_mul_f32 v68, v68, v79\n"
"v_mul_f32 v69, v69, v79\n"
"s_waitcnt lgkmcnt(4)\n"
"v_mfma_f32_32x32x16_bf16 v[96:111], v[16:19], a[128:131], v[96:111]\n"
"ds_read_b128 v[16:19], v1 offset:10240\n"
"v_cvt_pk_bf16_f32 v35, v66, v67\n"
"v_mul_f32 v70, v70, v79\n"
"v_mul_f32 v71, v71, v79\n"
"v_cvt_pk_bf16_f32 v36, v68, v69\n"
"v_mfma_f32_32x32x16_bf16 v[96:111], v[20:23], a[132:135], v[96:111]\n"
"ds_read_b128 v[20:23], v1 offset:11264\n"
"v_mul_f32 v72, v72, v79\n"
"v_mul_f32 v73, v73, v79\n"
"v_cvt_pk_bf16_f32 v37, v70, v71\n"
"s_waitcnt lgkmcnt(4)\n"
"v_mfma_f32_32x32x16_bf16 v[96:111], v[24:27], a[136:139], v[96:111]\n"
"ds_read_b128 v[24:27], v1 offset:12288\n"
"v_mul_f32 v74, v74, v79\n"
"v_mul_f32 v75, v75, v79\n"
"v_cvt_pk_bf16_f32 v38, v72, v73\n"
"v_mul_f32 v76, v76, v79\n"
"v_mfma_f32_32x32x16_bf16 v[96:111], v[28:31], a[140:143], v[96:111]\n"
"ds_read_b128 v[28:31], v1 offset:13312\n"
"v_mul_f32 v77, v77, v79\n"
"v_cvt_pk_bf16_f32 v39, v74, v75\n"
"v_cvt_pk_bf16_f32 v40, v76, v77\n"
"s_waitcnt lgkmcnt(4)\n"
"v_mfma_f32_32x32x16_bf16 v[112:127], v[8:11], a[0:3], 0\n"
"ds_read_b128 v[8:11], v1 offset:14336\n"
"v_cvt_pk_bf16_f32 v50, v194, v195\n"
"v_cvt_pk_bf16_f32 v51, v196, v197\n"
"v_cvt_pk_bf16_f32 v52, v198, v199\n"
"v_cvt_pk_bf16_f32 v53, v200, v201\n"
"v_cvt_pk_bf16_f32 v54, v202, v203\n"
"v_mfma_f32_32x32x16_bf16 v[112:127], v[12:15], a[4:7], v[112:127]\n"
"ds_read_b128 v[12:15], v1 offset:15360\n"
"v_cvt_pk_bf16_f32 v55, v204, v205\n"
"v_cvt_pk_bf16_f32 v56, v206, v207\n"
"v_cvt_pk_bf16_f32 v57, v208, v209\n"
"v_cvt_pk_bf16_f32 v58, v210, v211\n"
"s_waitcnt lgkmcnt(4)\n"
"v_mfma_f32_32x32x16_bf16 v[112:127], v[16:19], a[8:11], v[112:127]\n"
"ds_read_b128 v[16:19], v1 offset:16384\n"
"s_add_u32 m0, s45, 36864\n"
"s_add_u32 s44, s45, 479232\n"
"buffer_load_dwordx4 v0, s[40:43], s44 offen offset:0 lds\n"
"ds_write_b128 v2, v[32:35] offset:0\n"
"ds_write_b128 v2, v[36:39] offset:16\n"
"ds_write_b128 v2, v[52:55] offset:80\n"
"ds_write_b128 v2, v[56:59] offset:96\n"
"v_min3_f32 v78, v176, v177, v178\n"
"v_mfma_f32_32x32x16_bf16 v[112:127], v[20:23], a[12:15], v[112:127]\n"
"ds_read_b128 v[20:23], v1 offset:17408\n"
"v_min3_f32 v78, v78, v179, v180\n"
"v_min3_f32 v78, v78, v181, v182\n"
"v_min3_f32 v78, v78, v183, v184\n"
"v_min3_f32 v78, v78, v185, v186\n"
"s_waitcnt lgkmcnt(8)\n"
"v_mfma_f32_32x32x16_bf16 v[112:127], v[24:27], a[16:19], v[112:127]\n"
"ds_read_b128 v[24:27], v1 offset:18432\n"
"v_min3_f32 v78, v78, v187, v188\n"
"v_min3_f32 v78, v78, v189, v190\n"
"v_min3_f32 v78, v78, v191, v192\n"
"v_min_f32 v78, v78, v193\n"
"v_sub_f32 v60, v78, v176\n"
"v_mfma_f32_32x32x16_bf16 v[112:127], v[28:31], a[20:23], v[112:127]\n"
"ds_read_b128 v[28:31], v1 offset:19456\n"
"buffer_load_dwordx4 v0, s[40:43], s44 offen offset:1024 lds\n"
"v_sub_f32 v61, v78, v177\n"
"v_exp_f32 v60, v60\n"
"v_sub_f32 v62, v78, v178\n"
"s_waitcnt lgkmcnt(8)\n"
"v_mfma_f32_32x32x16_bf16 v[112:127], v[8:11], a[24:27], v[112:127]\n"
"ds_read_b128 v[8:11], v1 offset:20480\n"
"v_exp_f32 v61, v61\n"
"v_sub_f32 v63, v78, v179\n"
"v_exp_f32 v62, v62\n"
"v_mfma_f32_32x32x16_bf16 v[112:127], v[12:15], a[28:31], v[112:127]\n"
"ds_read_b128 v[12:15], v1 offset:21504\n"
"v_sub_f32 v64, v78, v180\n"
"v_add_f32 v79, v60, v61\n"
"v_exp_f32 v63, v63\n"
"s_waitcnt lgkmcnt(4)\n"
"v_mfma_f32_32x32x16_bf16 v[112:127], v[16:19], a[32:35], v[112:127]\n"
"ds_read_b128 v[16:19], v1 offset:22528\n"
"buffer_load_dwordx4 v0, s[40:43], s44 offen offset:2048 lds\n"
"v_sub_f32 v65, v78, v181\n"
"v_add_f32 v79, v79, v62\n"
"v_exp_f32 v64, v64\n"
"v_sub_f32 v66, v78, v182\n"
"v_mfma_f32_32x32x16_bf16 v[112:127], v[20:23], a[36:39], v[112:127]\n"
"ds_read_b128 v[20:23], v1 offset:23552\n"
"v_add_f32 v79, v79, v63\n"
"v_exp_f32 v65, v65\n"
"v_sub_f32 v67, v78, v183\n"
"s_waitcnt lgkmcnt(4)\n"
"v_mfma_f32_32x32x16_bf16 v[112:127], v[24:27], a[40:43], v[112:127]\n"
"ds_read_b128 v[24:27], v1 offset:24576\n"
"v_add_f32 v79, v79, v64\n"
"v_exp_f32 v66, v66\n"
"v_sub_f32 v68, v78, v184\n"
"v_add_f32 v79, v79, v65\n"
"v_mfma_f32_32x32x16_bf16 v[112:127], v[28:31], a[44:47], v[112:127]\n"
"ds_read_b128 v[28:31], v1 offset:25600\n"
"buffer_load_dwordx4 v0, s[40:43], s44 offen offset:3072 lds\n"
"v_exp_f32 v67, v67\n"
"v_sub_f32 v69, v78, v185\n"
"v_add_f32 v79, v79, v66\n"
"s_waitcnt lgkmcnt(4)\n"
"v_mfma_f32_32x32x16_bf16 v[112:127], v[8:11], a[48:51], v[112:127]\n"
"ds_read_b128 v[8:11], v1 offset:26624\n"
"v_exp_f32 v68, v68\n"
"v_sub_f32 v70, v78, v186\n"
"v_add_f32 v79, v79, v67\n"
"v_mfma_f32_32x32x16_bf16 v[112:127], v[12:15], a[52:55], v[112:127]\n"
"ds_read_b128 v[12:15], v1 offset:27648\n"
"v_exp_f32 v69, v69\n"
"v_sub_f32 v71, v78, v187\n"
"v_add_f32 v79, v79, v68\n"
"v_exp_f32 v70, v70\n"
"s_waitcnt lgkmcnt(4)\n"
"v_mfma_f32_32x32x16_bf16 v[112:127], v[16:19], a[56:59], v[112:127]\n"
"ds_read_b128 v[16:19], v1 offset:28672\n"
"s_add_u32 m0, s45, 40960\n"
"s_add_u32 s44, s45, 483328\n"
"buffer_load_dwordx4 v0, s[40:43], s44 offen offset:0 lds\n"
"v_sub_f32 v72, v78, v188\n"
"v_add_f32 v79, v79, v69\n"
"v_exp_f32 v71, v71\n"
"v_mfma_f32_32x32x16_bf16 v[112:127], v[20:23], a[60:63], v[112:127]\n"
"ds_read_b128 v[20:23], v1 offset:29696\n"
"v_sub_f32 v73, v78, v189\n"
"v_add_f32 v79, v79, v70\n"
"v_exp_f32 v72, v72\n"
"s_waitcnt lgkmcnt(4)\n"
"v_mfma_f32_32x32x16_bf16 v[112:127], v[24:27], a[64:67], v[112:127]\n"
"ds_read_b128 v[24:27], v1 offset:30720\n"
"v_sub_f32 v74, v78, v190\n"
"v_add_f32 v79, v79, v71\n"
"v_exp_f32 v73, v73\n"
"v_sub_f32 v75, v78, v191\n"
"v_mfma_f32_32x32x16_bf16 v[112:127], v[28:31], a[68:71], v[112:127]\n"
"ds_read_b128 v[28:31], v1 offset:31744\n"
"buffer_load_dwordx4 v0, s[40:43], s44 offen offset:1024 lds\n"
"v_add_f32 v79, v79, v72\n"
"v_exp_f32 v74, v74\n"
"v_sub_f32 v76, v78, v192\n"
"s_waitcnt lgkmcnt(4)\n"
"v_mfma_f32_32x32x16_bf16 v[112:127], v[8:11], a[72:75], v[112:127]\n"
"ds_read_b128 v[8:11], v1 offset:32768\n"
"v_add_f32 v79, v79, v73\n"
"v_exp_f32 v75, v75\n"
"v_sub_f32 v77, v78, v193\n"
"v_add_f32 v79, v79, v74\n"
"v_mfma_f32_32x32x16_bf16 v[112:127], v[12:15], a[76:79], v[112:127]\n"
"ds_read_b128 v[12:15], v1 offset:33792\n"
"v_exp_f32 v76, v76\n"
"v_add_f32 v79, v79, v75\n"
"v_exp_f32 v77, v77\n"
"s_waitcnt lgkmcnt(4)\n"
"v_mfma_f32_32x32x16_bf16 v[112:127], v[16:19], a[80:83], v[112:127]\n"
"ds_read_b128 v[16:19], v1 offset:34816\n"
"buffer_load_dwordx4 v0, s[40:43], s44 offen offset:2048 lds\n"
"v_add_f32 v79, v79, v76\n"
"v_add_f32 v79, v79, v77\n"
"v_rcp_f32 v79, v79\n"
"v_mfma_f32_32x32x16_bf16 v[112:127], v[20:23], a[84:87], v[112:127]\n"
"ds_read_b128 v[20:23], v1 offset:35840\n"
"s_nop 0\n"
"v_mul_f32 v60, v60, v79\n"
"v_mul_f32 v61, v61, v79\n"
"v_mul_f32 v62, v62, v79\n"
"s_waitcnt lgkmcnt(4)\n"
"v_mfma_f32_32x32x16_bf16 v[112:127], v[24:27], a[88:91], v[112:127]\n"
"ds_read_b128 v[24:27], v1 offset:36864\n"
"v_mul_f32 v63, v63, v79\n"
"v_cvt_pk_bf16_f32 v41, v60, v61\n"
"v_mul_f32 v64, v64, v79\n"
"v_mul_f32 v65, v65, v79\n"
"v_cvt_pk_bf16_f32 v42, v62, v63\n"
"v_mfma_f32_32x32x16_bf16 v[112:127], v[28:31], a[92:95], v[112:127]\n"
"ds_read_b128 v[28:31], v1 offset:37888\n"
"buffer_load_dwordx4 v0, s[40:43], s44 offen offset:3072 lds\n"
"v_mul_f32 v66, v66, v79\n"
"v_mul_f32 v67, v67, v79\n"
"v_cvt_pk_bf16_f32 v43, v64, v65\n"
"v_mul_f32 v68, v68, v79\n"
"s_waitcnt lgkmcnt(4)\n"
"v_mfma_f32_32x32x16_bf16 v[112:127], v[8:11], a[96:99], v[112:127]\n"
"ds_read_b128 v[8:11], v1 offset:38912\n"
"v_mul_f32 v69, v69, v79\n"
"v_cvt_pk_bf16_f32 v44, v66, v67\n"
"v_mul_f32 v70, v70, v79\n"
"v_mul_f32 v71, v71, v79\n"
"v_cvt_pk_bf16_f32 v45, v68, v69\n"
"v_mfma_f32_32x32x16_bf16 v[112:127], v[12:15], a[100:103], v[112:127]\n"
"ds_read_b128 v[12:15], v1 offset:39936\n"
"v_mul_f32 v72, v72, v79\n"
"v_mul_f32 v73, v73, v79\n"
"v_cvt_pk_bf16_f32 v46, v70, v71\n"
"v_mul_f32 v74, v74, v79\n"
"s_waitcnt lgkmcnt(4)\n"
"v_mfma_f32_32x32x16_bf16 v[112:127], v[16:19], a[104:107], v[112:127]\n"
"ds_read_b128 v[16:19], v1 offset:40960\n"
"s_add_u32 m0, s45, 45056\n"
"s_add_u32 s44, s45, 487424\n"
"buffer_load_dwordx4 v0, s[40:43], s44 offen offset:0 lds\n"
"v_mul_f32 v75, v75, v79\n"
"v_cvt_pk_bf16_f32 v47, v72, v73\n"
"v_mul_f32 v76, v76, v79\n"
"v_mul_f32 v77, v77, v79\n"
"v_cvt_pk_bf16_f32 v48, v74, v75\n"
"v_mfma_f32_32x32x16_bf16 v[112:127], v[20:23], a[108:111], v[112:127]\n"
"ds_read_b128 v[20:23], v1 offset:41984\n"
"v_cvt_pk_bf16_f32 v49, v76, v77\n"
"ds_write_b128 v2, v[40:43] offset:32\n"
"ds_write_b128 v2, v[44:47] offset:48\n"
"ds_write_b128 v2, v[48:51] offset:64\n"
"s_waitcnt lgkmcnt(7)\n"
"v_mfma_f32_32x32x16_bf16 v[112:127], v[24:27], a[112:115], v[112:127]\n"
"ds_read_b128 v[24:27], v1 offset:43008\n"
"v_mfma_f32_32x32x16_bf16 v[112:127], v[28:31], a[116:119], v[112:127]\n"
"ds_read_b128 v[28:31], v1 offset:44032\n"
"s_waitcnt lgkmcnt(7)\n"
"v_mfma_f32_32x32x16_bf16 v[112:127], v[8:11], a[120:123], v[112:127]\n"
"s_waitcnt lgkmcnt(2)\n"
"s_waitcnt vmcnt(9)\n"
"s_barrier\n"
"ds_read_b128 v[8:11], v0 offset:0\n"
"v_mfma_f32_32x32x16_bf16 v[112:127], v[12:15], a[124:127], v[112:127]\n"
"ds_read_b128 v[12:15], v0 offset:1024\n"
"v_mfma_f32_32x32x16_bf16 v[112:127], v[16:19], a[128:131], v[112:127]\n"
"ds_read_b128 v[16:19], v0 offset:2048\n"
"v_mfma_f32_32x32x16_bf16 v[112:127], v[20:23], a[132:135], v[112:127]\n"
"ds_read_b128 v[20:23], v0 offset:3072\n"
"s_waitcnt lgkmcnt(4)\n"
"v_mfma_f32_32x32x16_bf16 v[112:127], v[24:27], a[136:139], v[112:127]\n"
"ds_read_b128 v[24:27], v0 offset:4096\n"
"v_mfma_f32_32x32x16_bf16 v[112:127], v[28:31], a[140:143], v[112:127]\n"
"ds_read_b128 v[28:31], v0 offset:5120\n"
"s_waitcnt lgkmcnt(4)\n"
"v_mfma_f32_32x32x16_bf16 v[128:143], v[8:11], a[0:3], 0\n"
"ds_read_b128 v[8:11], v0 offset:6144\n"
"ds_read_b64_tr_b16 a[144:145], v3 offset:0\n"
"ds_read_b64_tr_b16 a[146:147], v3 offset:960\n"
"ds_read_b64_tr_b16 a[148:149], v4 offset:0\n"
"ds_read_b64_tr_b16 a[150:151], v4 offset:960\n"
"v_mfma_f32_32x32x16_bf16 v[128:143], v[12:15], a[4:7], v[128:143]\n"
"ds_read_b128 v[12:15], v0 offset:7168\n"
"ds_read_b64_tr_b16 a[152:153], v3 offset:3840\n"
"ds_read_b64_tr_b16 a[154:155], v3 offset:4800\n"
"ds_read_b64_tr_b16 a[156:157], v4 offset:3840\n"
"ds_read_b64_tr_b16 a[158:159], v4 offset:4800\n"
"s_waitcnt lgkmcnt(12)\n"
"v_mfma_f32_32x32x16_bf16 v[128:143], v[16:19], a[8:11], v[128:143]\n"
"ds_read_b128 v[16:19], v0 offset:8192\n"
"s_add_u32 m0, s45, 73728\n"
"s_add_u32 s44, s45, 516096\n"
"buffer_load_dwordx4 v0, s[40:43], s44 offen offset:0 lds\n"
"ds_read_b64_tr_b16 a[160:161], v3 offset:7680\n"
"ds_read_b64_tr_b16 a[162:163], v3 offset:8640\n"
"ds_read_b64_tr_b16 a[164:165], v4 offset:7680\n"
"ds_read_b64_tr_b16 a[166:167], v4 offset:8640\n"
"v_mfma_f32_32x32x16_bf16 v[128:143], v[20:23], a[12:15], v[128:143]\n"
"ds_read_b128 v[20:23], v0 offset:9216\n"
"ds_read_b64_tr_b16 a[168:169], v3 offset:11520\n"
"ds_read_b64_tr_b16 a[170:171], v3 offset:12480\n"
"ds_read_b64_tr_b16 a[172:173], v4 offset:11520\n"
"ds_read_b64_tr_b16 a[174:175], v4 offset:12480\n"
"s_waitcnt lgkmcnt(15)\n"
"v_mfma_f32_32x32x16_bf16 v[128:143], v[24:27], a[16:19], v[128:143]\n"
"ds_read_b128 v[24:27], v0 offset:10240\n"
"v_mfma_f32_32x32x16_bf16 v[80:95], a[144:147], a[148:151], 0\n"
"ds_read_b64_tr_b16 a[176:177], v3 offset:15360\n"
"ds_read_b64_tr_b16 a[178:179], v3 offset:16320\n"
"ds_read_b64_tr_b16 a[180:181], v4 offset:15360\n"
"ds_read_b64_tr_b16 a[182:183], v4 offset:16320\n"
"v_mfma_f32_32x32x16_bf16 v[128:143], v[28:31], a[20:23], v[128:143]\n"
"ds_read_b128 v[28:31], v0 offset:11264\n"
"buffer_load_dwordx4 v0, s[40:43], s44 offen offset:1024 lds\n"
"s_waitcnt lgkmcnt(15)\n"
"v_mfma_f32_32x32x16_bf16 v[80:95], a[152:155], a[156:159], v[80:95]\n"
"ds_read_b64_tr_b16 a[184:185], v3 offset:19200\n"
"ds_read_b64_tr_b16 a[186:187], v3 offset:20160\n"
"ds_read_b64_tr_b16 a[188:189], v4 offset:19200\n"
"ds_read_b64_tr_b16 a[190:191], v4 offset:20160\n"
"v_mfma_f32_32x32x16_bf16 v[128:143], v[8:11], a[24:27], v[128:143]\n"
"ds_read_b128 v[8:11], v0 offset:12288\n"
"s_waitcnt lgkmcnt(15)\n"
"v_mfma_f32_32x32x16_bf16 v[80:95], a[160:163], a[164:167], v[80:95]\n"
"ds_read_b64_tr_b16 a[192:193], v3 offset:23040\n"
"ds_read_b64_tr_b16 a[194:195], v3 offset:24000\n"
"ds_read_b64_tr_b16 a[196:197], v4 offset:23040\n"
"ds_read_b64_tr_b16 a[198:199], v4 offset:24000\n"
"v_mfma_f32_32x32x16_bf16 v[128:143], v[12:15], a[28:31], v[128:143]\n"
"ds_read_b128 v[12:15], v0 offset:13312\n"
"s_waitcnt lgkmcnt(15)\n"
"v_mfma_f32_32x32x16_bf16 v[80:95], a[168:171], a[172:175], v[80:95]\n"
"ds_read_b64_tr_b16 a[200:201], v3 offset:26880\n"
"ds_read_b64_tr_b16 a[202:203], v3 offset:27840\n"
"ds_read_b64_tr_b16 a[204:205], v4 offset:26880\n"
"ds_read_b64_tr_b16 a[206:207], v4 offset:27840\n"
"v_mfma_f32_32x32x16_bf16 v[128:143], v[16:19], a[32:35], v[128:143]\n"
"ds_read_b128 v[16:19], v0 offset:14336\n"
"buffer_load_dwordx4 v0, s[40:43], s44 offen offset:2048 lds\n"
"s_waitcnt lgkmcnt(15)\n"
"v_mfma_f32_32x32x16_bf16 v[80:95], a[176:179], a[180:183], v[80:95]\n"
"v_max3_f32 v78, v212, v213, v214\n"
"v_max3_f32 v78, v78, v215, v216\n"
"v_max3_f32 v78, v78, v217, v218\n"
"v_max3_f32 v78, v78, v219, v220\n"
"v_mfma_f32_32x32x16_bf16 v[128:143], v[20:23], a[36:39], v[128:143]\n"
"ds_read_b128 v[20:23], v0 offset:15360\n"
"s_waitcnt lgkmcnt(12)\n"
"v_mfma_f32_32x32x16_bf16 v[80:95], a[184:187], a[188:191], v[80:95]\n"
"v_max3_f32 v78, v78, v221, v222\n"
"v_max3_f32 v78, v78, v223, v224\n"
"v_max3_f32 v78, v78, v225, v226\n"
"v_max3_f32 v78, v78, v227, v228\n"
"v_mfma_f32_32x32x16_bf16 v[128:143], v[24:27], a[40:43], v[128:143]\n"
"ds_read_b128 v[24:27], v0 offset:16384\n"
"s_waitcnt lgkmcnt(8)\n"
"v_mfma_f32_32x32x16_bf16 v[80:95], a[192:195], a[196:199], v[80:95]\n"
"v_max_f32 v78, v78, v229\n"
"v_sub_f32 v60, v212, v78\n"
"v_sub_f32 v61, v213, v78\n"
"v_mfma_f32_32x32x16_bf16 v[128:143], v[28:31], a[44:47], v[128:143]\n"
"ds_read_b128 v[28:31], v0 offset:17408\n"
"buffer_load_dwordx4 v0, s[40:43], s44 offen offset:3072 lds\n"
"s_waitcnt lgkmcnt(4)\n"
"v_mfma_f32_32x32x16_bf16 v[80:95], a[200:203], a[204:207], v[80:95]\n"
"v_exp_f32 v60, v60\n"
"v_sub_f32 v62, v214, v78\n"
"v_exp_f32 v61, v61\n"
"v_mfma_f32_32x32x16_bf16 v[128:143], v[8:11], a[48:51], v[128:143]\n"
"ds_read_b128 v[8:11], v0 offset:18432\n"
"v_sub_f32 v63, v215, v78\n"
"v_exp_f32 v62, v62\n"
"v_sub_f32 v64, v216, v78\n"
"v_mfma_f32_32x32x16_bf16 v[128:143], v[12:15], a[52:55], v[128:143]\n"
"ds_read_b128 v[12:15], v0 offset:19456\n"
"v_add_f32 v79, v60, v61\n"
"v_exp_f32 v63, v63\n"
"v_sub_f32 v65, v217, v78\n"
"s_waitcnt lgkmcnt(4)\n"
"v_mfma_f32_32x32x16_bf16 v[128:143], v[16:19], a[56:59], v[128:143]\n"
"ds_read_b128 v[16:19], v0 offset:20480\n"
"s_add_u32 m0, s45, 77824\n"
"s_add_u32 s44, s45, 520192\n"
"buffer_load_dwordx4 v0, s[40:43], s44 offen offset:0 lds\n"
"v_add_f32 v79, v79, v62\n"
"v_exp_f32 v64, v64\n"
"v_sub_f32 v66, v218, v78\n"
"v_mfma_f32_32x32x16_bf16 v[128:143], v[20:23], a[60:63], v[128:143]\n"
"ds_read_b128 v[20:23], v0 offset:21504\n"
"v_add_f32 v79, v79, v63\n"
"v_exp_f32 v65, v65\n"
"s_waitcnt lgkmcnt(4)\n"
"v_mfma_f32_32x32x16_bf16 v[128:143], v[24:27], a[64:67], v[128:143]\n"
"ds_read_b128 v[24:27], v0 offset:22528\n"
"v_sub_f32 v67, v219, v78\n"
"v_add_f32 v79, v79, v64\n"
"v_exp_f32 v66, v66\n"
"v_mfma_f32_32x32x16_bf16 v[128:143], v[28:31], a[68:71], v[128:143]\n"
"ds_read_b128 v[28:31], v0 offset:23552\n"
"buffer_load_dwordx4 v0, s[40:43], s44 offen offset:1024 lds\n"
"v_sub_f32 v68, v220, v78\n"
"v_add_f32 v79, v79, v65\n"
"v_exp_f32 v67, v67\n"
"s_waitcnt lgkmcnt(4)\n"
"v_mfma_f32_32x32x16_bf16 v[128:143], v[8:11], a[72:75], v[128:143]\n"
"ds_read_b128 v[8:11], v0 offset:24576\n"
"s_mov_b64 exec, s[48:49]\n"
"global_atomic_add_f32 v5, v80, s[46:47] offset:0\n"
"global_atomic_add_f32 v5, v81, s[46:47] offset:160\n"
"global_atomic_add_f32 v5, v82, s[46:47] offset:320\n"
"global_atomic_add_f32 v5, v83, s[46:47] offset:480\n"
"s_mov_b64 exec, -1\n"
"v_sub_f32 v69, v221, v78\n"
"v_add_f32 v79, v79, v66\n"
"v_exp_f32 v68, v68\n"
"v_mfma_f32_32x32x16_bf16 v[128:143], v[12:15], a[76:79], v[128:143]\n"
"ds_read_b128 v[12:15], v0 offset:25600\n"
"v_sub_f32 v70, v222, v78\n"
"v_add_f32 v79, v79, v67\n"
"v_exp_f32 v69, v69\n"
"s_waitcnt lgkmcnt(4)\n"
"v_mfma_f32_32x32x16_bf16 v[128:143], v[16:19], a[80:83], v[128:143]\n"
"ds_read_b128 v[16:19], v0 offset:26624\n"
"buffer_load_dwordx4 v0, s[40:43], s44 offen offset:2048 lds\n"
"v_sub_f32 v71, v223, v78\n"
"v_add_f32 v79, v79, v68\n"
"v_exp_f32 v70, v70\n"
"v_mfma_f32_32x32x16_bf16 v[128:143], v[20:23], a[84:87], v[128:143]\n"
"ds_read_b128 v[20:23], v0 offset:27648\n"
"s_mov_b64 exec, s[48:49]\n"
"global_atomic_add_f32 v5, v84, s[46:47] offset:1280\n"
"global_atomic_add_f32 v5, v85, s[46:47] offset:1440\n"
"global_atomic_add_f32 v5, v86, s[46:47] offset:1600\n"
"global_atomic_add_f32 v5, v87, s[46:47] offset:1760\n"
"s_mov_b64 exec, -1\n"
"v_sub_f32 v72, v224, v78\n"
"v_add_f32 v79, v79, v69\n"
"v_exp_f32 v71, v71\n"
"s_waitcnt lgkmcnt(4)\n"
"v_mfma_f32_32x32x16_bf16 v[128:143], v[24:27], a[88:91], v[128:143]\n"
"ds_read_b128 v[24:27], v0 offset:28672\n"
"v_sub_f32 v73, v225, v78\n"
"v_add_f32 v79, v79, v70\n"
"v_exp_f32 v72, v72\n"
"v_mfma_f32_32x32x16_bf16 v[128:143], v[28:31], a[92:95], v[128:143]\n"
"ds_read_b128 v[28:31], v0 offset:29696\n"
"buffer_load_dwordx4 v0, s[40:43], s44 offen offset:3072 lds\n"
"v_sub_f32 v74, v226, v78\n"
"v_add_f32 v79, v79, v71\n"
"v_exp_f32 v73, v73\n"
"s_waitcnt lgkmcnt(4)\n"
"v_mfma_f32_32x32x16_bf16 v[128:143], v[8:11], a[96:99], v[128:143]\n"
"ds_read_b128 v[8:11], v0 offset:30720\n"
"s_mov_b64 exec, s[48:49]\n"
"s_mov_b32 exec_hi, 0\n"
"global_atomic_add_f32 v5, v88, s[46:47] offset:2560\n"
"global_atomic_add_f32 v5, v89, s[46:47] offset:2720\n"
"global_atomic_add_f32 v5, v90, s[46:47] offset:2880\n"
"s_mov_b64 exec, -1\n"
"s_add_u32 s46, s46, 3040\n"
"s_addc_u32 s47, s47, 0\n"
"v_sub_f32 v75, v227, v78\n"
"v_add_f32 v79, v79, v72\n"
"v_exp_f32 v74, v74\n"
"v_mfma_f32_32x32x16_bf16 v[128:143], v[12:15], a[100:103], v[128:143]\n"
"ds_read_b128 v[12:15], v0 offset:31744\n"
"v_sub_f32 v76, v228, v78\n"
"v_add_f32 v79, v79, v73\n"
"v_exp_f32 v75, v75\n"
"s_waitcnt lgkmcnt(4)\n"
"v_mfma_f32_32x32x16_bf16 v[128:143], v[16:19], a[104:107], v[128:143]\n"
"ds_read_b128 v[16:19], v0 offset:32768\n"
"s_add_u32 m0, s45, 81920\n"
"s_add_u32 s44, s45, 524288\n"
"buffer_load_dwordx4 v0, s[40:43], s44 offen offset:0 lds\n"
"v_sub_f32 v77, v229, v78\n"
"v_add_f32 v79, v79, v74\n"
"v_exp_f32 v76, v76\n"
"v_mfma_f32_32x32x16_bf16 v[128:143], v[20:23], a[108:111], v[128:143]\n"
"ds_read_b128 v[20:23], v0 offset:33792\n"
"v_add_f32 v79, v79, v75\n"
"v_exp_f32 v77, v77\n"
"v_add_f32 v79, v79, v76\n"
"s_waitcnt lgkmcnt(4)\n"
"v_mfma_f32_32x32x16_bf16 v[128:143], v[24:27], a[112:115], v[128:143]\n"
"ds_read_b128 v[24:27], v0 offset:34816\n"
"v_add_f32 v79, v79, v77\n"
"v_rcp_f32 v79, v79\n"
"s_nop 0\n"
"v_mfma_f32_32x32x16_bf16 v[128:143], v[28:31], a[116:119], v[128:143]\n"
"ds_read_b128 v[28:31], v0 offset:35840\n"
"v_mul_f32 v60, v60, v79\n"
"v_mul_f32 v61, v61, v79\n"
"v_mul_f32 v62, v62, v79\n"
"s_waitcnt lgkmcnt(4)\n"
"v_mfma_f32_32x32x16_bf16 v[128:143], v[8:11], a[120:123], v[128:143]\n"
"s_waitcnt vmcnt(20)\n"
"s_barrier\n"
"ds_read_b128 v[8:11], v0 offset:36864\n"
"v_mul_f32 v63, v63, v79\n"
"v_cvt_pk_bf16_f32 v32, v60, v61\n"
"v_mul_f32 v64, v64, v79\n"
"v_mul_f32 v65, v65, v79\n"
"v_mfma_f32_32x32x16_bf16 v[128:143], v[12:15], a[124:127], v[128:143]\n"
"ds_read_b128 v[12:15], v0 offset:37888\n"
"v_cvt_pk_bf16_f32 v33, v62, v63\n"
"v_mul_f32 v66, v66, v79\n"
"v_mul_f32 v67, v67, v79\n"
"v_cvt_pk_bf16_f32 v34, v64, v65\n"
"s_waitcnt lgkmcnt(4)\n"
"v_mfma_f32_32x32x16_bf16 v[128:143], v[16:19], a[128:131], v[128:143]\n"
"ds_read_b128 v[16:19], v0 offset:38912\n"
"v_mul_f32 v68, v68, v79\n"
"v_mul_f32 v69, v69, v79\n"
"v_cvt_pk_bf16_f32 v35, v66, v67\n"
"v_mul_f32 v70, v70, v79\n"
"v_mfma_f32_32x32x16_bf16 v[128:143], v[20:23], a[132:135], v[128:143]\n"
"ds_read_b128 v[20:23], v0 offset:39936\n"
"v_mul_f32 v71, v71, v79\n"
"v_cvt_pk_bf16_f32 v36, v68, v69\n"
"v_mul_f32 v72, v72, v79\n"
"v_mul_f32 v73, v73, v79\n"
"s_waitcnt lgkmcnt(4)\n"
"v_mfma_f32_32x32x16_bf16 v[128:143], v[24:27], a[136:139], v[128:143]\n"
"ds_read_b128 v[24:27], v0 offset:40960\n"
"v_cvt_pk_bf16_f32 v37, v70, v71\n"
"v_mul_f32 v74, v74, v79\n"
"v_mul_f32 v75, v75, v79\n"
"v_cvt_pk_bf16_f32 v38, v72, v73\n"
"v_mfma_f32_32x32x16_bf16 v[128:143], v[28:31], a[140:143], v[128:143]\n"
"ds_read_b128 v[28:31], v0 offset:41984\n"
"v_mul_f32 v76, v76, v79\n"
"v_mul_f32 v77, v77, v79\n"
"v_cvt_pk_bf16_f32 v39, v74, v75\n"
"v_cvt_pk_bf16_f32 v40, v76, v77\n"
"s_waitcnt lgkmcnt(4)\n"
"v_mfma_f32_32x32x16_bf16 v[144:159], v[8:11], a[0:3], 0\n"
"ds_read_b128 v[8:11], v0 offset:43008\n"
"v_cvt_pk_bf16_f32 v50, v230, v231\n"
"v_cvt_pk_bf16_f32 v51, v232, v233\n"
"v_cvt_pk_bf16_f32 v52, v234, v235\n"
"v_cvt_pk_bf16_f32 v53, v236, v237\n"
"v_cvt_pk_bf16_f32 v54, v238, v239\n"
"v_mfma_f32_32x32x16_bf16 v[144:159], v[12:15], a[4:7], v[144:159]\n"
"ds_read_b128 v[12:15], v0 offset:44032\n"
"v_cvt_pk_bf16_f32 v55, v240, v241\n"
"v_cvt_pk_bf16_f32 v56, v242, v243\n"
"v_cvt_pk_bf16_f32 v57, v244, v245\n"
"v_cvt_pk_bf16_f32 v58, v246, v247\n"
"s_waitcnt lgkmcnt(4)\n"
"v_mfma_f32_32x32x16_bf16 v[144:159], v[16:19], a[8:11], v[144:159]\n"
"ds_read_b128 v[16:19], v0 offset:45056\n"
"s_add_u32 m0, s45, 0\n"
"s_add_u32 s44, s45, 552960\n"
"buffer_load_dwordx4 v0, s[40:43], s44 offen offset:0 lds\n"
"ds_write_b128 v2, v[32:35] offset:0\n"
"ds_write_b128 v2, v[36:39] offset:16\n"
"ds_write_b128 v2, v[52:55] offset:80\n"
"ds_write_b128 v2, v[56:59] offset:96\n"
"v_min3_f32 v78, v212, v213, v214\n"
"v_mfma_f32_32x32x16_bf16 v[144:159], v[20:23], a[12:15], v[144:159]\n"
"ds_read_b128 v[20:23], v0 offset:46080\n"
"v_min3_f32 v78, v78, v215, v216\n"
"v_min3_f32 v78, v78, v217, v218\n"
"v_min3_f32 v78, v78, v219, v220\n"
"v_min3_f32 v78, v78, v221, v222\n"
"s_waitcnt lgkmcnt(8)\n"
"v_mfma_f32_32x32x16_bf16 v[144:159], v[24:27], a[16:19], v[144:159]\n"
"ds_read_b128 v[24:27], v0 offset:47104\n"
"v_min3_f32 v78, v78, v223, v224\n"
"v_min3_f32 v78, v78, v225, v226\n"
"v_min3_f32 v78, v78, v227, v228\n"
"v_min_f32 v78, v78, v229\n"
"v_sub_f32 v60, v78, v212\n"
"v_mfma_f32_32x32x16_bf16 v[144:159], v[28:31], a[20:23], v[144:159]\n"
"ds_read_b128 v[28:31], v0 offset:48128\n"
"buffer_load_dwordx4 v0, s[40:43], s44 offen offset:1024 lds\n"
"v_sub_f32 v61, v78, v213\n"
"v_exp_f32 v60, v60\n"
"v_sub_f32 v62, v78, v214\n"
"s_waitcnt lgkmcnt(8)\n"
"v_mfma_f32_32x32x16_bf16 v[144:159], v[8:11], a[24:27], v[144:159]\n"
"ds_read_b128 v[8:11], v0 offset:49152\n"
"v_exp_f32 v61, v61\n"
"v_sub_f32 v63, v78, v215\n"
"v_exp_f32 v62, v62\n"
"v_mfma_f32_32x32x16_bf16 v[144:159], v[12:15], a[28:31], v[144:159]\n"
"ds_read_b128 v[12:15], v0 offset:50176\n"
"v_sub_f32 v64, v78, v216\n"
"v_add_f32 v79, v60, v61\n"
"v_exp_f32 v63, v63\n"
"s_waitcnt lgkmcnt(4)\n"
"v_mfma_f32_32x32x16_bf16 v[144:159], v[16:19], a[32:35], v[144:159]\n"
"ds_read_b128 v[16:19], v0 offset:51200\n"
"buffer_load_dwordx4 v0, s[40:43], s44 offen offset:2048 lds\n"
"v_sub_f32 v65, v78, v217\n"
"v_add_f32 v79, v79, v62\n"
"v_exp_f32 v64, v64\n"
"v_sub_f32 v66, v78, v218\n"
"v_mfma_f32_32x32x16_bf16 v[144:159], v[20:23], a[36:39], v[144:159]\n"
"ds_read_b128 v[20:23], v0 offset:52224\n"
"v_add_f32 v79, v79, v63\n"
"v_exp_f32 v65, v65\n"
"v_sub_f32 v67, v78, v219\n"
"s_waitcnt lgkmcnt(4)\n"
"v_mfma_f32_32x32x16_bf16 v[144:159], v[24:27], a[40:43], v[144:159]\n"
"ds_read_b128 v[24:27], v0 offset:53248\n"
"v_add_f32 v79, v79, v64\n"
"v_exp_f32 v66, v66\n"
"v_sub_f32 v68, v78, v220\n"
"v_add_f32 v79, v79, v65\n"
"v_mfma_f32_32x32x16_bf16 v[144:159], v[28:31], a[44:47], v[144:159]\n"
"ds_read_b128 v[28:31], v0 offset:54272\n"
"buffer_load_dwordx4 v0, s[40:43], s44 offen offset:3072 lds\n"
"v_exp_f32 v67, v67\n"
"v_sub_f32 v69, v78, v221\n"
"v_add_f32 v79, v79, v66\n"
"s_waitcnt lgkmcnt(4)\n"
"v_mfma_f32_32x32x16_bf16 v[144:159], v[8:11], a[48:51], v[144:159]\n"
"ds_read_b128 v[8:11], v0 offset:55296\n"
"v_exp_f32 v68, v68\n"
"v_sub_f32 v70, v78, v222\n"
"v_add_f32 v79, v79, v67\n"
"v_mfma_f32_32x32x16_bf16 v[144:159], v[12:15], a[52:55], v[144:159]\n"
"ds_read_b128 v[12:15], v0 offset:56320\n"
"v_exp_f32 v69, v69\n"
"v_sub_f32 v71, v78, v223\n"
"v_add_f32 v79, v79, v68\n"
"v_exp_f32 v70, v70\n"
"s_waitcnt lgkmcnt(4)\n"
"v_mfma_f32_32x32x16_bf16 v[144:159], v[16:19], a[56:59], v[144:159]\n"
"ds_read_b128 v[16:19], v0 offset:57344\n"
"s_add_u32 m0, s45, 4096\n"
"s_add_u32 s44, s45, 557056\n"
"buffer_load_dwordx4 v0, s[40:43], s44 offen offset:0 lds\n"
"v_sub_f32 v72, v78, v224\n"
"v_add_f32 v79, v79, v69\n"
"v_exp_f32 v71, v71\n"
"v_mfma_f32_32x32x16_bf16 v[144:159], v[20:23], a[60:63], v[144:159]\n"
"ds_read_b128 v[20:23], v0 offset:58368\n"
"v_sub_f32 v73, v78, v225\n"
"v_add_f32 v79, v79, v70\n"
"v_exp_f32 v72, v72\n"
"s_waitcnt lgkmcnt(4)\n"
"v_mfma_f32_32x32x16_bf16 v[144:159], v[24:27], a[64:67], v[144:159]\n"
"ds_read_b128 v[24:27], v0 offset:59392\n"
"v_sub_f32 v74, v78, v226\n"
"v_add_f32 v79, v79, v71\n"
"v_exp_f32 v73, v73\n"
"v_sub_f32 v75, v78, v227\n"
"v_mfma_f32_32x32x16_bf16 v[144:159], v[28:31], a[68:71], v[144:159]\n"
"ds_read_b128 v[28:31], v0 offset:60416\n"
"buffer_load_dwordx4 v0, s[40:43], s44 offen offset:1024 lds\n"
"v_add_f32 v79, v79, v72\n"
"v_exp_f32 v74, v74\n"
"v_sub_f32 v76, v78, v228\n"
"s_waitcnt lgkmcnt(4)\n"
"v_mfma_f32_32x32x16_bf16 v[144:159], v[8:11], a[72:75], v[144:159]\n"
"ds_read_b128 v[8:11], v0 offset:61440\n"
"v_add_f32 v79, v79, v73\n"
"v_exp_f32 v75, v75\n"
"v_sub_f32 v77, v78, v229\n"
"v_add_f32 v79, v79, v74\n"
"v_mfma_f32_32x32x16_bf16 v[144:159], v[12:15], a[76:79], v[144:159]\n"
"ds_read_b128 v[12:15], v0 offset:62464\n"
"v_exp_f32 v76, v76\n"
"v_add_f32 v79, v79, v75\n"
"v_exp_f32 v77, v77\n"
"s_waitcnt lgkmcnt(4)\n"
"v_mfma_f32_32x32x16_bf16 v[144:159], v[16:19], a[80:83], v[144:159]\n"
"ds_read_b128 v[16:19], v0 offset:63488\n"
"buffer_load_dwordx4 v0, s[40:43], s44 offen offset:2048 lds\n"
"v_add_f32 v79, v79, v76\n"
"v_add_f32 v79, v79, v77\n"
"v_rcp_f32 v79, v79\n"
"v_mfma_f32_32x32x16_bf16 v[144:159], v[20:23], a[84:87], v[144:159]\n"
"ds_read_b128 v[20:23], v0 offset:64512\n"
"s_nop 0\n"
"v_mul_f32 v60, v60, v79\n"
"v_mul_f32 v61, v61, v79\n"
"v_mul_f32 v62, v62, v79\n"
"s_waitcnt lgkmcnt(4)\n"
"v_mfma_f32_32x32x16_bf16 v[144:159], v[24:27], a[88:91], v[144:159]\n"
"ds_read_b128 v[24:27], v1 offset:0\n"
"v_mul_f32 v63, v63, v79\n"
"v_cvt_pk_bf16_f32 v41, v60, v61\n"
"v_mul_f32 v64, v64, v79\n"
"v_mul_f32 v65, v65, v79\n"
"v_cvt_pk_bf16_f32 v42, v62, v63\n"
"v_mfma_f32_32x32x16_bf16 v[144:159], v[28:31], a[92:95], v[144:159]\n"
"ds_read_b128 v[28:31], v1 offset:1024\n"
"buffer_load_dwordx4 v0, s[40:43], s44 offen offset:3072 lds\n"
"v_mul_f32 v66, v66, v79\n"
"v_mul_f32 v67, v67, v79\n"
"v_cvt_pk_bf16_f32 v43, v64, v65\n"
"v_mul_f32 v68, v68, v79\n"
"s_waitcnt lgkmcnt(4)\n"
"v_mfma_f32_32x32x16_bf16 v[144:159], v[8:11], a[96:99], v[144:159]\n"
"ds_read_b128 v[8:11], v1 offset:2048\n"
"v_mul_f32 v69, v69, v79\n"
"v_cvt_pk_bf16_f32 v44, v66, v67\n"
"v_mul_f32 v70, v70, v79\n"
"v_mul_f32 v71, v71, v79\n"
"v_cvt_pk_bf16_f32 v45, v68, v69\n"
"v_mfma_f32_32x32x16_bf16 v[144:159], v[12:15], a[100:103], v[144:159]\n"
"ds_read_b128 v[12:15], v1 offset:3072\n"
"v_mul_f32 v72, v72, v79\n"
"v_mul_f32 v73, v73, v79\n"
"v_cvt_pk_bf16_f32 v46, v70, v71\n"
"v_mul_f32 v74, v74, v79\n"
"s_waitcnt lgkmcnt(4)\n"
"v_mfma_f32_32x32x16_bf16 v[144:159], v[16:19], a[104:107], v[144:159]\n"
"ds_read_b128 v[16:19], v1 offset:4096\n"
"s_add_u32 m0, s45, 8192\n"
"s_add_u32 s44, s45, 561152\n"
"buffer_load_dwordx4 v0, s[40:43], s44 offen offset:0 lds\n"
"v_mul_f32 v75, v75, v79\n"
"v_cvt_pk_bf16_f32 v47, v72, v73\n"
"v_mul_f32 v76, v76, v79\n"
"v_mul_f32 v77, v77, v79\n"
"v_cvt_pk_bf16_f32 v48, v74, v75\n"
"v_mfma_f32_32x32x16_bf16 v[144:159], v[20:23], a[108:111], v[144:159]\n"
"ds_read_b128 v[20:23], v1 offset:5120\n"
"v_cvt_pk_bf16_f32 v49, v76, v77\n"
"ds_write_b128 v2, v[40:43] offset:32\n"
"ds_write_b128 v2, v[44:47] offset:48\n"
"ds_write_b128 v2, v[48:51] offset:64\n"
"s_waitcnt lgkmcnt(7)\n"
"v_mfma_f32_32x32x16_bf16 v[144:159], v[24:27], a[112:115], v[144:159]\n"
"ds_read_b128 v[24:27], v1 offset:6144\n"
"v_mfma_f32_32x32x16_bf16 v[144:159], v[28:31], a[116:119], v[144:159]\n"
"ds_read_b128 v[28:31], v1 offset:7168\n"
"s_waitcnt lgkmcnt(7)\n"
"v_mfma_f32_32x32x16_bf16 v[144:159], v[8:11], a[120:123], v[144:159]\n"
"s_waitcnt lgkmcnt(2)\n"
"s_waitcnt vmcnt(9)\n"
"s_barrier\n"
"ds_read_b128 v[8:11], v1 offset:8192\n"
"v_mfma_f32_32x32x16_bf16 v[144:159], v[12:15], a[124:127], v[144:159]\n"
"ds_read_b128 v[12:15], v1 offset:9216\n"
"v_mfma_f32_32x32x16_bf16 v[144:159], v[16:19], a[128:131], v[144:159]\n"
"ds_read_b128 v[16:19], v1 offset:10240\n"
"v_mfma_f32_32x32x16_bf16 v[144:159], v[20:23], a[132:135], v[144:159]\n"
"ds_read_b128 v[20:23], v1 offset:11264\n"
"s_waitcnt lgkmcnt(4)\n"
"v_mfma_f32_32x32x16_bf16 v[144:159], v[24:27], a[136:139], v[144:159]\n"
"ds_read_b128 v[24:27], v1 offset:12288\n"
"v_mfma_f32_32x32x16_bf16 v[144:159], v[28:31], a[140:143], v[144:159]\n"
"ds_read_b128 v[28:31], v1 offset:13312\n"
"s_waitcnt lgkmcnt(4)\n"
"v_mfma_f32_32x32x16_bf16 v[160:175], v[8:11], a[0:3], 0\n"
"ds_read_b128 v[8:11], v1 offset:14336\n"
"ds_read_b64_tr_b16 a[144:145], v3 offset:0\n"
"ds_read_b64_tr_b16 a[146:147], v3 offset:960\n"
"ds_read_b64_tr_b16 a[148:149], v4 offset:0\n"
"ds_read_b64_tr_b16 a[150:151], v4 offset:960\n"
"v_mfma_f32_32x32x16_bf16 v[160:175], v[12:15], a[4:7], v[160:175]\n"
"ds_read_b128 v[12:15], v1 offset:15360\n"
"ds_read_b64_tr_b16 a[152:153], v3 offset:3840\n"
"ds_read_b64_tr_b16 a[154:155], v3 offset:4800\n"
"ds_read_b64_tr_b16 a[156:157], v4 offset:3840\n"
"ds_read_b64_tr_b16 a[158:159], v4 offset:4800\n"
"s_waitcnt lgkmcnt(12)\n"
"v_mfma_f32_32x32x16_bf16 v[160:175], v[16:19], a[8:11], v[160:175]\n"
"ds_read_b128 v[16:19], v1 offset:16384\n"
"s_add_u32 m0, s45, 36864\n"
"s_add_u32 s44, s45, 589824\n"
"buffer_load_dwordx4 v0, s[40:43], s44 offen offset:0 lds\n"
"ds_read_b64_tr_b16 a[160:161], v3 offset:7680\n"
"ds_read_b64_tr_b16 a[162:163], v3 offset:8640\n"
"ds_read_b64_tr_b16 a[164:165], v4 offset:7680\n"
"ds_read_b64_tr_b16 a[166:167], v4 offset:8640\n"
"v_mfma_f32_32x32x16_bf16 v[160:175], v[20:23], a[12:15], v[160:175]\n"
"ds_read_b128 v[20:23], v1 offset:17408\n"
"ds_read_b64_tr_b16 a[168:169], v3 offset:11520\n"
"ds_read_b64_tr_b16 a[170:171], v3 offset:12480\n"
"ds_read_b64_tr_b16 a[172:173], v4 offset:11520\n"
"ds_read_b64_tr_b16 a[174:175], v4 offset:12480\n"
"s_waitcnt lgkmcnt(15)\n"
"v_mfma_f32_32x32x16_bf16 v[160:175], v[24:27], a[16:19], v[160:175]\n"
"ds_read_b128 v[24:27], v1 offset:18432\n"
"v_mfma_f32_32x32x16_bf16 v[80:95], a[144:147], a[148:151], 0\n"
"ds_read_b64_tr_b16 a[176:177], v3 offset:15360\n"
"ds_read_b64_tr_b16 a[178:179], v3 offset:16320\n"
"ds_read_b64_tr_b16 a[180:181], v4 offset:15360\n"
"ds_read_b64_tr_b16 a[182:183], v4 offset:16320\n"
"v_mfma_f32_32x32x16_bf16 v[160:175], v[28:31], a[20:23], v[160:175]\n"
"ds_read_b128 v[28:31], v1 offset:19456\n"
"buffer_load_dwordx4 v0, s[40:43], s44 offen offset:1024 lds\n"
"s_waitcnt lgkmcnt(15)\n"
"v_mfma_f32_32x32x16_bf16 v[80:95], a[152:155], a[156:159], v[80:95]\n"
"ds_read_b64_tr_b16 a[184:185], v3 offset:19200\n"
"ds_read_b64_tr_b16 a[186:187], v3 offset:20160\n"
"ds_read_b64_tr_b16 a[188:189], v4 offset:19200\n"
"ds_read_b64_tr_b16 a[190:191], v4 offset:20160\n"
"v_mfma_f32_32x32x16_bf16 v[160:175], v[8:11], a[24:27], v[160:175]\n"
"ds_read_b128 v[8:11], v1 offset:20480\n"
"s_waitcnt lgkmcnt(15)\n"
"v_mfma_f32_32x32x16_bf16 v[80:95], a[160:163], a[164:167], v[80:95]\n"
"ds_read_b64_tr_b16 a[192:193], v3 offset:23040\n"
"ds_read_b64_tr_b16 a[194:195], v3 offset:24000\n"
"ds_read_b64_tr_b16 a[196:197], v4 offset:23040\n"
"ds_read_b64_tr_b16 a[198:199], v4 offset:24000\n"
"v_mfma_f32_32x32x16_bf16 v[160:175], v[12:15], a[28:31], v[160:175]\n"
"ds_read_b128 v[12:15], v1 offset:21504\n"
"s_waitcnt lgkmcnt(15)\n"
"v_mfma_f32_32x32x16_bf16 v[80:95], a[168:171], a[172:175], v[80:95]\n"
"ds_read_b64_tr_b16 a[200:201], v3 offset:26880\n"
"ds_read_b64_tr_b16 a[202:203], v3 offset:27840\n"
"ds_read_b64_tr_b16 a[204:205], v4 offset:26880\n"
"ds_read_b64_tr_b16 a[206:207], v4 offset:27840\n"
"v_mfma_f32_32x32x16_bf16 v[160:175], v[16:19], a[32:35], v[160:175]\n"
"ds_read_b128 v[16:19], v1 offset:22528\n"
"buffer_load_dwordx4 v0, s[40:43], s44 offen offset:2048 lds\n"
"s_waitcnt lgkmcnt(15)\n"
"v_mfma_f32_32x32x16_bf16 v[80:95], a[176:179], a[180:183], v[80:95]\n"
"v_max3_f32 v78, v96, v97, v98\n"
"v_max3_f32 v78, v78, v99, v100\n"
"v_max3_f32 v78, v78, v101, v102\n"
"v_max3_f32 v78, v78, v103, v104\n"
"v_max3_f32 v78, v78, v105, v106\n"
"v_max3_f32 v78, v78, v107, v108\n"
"v_max3_f32 v78, v78, v109, v110\n"
"v_max3_f32 v78, v78, v111, v112\n"
"v_mfma_f32_32x32x16_bf16 v[160:175], v[20:23], a[36:39], v[160:175]\n"
"ds_read_b128 v[20:23], v1 offset:23552\n"
"s_waitcnt lgkmcnt(12)\n"
"v_mfma_f32_32x32x16_bf16 v[80:95], a[184:187], a[188:191], v[80:95]\n"
"v_max_f32 v78, v78, v113\n"
"v_sub_f32 v60, v96, v78\n"
"v_sub_f32 v61, v97, v78\n"
"v_exp_f32 v60, v60\n"
"v_sub_f32 v62, v98, v78\n"
"v_exp_f32 v61, v61\n"
"v_mfma_f32_32x32x16_bf16 v[160:175], v[24:27], a[40:43], v[160:175]\n"
"ds_read_b128 v[24:27], v1 offset:24576\n"
"s_waitcnt lgkmcnt(8)\n"
"v_mfma_f32_32x32x16_bf16 v[80:95], a[192:195], a[196:199], v[80:95]\n"
"v_sub_f32 v63, v99, v78\n"
"v_exp_f32 v62, v62\n"
"v_sub_f32 v64, v100, v78\n"
"v_add_f32 v79, v60, v61\n"
"v_exp_f32 v63, v63\n"
"v_sub_f32 v65, v101, v78\n"
"v_mfma_f32_32x32x16_bf16 v[160:175], v[28:31], a[44:47], v[160:175]\n"
"ds_read_b128 v[28:31], v1 offset:25600\n"
"buffer_load_dwordx4 v0, s[40:43], s44 offen offset:3072 lds\n"
"s_waitcnt lgkmcnt(4)\n"
"v_mfma_f32_32x32x16_bf16 v[80:95], a[200:203], a[204:207], v[80:95]\n"
"v_add_f32 v79, v79, v62\n"
"v_exp_f32 v64, v64\n"
"v_sub_f32 v66, v102, v78\n"
"v_add_f32 v79, v79, v63\n"
"v_exp_f32 v65, v65\n"
"v_mfma_f32_32x32x16_bf16 v[160:175], v[8:11], a[48:51], v[160:175]\n"
"ds_read_b128 v[8:11], v1 offset:26624\n"
"v_sub_f32 v67, v103, v78\n"
"v_add_f32 v79, v79, v64\n"
"v_exp_f32 v66, v66\n"
"v_sub_f32 v68, v104, v78\n"
"v_add_f32 v79, v79, v65\n"
"v_exp_f32 v67, v67\n"
"v_mfma_f32_32x32x16_bf16 v[160:175], v[12:15], a[52:55], v[160:175]\n"
"ds_read_b128 v[12:15], v1 offset:27648\n"
"v_sub_f32 v69, v105, v78\n"
"v_add_f32 v79, v79, v66\n"
"v_exp_f32 v68, v68\n"
"v_sub_f32 v70, v106, v78\n"
"v_add_f32 v79, v79, v67\n"
"v_exp_f32 v69, v69\n"
"s_waitcnt lgkmcnt(4)\n"
"v_mfma_f32_32x32x16_bf16 v[160:175], v[16:19], a[56:59], v[160:175]\n"
"ds_read_b128 v[16:19], v1 offset:28672\n"
"s_add_u32 m0, s45, 40960\n"
"s_add_u32 s44, s45, 593920\n"
"buffer_load_dwordx4 v0, s[40:43], s44 offen offset:0 lds\n"
"v_sub_f32 v71, v107, v78\n"
"v_add_f32 v79, v79, v68\n"
"v_exp_f32 v70, v70\n"
"v_sub_f32 v72, v108, v78\n"
"v_add_f32 v79, v79, v69\n"
"v_exp_f32 v71, v71\n"
"v_mfma_f32_32x32x16_bf16 v[160:175], v[20:23], a[60:63], v[160:175]\n"
"ds_read_b128 v[20:23], v1 offset:29696\n"
"v_sub_f32 v73, v109, v78\n"
"v_add_f32 v79, v79, v70\n"
"v_exp_f32 v72, v72\n"
"v_sub_f32 v74, v110, v78\n"
"v_add_f32 v79, v79, v71\n"
"v_exp_f32 v73, v73\n"
"s_waitcnt lgkmcnt(4)\n"
"v_mfma_f32_32x32x16_bf16 v[160:175], v[24:27], a[64:67], v[160:175]\n"
"ds_read_b128 v[24:27], v1 offset:30720\n"
"v_sub_f32 v75, v111, v78\n"
"v_add_f32 v79, v79, v72\n"
"v_exp_f32 v74, v74\n"
"v_sub_f32 v76, v112, v78\n"
"v_add_f32 v79, v79, v73\n"
"v_exp_f32 v75, v75\n"
"v_mfma_f32_32x32x16_bf16 v[160:175], v[28:31], a[68:71], v[160:175]\n"
"ds_read_b128 v[28:31], v1 offset:31744\n"
"buffer_load_dwordx4 v0, s[40:43], s44 offen offset:1024 lds\n"
"v_sub_f32 v77, v113, v78\n"
"v_add_f32 v79, v79, v74\n"
"v_exp_f32 v76, v76\n"
"v_add_f32 v79, v79, v75\n"
"v_exp_f32 v77, v77\n"
"v_add_f32 v79, v79, v76\n"
"s_waitcnt lgkmcnt(4)\n"
"v_mfma_f32_32x32x16_bf16 v[160:175], v[8:11], a[72:75], v[160:175]\n"
"ds_read_b128 v[8:11], v1 offset:32768\n"
"s_mov_b64 exec, s[48:49]\n"
"global_atomic_add_f32 v5, v80, s[46:47] offset:0\n"
"global_atomic_add_f32 v5, v81, s[46:47] offset:160\n"
"global_atomic_add_f32 v5, v82, s[46:47] offset:320\n"
"global_atomic_add_f32 v5, v83, s[46:47] offset:480\n"
"s_mov_b64 exec, -1\n"
"v_add_f32 v79, v79, v77\n"
"v_rcp_f32 v79, v79\n"
"s_nop 0\n"
"v_mul_f32 v60, v60, v79\n"
"v_mul_f32 v61, v61, v79\n"
"v_mul_f32 v62, v62, v79\n"
"v_mfma_f32_32x32x16_bf16 v[160:175], v[12:15], a[76:79], v[160:175]\n"
"ds_read_b128 v[12:15], v1 offset:33792\n"
"v_mul_f32 v63, v63, v79\n"
"v_cvt_pk_bf16_f32 v176, v60, v61\n"
"v_mul_f32 v64, v64, v79\n"
"v_mul_f32 v65, v65, v79\n"
"v_cvt_pk_bf16_f32 v177, v62, v63\n"
"v_mul_f32 v66, v66, v79\n"
"v_mul_f32 v67, v67, v79\n"
"v_cvt_pk_bf16_f32 v178, v64, v65\n"
"s_waitcnt lgkmcnt(4)\n"
"v_mfma_f32_32x32x16_bf16 v[160:175], v[16:19], a[80:83], v[160:175]\n"
"ds_read_b128 v[16:19], v1 offset:34816\n"
"buffer_load_dwordx4 v0, s[40:43], s44 offen offset:2048 lds\n"
"v_mul_f32 v68, v68, v79\n"
"v_mul_f32 v69, v69, v79\n"
"v_cvt_pk_bf16_f32 v179, v66, v67\n"
"v_mul_f32 v70, v70, v79\n"
"v_mul_f32 v71, v71, v79\n"
"v_cvt_pk_bf16_f32 v180, v68, v69\n"
"v_mul_f32 v72, v72, v79\n"
"v_mul_f32 v73, v73, v79\n"
"v_mfma_f32_32x32x16_bf16 v[160:175], v[20:23], a[84:87], v[160:175]\n"
"ds_read_b128 v[20:23], v1 offset:35840\n"
"s_mov_b64 exec, s[48:49]\n"
"global_atomic_add_f32 v5, v84, s[46:47] offset:1280\n"
"global_atomic_add_f32 v5, v85, s[46:47] offset:1440\n"
"global_atomic_add_f32 v5, v86, s[46:47] offset:1600\n"
"global_atomic_add_f32 v5, v87, s[46:47] offset:1760\n"
"s_mov_b64 exec, -1\n"
"v_cvt_pk_bf16_f32 v181, v70, v71\n"
"v_mul_f32 v74, v74, v79\n"
"v_mul_f32 v75, v75, v79\n"
"v_cvt_pk_bf16_f32 v182, v72, v73\n"
"v_mul_f32 v76, v76, v79\n"
"v_mul_f32 v77, v77, v79\n"
"v_cvt_pk_bf16_f32 v183, v74, v75\n"
"v_cvt_pk_bf16_f32 v184, v76, v77\n"
"s_waitcnt lgkmcnt(4)\n"
"v_mfma_f32_32x32x16_bf16 v[160:175], v[24:27], a[88:91], v[160:175]\n"
"ds_read_b128 v[24:27], v1 offset:36864\n"
"v_min3_f32 v78, v96, v97, v98\n"
"v_min3_f32 v78, v78, v99, v100\n"
"v_min3_f32 v78, v78, v101, v102\n"
"v_min3_f32 v78, v78, v103, v104\n"
"v_min3_f32 v78, v78, v105, v106\n"
"v_min3_f32 v78, v78, v107, v108\n"
"v_min3_f32 v78, v78, v109, v110\n"
"v_min3_f32 v78, v78, v111, v112\n"
"v_mfma_f32_32x32x16_bf16 v[160:175], v[28:31], a[92:95], v[160:175]\n"
"ds_read_b128 v[28:31], v1 offset:37888\n"
"buffer_load_dwordx4 v0, s[40:43], s44 offen offset:3072 lds\n"
"v_min_f32 v78, v78, v113\n"
"v_sub_f32 v60, v78, v96\n"
"v_sub_f32 v61, v78, v97\n"
"v_exp_f32 v60, v60\n"
"v_sub_f32 v62, v78, v98\n"
"v_exp_f32 v61, v61\n"
"s_waitcnt lgkmcnt(4)\n"
"v_mfma_f32_32x32x16_bf16 v[160:175], v[8:11], a[96:99], v[160:175]\n"
"ds_read_b128 v[8:11], v1 offset:38912\n"
"s_mov_b64 exec, s[48:49]\n"
"s_mov_b32 exec_hi, 0\n"
"global_atomic_add_f32 v5, v88, s[46:47] offset:2560\n"
"global_atomic_add_f32 v5, v89, s[46:47] offset:2720\n"
"global_atomic_add_f32 v5, v90, s[46:47] offset:2880\n"
"s_mov_b64 exec, -1\n"
"s_add_u32 s46, s46, 3040\n"
"s_addc_u32 s47, s47, 0\n"
"v_sub_f32 v63, v78, v99\n"
"v_exp_f32 v62, v62\n"
"v_sub_f32 v64, v78, v100\n"
"v_add_f32 v79, v60, v61\n"
"v_exp_f32 v63, v63\n"
"v_sub_f32 v65, v78, v101\n"
"v_mfma_f32_32x32x16_bf16 v[160:175], v[12:15], a[100:103], v[160:175]\n"
"ds_read_b128 v[12:15], v1 offset:39936\n"
"v_add_f32 v79, v79, v62\n"
"v_exp_f32 v64, v64\n"
"v_sub_f32 v66, v78, v102\n"
"v_add_f32 v79, v79, v63\n"
"v_exp_f32 v65, v65\n"
"s_waitcnt lgkmcnt(4)\n"
"v_mfma_f32_32x32x16_bf16 v[160:175], v[16:19], a[104:107], v[160:175]\n"
"ds_read_b128 v[16:19], v1 offset:40960\n"
"s_add_u32 m0, s45, 45056\n"
"s_add_u32 s44, s45, 598016\n"
"buffer_load_dwordx4 v0, s[40:43], s44 offen offset:0 lds\n"
"v_sub_f32 v67, v78, v103\n"
"v_add_f32 v79, v79, v64\n"
"v_exp_f32 v66, v66\n"
"v_sub_f32 v68, v78, v104\n"
"v_add_f32 v79, v79, v65\n"
"v_exp_f32 v67, v67\n"
"v_mfma_f32_32x32x16_bf16 v[160:175], v[20:23], a[108:111], v[160:175]\n"
"ds_read_b128 v[20:23], v1 offset:41984\n"
"v_sub_f32 v69, v78, v105\n"
"v_add_f32 v79, v79, v66\n"
"v_exp_f32 v68, v68\n"
"v_sub_f32 v70, v78, v106\n"
"v_add_f32 v79, v79, v67\n"
"v_exp_f32 v69, v69\n"
"s_waitcnt lgkmcnt(4)\n"
"v_mfma_f32_32x32x16_bf16 v[160:175], v[24:27], a[112:115], v[160:175]\n"
"ds_read_b128 v[24:27], v1 offset:43008\n"
"v_sub_f32 v71, v78, v107\n"
"v_add_f32 v79, v79, v68\n"
"v_exp_f32 v70, v70\n"
"v_sub_f32 v72, v78, v108\n"
"v_add_f32 v79, v79, v69\n"
"v_exp_f32 v71, v71\n"
"v_mfma_f32_32x32x16_bf16 v[160:175], v[28:31], a[116:119], v[160:175]\n"
"ds_read_b128 v[28:31], v1 offset:44032\n"
"v_sub_f32 v73, v78, v109\n"
"v_add_f32 v79, v79, v70\n"
"v_exp_f32 v72, v72\n"
"v_sub_f32 v74, v78, v110\n"
"v_add_f32 v79, v79, v71\n"
"v_exp_f32 v73, v73\n"
"s_waitcnt lgkmcnt(4)\n"
"v_mfma_f32_32x32x16_bf16 v[160:175], v[8:11], a[120:123], v[160:175]\n"
"s_waitcnt vmcnt(20)\n"
"s_barrier\n"
"ds_read_b128 v[8:11], v0 offset:0\n"
"v_sub_f32 v75, v78, v111\n"
"v_add_f32 v79, v79, v72\n"
"v_exp_f32 v74, v74\n"
"v_sub_f32 v76, v78, v112\n"
"v_add_f32 v79, v79, v73\n"
"v_exp_f32 v75, v75\n"
"v_mfma_f32_32x32x16_bf16 v[160:175], v[12:15], a[124:127], v[160:175]\n"
"ds_read_b128 v[12:15], v0 offset:1024\n"
"v_sub_f32 v77, v78, v113\n"
"v_add_f32 v79, v79, v74\n"
"v_exp_f32 v76, v76\n"
"v_add_f32 v79, v79, v75\n"
"v_exp_f32 v77, v77\n"
"v_add_f32 v79, v79, v76\n"
"s_waitcnt lgkmcnt(4)\n"
"v_mfma_f32_32x32x16_bf16 v[160:175], v[16:19], a[128:131], v[160:175]\n"
"ds_read_b128 v[16:19], v0 offset:2048\n"
"v_add_f32 v79, v79, v77\n"
"v_rcp_f32 v79, v79\n"
"s_nop 0\n"
"v_mul_f32 v60, v60, v79\n"
"v_mul_f32 v61, v61, v79\n"
"v_mul_f32 v62, v62, v79\n"
"v_mfma_f32_32x32x16_bf16 v[160:175], v[20:23], a[132:135], v[160:175]\n"
"ds_read_b128 v[20:23], v0 offset:3072\n"
"v_mul_f32 v63, v63, v79\n"
"v_cvt_pk_bf16_f32 v185, v60, v61\n"
"v_mul_f32 v64, v64, v79\n"
"v_mul_f32 v65, v65, v79\n"
"v_cvt_pk_bf16_f32 v186, v62, v63\n"
"v_mul_f32 v66, v66, v79\n"
"v_mul_f32 v67, v67, v79\n"
"v_cvt_pk_bf16_f32 v187, v64, v65\n"
"s_waitcnt lgkmcnt(4)\n"
"v_mfma_f32_32x32x16_bf16 v[160:175], v[24:27], a[136:139], v[160:175]\n"
"ds_read_b128 v[24:27], v0 offset:4096\n"
"v_mul_f32 v68, v68, v79\n"
"v_mul_f32 v69, v69, v79\n"
"v_cvt_pk_bf16_f32 v188, v66, v67\n"
"v_mul_f32 v70, v70, v79\n"
"v_mul_f32 v71, v71, v79\n"
"v_cvt_pk_bf16_f32 v189, v68, v69\n"
"v_mul_f32 v72, v72, v79\n"
"v_mul_f32 v73, v73, v79\n"
"v_mfma_f32_32x32x16_bf16 v[160:175], v[28:31], a[140:143], v[160:175]\n"
"ds_read_b128 v[28:31], v0 offset:5120\n"
"v_cvt_pk_bf16_f32 v190, v70, v71\n"
"v_mul_f32 v74, v74, v79\n"
"v_mul_f32 v75, v75, v79\n"
"v_cvt_pk_bf16_f32 v191, v72, v73\n"
"v_mul_f32 v76, v76, v79\n"
"v_mul_f32 v77, v77, v79\n"
"v_cvt_pk_bf16_f32 v192, v74, v75\n"
"v_cvt_pk_bf16_f32 v193, v76, v77\n"
"s_waitcnt vmcnt(1)\n"
"s_barrier\n"
"s_cmp_lg_u32 %10, 0\n"
"s_cbranch_scc1 NOPUB_%=\n"
"v_mov_b32 v7, 0\n"
"v_mov_b32 v60, 1\n"
"s_mov_b64 exec, 1\n"
"global_atomic_add v7, v60, %8 sc1\n"
"s_mov_b64 exec, -1\n"
"s_nop 1\n"
"NOPUB_%=:\n"
"v_max3_f32 v78, v114, v115, v116\n"
"v_max3_f32 v78, v78, v117, v118\n"
"v_max3_f32 v78, v78, v119, v120\n"
"v_max3_f32 v78, v78, v121, v122\n"
"v_max3_f32 v78, v78, v123, v124\n"
"v_max3_f32 v78, v78, v125, v126\n"
"v_max3_f32 v78, v78, v127, v128\n"
"v_max3_f32 v78, v78, v129, v130\n"
"v_max_f32 v78, v78, v131\n"
"v_sub_f32 v60, v114, v78\n"
"v_sub_f32 v61, v115, v78\n"
"v_exp_f32 v60, v60\n"
"v_sub_f32 v62, v116, v78\n"
"v_exp_f32 v61, v61\n"
"v_sub_f32 v63, v117, v78\n"
"v_exp_f32 v62, v62\n"
"s_add_u32 m0, s45, 73728\n"
"s_add_u32 s44, s45, 626688\n"
"buffer_load_dwordx4 v0, s[40:43], s44 offen offset:0 lds\n"
"v_sub_f32 v64, v118, v78\n"
"v_add_f32 v79, v60, v61\n"
"v_exp_f32 v63, v63\n"
"v_sub_f32 v65, v119, v78\n"
"v_add_f32 v79, v79, v62\n"
"v_exp_f32 v64, v64\n"
"v_sub_f32 v66, v120, v78\n"
"v_add_f32 v79, v79, v63\n"
"v_exp_f32 v65, v65\n"
"v_sub_f32 v67, v121, v78\n"
"v_add_f32 v79, v79, v64\n"
"v_exp_f32 v66, v66\n"
"v_sub_f32 v68, v122, v78\n"
"v_add_f32 v79, v79, v65\n"
"v_exp_f32 v67, v67\n"
"v_sub_f32 v69, v123, v78\n"
"buffer_load_dwordx4 v0, s[40:43], s44 offen offset:1024 lds\n"
"v_add_f32 v79, v79, v66\n"
"v_exp_f32 v68, v68\n"
"v_sub_f32 v70, v124, v78\n"
"v_add_f32 v79, v79, v67\n"
"v_exp_f32 v69, v69\n"
"v_sub_f32 v71, v125, v78\n"
"v_add_f32 v79, v79, v68\n"
"v_exp_f32 v70, v70\n"
"v_sub_f32 v72, v126, v78\n"
"v_add_f32 v79, v79, v69\n"
"v_exp_f32 v71, v71\n"
"v_sub_f32 v73, v127, v78\n"
"v_add_f32 v79, v79, v70\n"
"v_exp_f32 v72, v72\n"
"v_sub_f32 v74, v128, v78\n"
"v_add_f32 v79, v79, v71\n"
"buffer_load_dwordx4 v0, s[40:43], s44 offen offset:2048 lds\n"
"v_exp_f32 v73, v73\n"
"v_sub_f32 v75, v129, v78\n"
"v_add_f32 v79, v79, v72\n"
"v_exp_f32 v74, v74\n"
"v_sub_f32 v76, v130, v78\n"
"v_add_f32 v79, v79, v73\n"
"v_exp_f32 v75, v75\n"
"v_sub_f32 v77, v131, v78\n"
"v_add_f32 v79, v79, v74\n"
"v_exp_f32 v76, v76\n"
"v_add_f32 v79, v79, v75\n"
"v_exp_f32 v77, v77\n"
"v_add_f32 v79, v79, v76\n"
"v_add_f32 v79, v79, v77\n"
"v_rcp_f32 v79, v79\n"
"s_nop 0\n"
"buffer_load_dwordx4 v0, s[40:43], s44 offen offset:3072 lds\n"
"v_mul_f32 v60, v60, v79\n"
"v_mul_f32 v61, v61, v79\n"
"v_mul_f32 v62, v62, v79\n"
"v_mul_f32 v63, v63, v79\n"
"v_cvt_pk_bf16_f32 v196, v60, v61\n"
"v_mul_f32 v64, v64, v79\n"
"v_mul_f32 v65, v65, v79\n"
"v_cvt_pk_bf16_f32 v197, v62, v63\n"
"v_mul_f32 v66, v66, v79\n"
"v_mul_f32 v67, v67, v79\n"
"v_cvt_pk_bf16_f32 v198, v64, v65\n"
"v_mul_f32 v68, v68, v79\n"
"v_mul_f32 v69, v69, v79\n"
"v_cvt_pk_bf16_f32 v199, v66, v67\n"
"v_mul_f32 v70, v70, v79\n"
"v_mul_f32 v71, v71, v79\n"
"s_add_u32 m0, s45, 77824\n"
"s_add_u32 s44, s45, 630784\n"
"buffer_load_dwordx4 v0, s[40:43], s44 offen offset:0 lds\n"
"v_cvt_pk_bf16_f32 v200, v68, v69\n"
"v_mul_f32 v72, v72, v79\n"
"v_mul_f32 v73, v73, v79\n"
"v_cvt_pk_bf16_f32 v201, v70, v71\n"
"v_mul_f32 v74, v74, v79\n"
"v_mul_f32 v75, v75, v79\n"
"v_cvt_pk_bf16_f32 v202, v72, v73\n"
"v_mul_f32 v76, v76, v79\n"
"v_mul_f32 v77, v77, v79\n"
"v_cvt_pk_bf16_f32 v203, v74, v75\n"
"v_cvt_pk_bf16_f32 v204, v76, v77\n"
"v_min3_f32 v78, v114, v115, v116\n"
"v_min3_f32 v78, v78, v117, v118\n"
"v_min3_f32 v78, v78, v119, v120\n"
"v_min3_f32 v78, v78, v121, v122\n"
"v_min3_f32 v78, v78, v123, v124\n"
"buffer_load_dwordx4 v0, s[40:43], s44 offen offset:1024 lds\n"
"v_min3_f32 v78, v78, v125, v126\n"
"v_min3_f32 v78, v78, v127, v128\n"
"v_min3_f32 v78, v78, v129, v130\n"
"v_min_f32 v78, v78, v131\n"
"v_sub_f32 v60, v78, v114\n"
"v_sub_f32 v61, v78, v115\n"
"v_exp_f32 v60, v60\n"
"v_sub_f32 v62, v78, v116\n"
"v_exp_f32 v61, v61\n"
"v_sub_f32 v63, v78, v117\n"
"v_exp_f32 v62, v62\n"
"v_sub_f32 v64, v78, v118\n"
"v_add_f32 v79, v60, v61\n"
"v_exp_f32 v63, v63\n"
"v_sub_f32 v65, v78, v119\n"
"v_add_f32 v79, v79, v62\n"
"buffer_load_dwordx4 v0, s[40:43], s44 offen offset:2048 lds\n"
"v_exp_f32 v64, v64\n"
"v_sub_f32 v66, v78, v120\n"
"v_add_f32 v79, v79, v63\n"
"v_exp_f32 v65, v65\n"
"v_sub_f32 v67, v78, v121\n"
"v_add_f32 v79, v79, v64\n"
"v_exp_f32 v66, v66\n"
"v_sub_f32 v68, v78, v122\n"
"v_add_f32 v79, v79, v65\n"
"v_exp_f32 v67, v67\n"
"v_sub_f32 v69, v78, v123\n"
"v_add_f32 v79, v79, v66\n"
"v_exp_f32 v68, v68\n"
"v_sub_f32 v70, v78, v124\n"
"v_add_f32 v79, v79, v67\n"
"v_exp_f32 v69, v69\n"
"buffer_load_dwordx4 v0, s[40:43], s44 offen offset:3072 lds\n"
"v_sub_f32 v71, v78, v125\n"
"v_add_f32 v79, v79, v68\n"
"v_exp_f32 v70, v70\n"
"v_sub_f32 v72, v78, v126\n"
"v_add_f32 v79, v79, v69\n"
"v_exp_f32 v71, v71\n"
"v_sub_f32 v73, v78, v127\n"
"v_add_f32 v79, v79, v70\n"
"v_exp_f32 v72, v72\n"
"v_sub_f32 v74, v78, v128\n"
"v_add_f32 v79, v79, v71\n"
"v_exp_f32 v73, v73\n"
"v_sub_f32 v75, v78, v129\n"
"v_add_f32 v79, v79, v72\n"
"v_exp_f32 v74, v74\n"
"v_sub_f32 v76, v78, v130\n"
"s_add_u32 m0, s45, 81920\n"
"s_add_u32 s44, s45, 634880\n"
"buffer_load_dwordx4 v0, s[40:43], s44 offen offset:0 lds\n"
"v_add_f32 v79, v79, v73\n"
"v_exp_f32 v75, v75\n"
"v_sub_f32 v77, v78, v131\n"
"v_add_f32 v79, v79, v74\n"
"v_exp_f32 v76, v76\n"
"v_add_f32 v79, v79, v75\n"
"v_exp_f32 v77, v77\n"
"v_add_f32 v79, v79, v76\n"
"v_add_f32 v79, v79, v77\n"
"v_rcp_f32 v79, v79\n"
"s_nop 0\n"
"v_mul_f32 v60, v60, v79\n"
"v_mul_f32 v61, v61, v79\n"
"v_mul_f32 v62, v62, v79\n"
"v_mul_f32 v63, v63, v79\n"
"v_cvt_pk_bf16_f32 v205, v60, v61\n"
"v_mul_f32 v64, v64, v79\n"
"v_mul_f32 v65, v65, v79\n"
"v_cvt_pk_bf16_f32 v206, v62, v63\n"
"v_mul_f32 v66, v66, v79\n"
"v_mul_f32 v67, v67, v79\n"
"v_cvt_pk_bf16_f32 v207, v64, v65\n"
"v_mul_f32 v68, v68, v79\n"
"v_mul_f32 v69, v69, v79\n"
"v_cvt_pk_bf16_f32 v208, v66, v67\n"
"v_mul_f32 v70, v70, v79\n"
"v_mul_f32 v71, v71, v79\n"
"v_cvt_pk_bf16_f32 v209, v68, v69\n"
"v_mul_f32 v72, v72, v79\n"
"v_mul_f32 v73, v73, v79\n"
"v_cvt_pk_bf16_f32 v210, v70, v71\n"
"v_mul_f32 v74, v74, v79\n"
"v_mul_f32 v75, v75, v79\n"
"v_cvt_pk_bf16_f32 v211, v72, v73\n"
"v_mul_f32 v76, v76, v79\n"
"v_mul_f32 v77, v77, v79\n"
"v_cvt_pk_bf16_f32 v212, v74, v75\n"
"v_cvt_pk_bf16_f32 v213, v76, v77\n"
"v_max3_f32 v78, v132, v133, v134\n"
"v_max3_f32 v78, v78, v135, v136\n"
"v_max3_f32 v78, v78, v137, v138\n"
"v_max3_f32 v78, v78, v139, v140\n"
"v_max3_f32 v78, v78, v141, v142\n"
"v_max3_f32 v78, v78, v143, v144\n"
"v_max3_f32 v78, v78, v145, v146\n"
"v_max3_f32 v78, v78, v147, v148\n"
"v_max_f32 v78, v78, v149\n"
"v_sub_f32 v60, v132, v78\n"
"v_sub_f32 v61, v133, v78\n"
"v_exp_f32 v60, v60\n"
"v_sub_f32 v62, v134, v78\n"
"v_exp_f32 v61, v61\n"
"v_sub_f32 v63, v135, v78\n"
"v_exp_f32 v62, v62\n"
"v_sub_f32 v64, v136, v78\n"
"v_add_f32 v79, v60, v61\n"
"v_exp_f32 v63, v63\n"
"v_sub_f32 v65, v137, v78\n"
"v_add_f32 v79, v79, v62\n"
"v_exp_f32 v64, v64\n"
"v_sub_f32 v66, v138, v78\n"
"v_add_f32 v79, v79, v63\n"
"v_exp_f32 v65, v65\n"
"v_sub_f32 v67, v139, v78\n"
"v_add_f32 v79, v79, v64\n"
"v_exp_f32 v66, v66\n"
"v_sub_f32 v68, v140, v78\n"
"v_add_f32 v79, v79, v65\n"
"v_exp_f32 v67, v67\n"
"v_sub_f32 v69, v141, v78\n"
"v_add_f32 v79, v79, v66\n"
"v_exp_f32 v68, v68\n"
"v_sub_f32 v70, v142, v78\n"
"v_add_f32 v79, v79, v67\n"
"v_exp_f32 v69, v69\n"
"v_sub_f32 v71, v143, v78\n"
"v_add_f32 v79, v79, v68\n"
"v_exp_f32 v70, v70\n"
"v_sub_f32 v72, v144, v78\n"
"v_add_f32 v79, v79, v69\n"
"v_exp_f32 v71, v71\n"
"v_sub_f32 v73, v145, v78\n"
"v_add_f32 v79, v79, v70\n"
"v_exp_f32 v72, v72\n"
"v_sub_f32 v74, v146, v78\n"
"v_add_f32 v79, v79, v71\n"
"v_exp_f32 v73, v73\n"
"v_sub_f32 v75, v147, v78\n"
"v_add_f32 v79, v79, v72\n"
"v_exp_f32 v74, v74\n"
"v_sub_f32 v76, v148, v78\n"
"v_add_f32 v79, v79, v73\n"
"v_exp_f32 v75, v75\n"
"v_sub_f32 v77, v149, v78\n"
"v_add_f32 v79, v79, v74\n"
"v_exp_f32 v76, v76\n"
"v_add_f32 v79, v79, v75\n"
"v_exp_f32 v77, v77\n"
"v_add_f32 v79, v79, v76\n"
"v_add_f32 v79, v79, v77\n"
"v_rcp_f32 v79, v79\n"
"s_nop 0\n"
"v_mul_f32 v60, v60, v79\n"
"v_mul_f32 v61, v61, v79\n"
"v_mul_f32 v62, v62, v79\n"
"v_mul_f32 v63, v63, v79\n"
"v_cvt_pk_bf16_f32 v216, v60, v61\n"
"v_mul_f32 v64, v64, v79\n"
"v_mul_f32 v65, v65, v79\n"
"v_cvt_pk_bf16_f32 v217, v62, v63\n"
"v_mul_f32 v66, v66, v79\n"
"v_mul_f32 v67, v67, v79\n"
"v_cvt_pk_bf16_f32 v218, v64, v65\n"
"v_mul_f32 v68, v68, v79\n"
"v_mul_f32 v69, v69, v79\n"
"v_cvt_pk_bf16_f32 v219, v66, v67\n"
"v_mul_f32 v70, v70, v79\n"
"v_mul_f32 v71, v71, v79\n"
"v_cvt_pk_bf16_f32 v220, v68, v69\n"
"v_mul_f32 v72, v72, v79\n"
"v_mul_f32 v73, v73, v79\n"
"v_cvt_pk_bf16_f32 v221, v70, v71\n"
"v_mul_f32 v74, v74, v79\n"
"v_mul_f32 v75, v75, v79\n"
"v_cvt_pk_bf16_f32 v222, v72, v73\n"
"v_mul_f32 v76, v76, v79\n"
"v_mul_f32 v77, v77, v79\n"
"v_cvt_pk_bf16_f32 v223, v74, v75\n"
"v_cvt_pk_bf16_f32 v224, v76, v77\n"
"v_min3_f32 v78, v132, v133, v134\n"
"v_min3_f32 v78, v78, v135, v136\n"
"v_min3_f32 v78, v78, v137, v138\n"
"v_min3_f32 v78, v78, v139, v140\n"
"v_min3_f32 v78, v78, v141, v142\n"
"v_min3_f32 v78, v78, v143, v144\n"
"v_min3_f32 v78, v78, v145, v146\n"
"v_min3_f32 v78, v78, v147, v148\n"
"v_min_f32 v78, v78, v149\n"
"v_sub_f32 v60, v78, v132\n"
"v_sub_f32 v61, v78, v133\n"
"v_exp_f32 v60, v60\n"
"v_sub_f32 v62, v78, v134\n"
"v_exp_f32 v61, v61\n"
"v_sub_f32 v63, v78, v135\n"
"v_exp_f32 v62, v62\n"
"v_sub_f32 v64, v78, v136\n"
"v_add_f32 v79, v60, v61\n"
"v_exp_f32 v63, v63\n"
"v_sub_f32 v65, v78, v137\n"
"v_add_f32 v79, v79, v62\n"
"v_exp_f32 v64, v64\n"
"v_sub_f32 v66, v78, v138\n"
"v_add_f32 v79, v79, v63\n"
"v_exp_f32 v65, v65\n"
"v_sub_f32 v67, v78, v139\n"
"v_add_f32 v79, v79, v64\n"
"v_exp_f32 v66, v66\n"
"v_sub_f32 v68, v78, v140\n"
"v_add_f32 v79, v79, v65\n"
"v_exp_f32 v67, v67\n"
"v_sub_f32 v69, v78, v141\n"
"v_add_f32 v79, v79, v66\n"
"v_exp_f32 v68, v68\n"
"v_sub_f32 v70, v78, v142\n"
"v_add_f32 v79, v79, v67\n"
"v_exp_f32 v69, v69\n"
"v_sub_f32 v71, v78, v143\n"
"v_add_f32 v79, v79, v68\n"
"v_exp_f32 v70, v70\n"
"v_sub_f32 v72, v78, v144\n"
"v_add_f32 v79, v79, v69\n"
"v_exp_f32 v71, v71\n"
"v_sub_f32 v73, v78, v145\n"
"v_add_f32 v79, v79, v70\n"
"v_exp_f32 v72, v72\n"
"v_sub_f32 v74, v78, v146\n"
"v_add_f32 v79, v79, v71\n"
"v_exp_f32 v73, v73\n"
"v_sub_f32 v75, v78, v147\n"
"v_add_f32 v79, v79, v72\n"
"v_exp_f32 v74, v74\n"
"v_sub_f32 v76, v78, v148\n"
"v_add_f32 v79, v79, v73\n"
"v_exp_f32 v75, v75\n"
"v_sub_f32 v77, v78, v149\n"
"v_add_f32 v79, v79, v74\n"
"v_exp_f32 v76, v76\n"
"v_add_f32 v79, v79, v75\n"
"v_exp_f32 v77, v77\n"
"v_add_f32 v79, v79, v76\n"
"v_add_f32 v79, v79, v77\n"
"v_rcp_f32 v79, v79\n"
"s_nop 0\n"
"v_mul_f32 v60, v60, v79\n"
"v_mul_f32 v61, v61, v79\n"
"v_mul_f32 v62, v62, v79\n"
"v_mul_f32 v63, v63, v79\n"
"v_cvt_pk_bf16_f32 v225, v60, v61\n"
"v_mul_f32 v64, v64, v79\n"
"v_mul_f32 v65, v65, v79\n"
"v_cvt_pk_bf16_f32 v226, v62, v63\n"
"v_mul_f32 v66, v66, v79\n"
"v_mul_f32 v67, v67, v79\n"
"v_cvt_pk_bf16_f32 v227, v64, v65\n"
"v_mul_f32 v68, v68, v79\n"
"v_mul_f32 v69, v69, v79\n"
"v_cvt_pk_bf16_f32 v228, v66, v67\n"
"v_mul_f32 v70, v70, v79\n"
"v_mul_f32 v71, v71, v79\n"
"v_cvt_pk_bf16_f32 v229, v68, v69\n"
"v_mul_f32 v72, v72, v79\n"
"v_mul_f32 v73, v73, v79\n"
"v_cvt_pk_bf16_f32 v230, v70, v71\n"
"v_mul_f32 v74, v74, v79\n"
"v_mul_f32 v75, v75, v79\n"
"v_cvt_pk_bf16_f32 v231, v72, v73\n"
"v_mul_f32 v76, v76, v79\n"
"v_mul_f32 v77, v77, v79\n"
"v_cvt_pk_bf16_f32 v232, v74, v75\n"
"v_cvt_pk_bf16_f32 v233, v76, v77\n"
"v_max3_f32 v78, v150, v151, v152\n"
"v_max3_f32 v78, v78, v153, v154\n"
"v_max3_f32 v78, v78, v155, v156\n"
"v_max3_f32 v78, v78, v157, v158\n"
"v_max3_f32 v78, v78, v159, v160\n"
"v_max3_f32 v78, v78, v161, v162\n"
"v_max3_f32 v78, v78, v163, v164\n"
"v_max3_f32 v78, v78, v165, v166\n"
"v_max_f32 v78, v78, v167\n"
"v_sub_f32 v60, v150, v78\n"
"v_sub_f32 v61, v151, v78\n"
"v_exp_f32 v60, v60\n"
"v_sub_f32 v62, v152, v78\n"
"v_exp_f32 v61, v61\n"
"v_sub_f32 v63, v153, v78\n"
"v_exp_f32 v62, v62\n"
"v_sub_f32 v64, v154, v78\n"
"v_add_f32 v79, v60, v61\n"
"v_exp_f32 v63, v63\n"
"v_sub_f32 v65, v155, v78\n"
"v_add_f32 v79, v79, v62\n"
"v_exp_f32 v64, v64\n"
"v_sub_f32 v66, v156, v78\n"
"v_add_f32 v79, v79, v63\n"
"v_exp_f32 v65, v65\n"
"v_sub_f32 v67, v157, v78\n"
"v_add_f32 v79, v79, v64\n"
"v_exp_f32 v66, v66\n"
"v_sub_f32 v68, v158, v78\n"
"v_add_f32 v79, v79, v65\n"
"v_exp_f32 v67, v67\n"
"v_sub_f32 v69, v159, v78\n"
"v_add_f32 v79, v79, v66\n"
"v_exp_f32 v68, v68\n"
"v_sub_f32 v70, v160, v78\n"
"v_add_f32 v79, v79, v67\n"
"v_exp_f32 v69, v69\n"
"v_sub_f32 v71, v161, v78\n"
"v_add_f32 v79, v79, v68\n"
"v_exp_f32 v70, v70\n"
"v_sub_f32 v72, v162, v78\n"
"v_add_f32 v79, v79, v69\n"
"v_exp_f32 v71, v71\n"
"v_sub_f32 v73, v163, v78\n"
"v_add_f32 v79, v79, v70\n"
"v_exp_f32 v72, v72\n"
"v_sub_f32 v74, v164, v78\n"
"v_add_f32 v79, v79, v71\n"
"v_exp_f32 v73, v73\n"
"v_sub_f32 v75, v165, v78\n"
"v_add_f32 v79, v79, v72\n"
"v_exp_f32 v74, v74\n"
"v_sub_f32 v76, v166, v78\n"
"v_add_f32 v79, v79, v73\n"
"v_exp_f32 v75, v75\n"
"v_sub_f32 v77, v167, v78\n"
"v_add_f32 v79, v79, v74\n"
"v_exp_f32 v76, v76\n"
"v_add_f32 v79, v79, v75\n"
"v_exp_f32 v77, v77\n"
"v_add_f32 v79, v79, v76\n"
"v_add_f32 v79, v79, v77\n"
"v_rcp_f32 v79, v79\n"
"s_nop 0\n"
"v_mul_f32 v60, v60, v79\n"
"v_mul_f32 v61, v61, v79\n"
"v_mul_f32 v62, v62, v79\n"
"v_mul_f32 v63, v63, v79\n"
"v_cvt_pk_bf16_f32 v236, v60, v61\n"
"v_mul_f32 v64, v64, v79\n"
"v_mul_f32 v65, v65, v79\n"
"v_cvt_pk_bf16_f32 v237, v62, v63\n"
"v_mul_f32 v66, v66, v79\n"
"v_mul_f32 v67, v67, v79\n"
"v_cvt_pk_bf16_f32 v238, v64, v65\n"
"v_mul_f32 v68, v68, v79\n"
"v_mul_f32 v69, v69, v79\n"
"v_cvt_pk_bf16_f32 v239, v66, v67\n"
"v_mul_f32 v70, v70, v79\n"
"v_mul_f32 v71, v71, v79\n"
"v_cvt_pk_bf16_f32 v240, v68, v69\n"
"v_mul_f32 v72, v72, v79\n"
"v_mul_f32 v73, v73, v79\n"
"v_cvt_pk_bf16_f32 v241, v70, v71\n"
"v_mul_f32 v74, v74, v79\n"
"v_mul_f32 v75, v75, v79\n"
"v_cvt_pk_bf16_f32 v242, v72, v73\n"
"v_mul_f32 v76, v76, v79\n"
"v_mul_f32 v77, v77, v79\n"
"v_cvt_pk_bf16_f32 v243, v74, v75\n"
"v_cvt_pk_bf16_f32 v244, v76, v77\n"
"v_min3_f32 v78, v150, v151, v152\n"
"v_min3_f32 v78, v78, v153, v154\n"
"v_min3_f32 v78, v78, v155, v156\n"
"v_min3_f32 v78, v78, v157, v158\n"
"v_min3_f32 v78, v78, v159, v160\n"
"v_min3_f32 v78, v78, v161, v162\n"
"v_min3_f32 v78, v78, v163, v164\n"
"v_min3_f32 v78, v78, v165, v166\n"
"v_min_f32 v78, v78, v167\n"
"v_sub_f32 v60, v78, v150\n"
"v_sub_f32 v61, v78, v151\n"
"v_exp_f32 v60, v60\n"
"v_sub_f32 v62, v78, v152\n"
"v_exp_f32 v61, v61\n"
"v_sub_f32 v63, v78, v153\n"
"v_exp_f32 v62, v62\n"
"v_sub_f32 v64, v78, v154\n"
"v_add_f32 v79, v60, v61\n"
"v_exp_f32 v63, v63\n"
"v_sub_f32 v65, v78, v155\n"
"v_add_f32 v79, v79, v62\n"
"v_exp_f32 v64, v64\n"
"v_sub_f32 v66, v78, v156\n"
"v_add_f32 v79, v79, v63\n"
"v_exp_f32 v65, v65\n"
"v_sub_f32 v67, v78, v157\n"
"v_add_f32 v79, v79, v64\n"
"v_exp_f32 v66, v66\n"
"v_sub_f32 v68, v78, v158\n"
"v_add_f32 v79, v79, v65\n"
"v_exp_f32 v67, v67\n"
"v_sub_f32 v69, v78, v159\n"
"v_add_f32 v79, v79, v66\n"
"v_exp_f32 v68, v68\n"
"v_sub_f32 v70, v78, v160\n"
"v_add_f32 v79, v79, v67\n"
"v_exp_f32 v69, v69\n"
"v_sub_f32 v71, v78, v161\n"
"v_add_f32 v79, v79, v68\n"
"v_exp_f32 v70, v70\n"
"v_sub_f32 v72, v78, v162\n"
"v_add_f32 v79, v79, v69\n"
"v_exp_f32 v71, v71\n"
"v_sub_f32 v73, v78, v163\n"
"v_add_f32 v79, v79, v70\n"
"v_exp_f32 v72, v72\n"
"v_sub_f32 v74, v78, v164\n"
"v_add_f32 v79, v79, v71\n"
"v_exp_f32 v73, v73\n"
"v_sub_f32 v75, v78, v165\n"
"v_add_f32 v79, v79, v72\n"
"v_exp_f32 v74, v74\n"
"v_sub_f32 v76, v78, v166\n"
"v_add_f32 v79, v79, v73\n"
"v_exp_f32 v75, v75\n"
"v_sub_f32 v77, v78, v167\n"
"v_add_f32 v79, v79, v74\n"
"v_exp_f32 v76, v76\n"
"v_add_f32 v79, v79, v75\n"
"v_exp_f32 v77, v77\n"
"v_add_f32 v79, v79, v76\n"
"v_add_f32 v79, v79, v77\n"
"v_rcp_f32 v79, v79\n"
"s_nop 0\n"
"v_mul_f32 v60, v60, v79\n"
"v_mul_f32 v61, v61, v79\n"
"v_mul_f32 v62, v62, v79\n"
"v_mul_f32 v63, v63, v79\n"
"v_cvt_pk_bf16_f32 v245, v60, v61\n"
"v_mul_f32 v64, v64, v79\n"
"v_mul_f32 v65, v65, v79\n"
"v_cvt_pk_bf16_f32 v246, v62, v63\n"
"v_mul_f32 v66, v66, v79\n"
"v_mul_f32 v67, v67, v79\n"
"v_cvt_pk_bf16_f32 v247, v64, v65\n"
"v_mul_f32 v68, v68, v79\n"
"v_mul_f32 v69, v69, v79\n"
"v_cvt_pk_bf16_f32 v248, v66, v67\n"
"v_mul_f32 v70, v70, v79\n"
"v_mul_f32 v71, v71, v79\n"
"v_cvt_pk_bf16_f32 v249, v68, v69\n"
"v_mul_f32 v72, v72, v79\n"
"v_mul_f32 v73, v73, v79\n"
"v_cvt_pk_bf16_f32 v250, v70, v71\n"
"v_mul_f32 v74, v74, v79\n"
"v_mul_f32 v75, v75, v79\n"
"v_cvt_pk_bf16_f32 v251, v72, v73\n"
"v_mul_f32 v76, v76, v79\n"
"v_mul_f32 v77, v77, v79\n"
"v_cvt_pk_bf16_f32 v252, v74, v75\n"
"v_cvt_pk_bf16_f32 v253, v76, v77\n"
"v_mov_b32 v194, 0\n"
"v_mov_b32 v195, 0\n"
"v_mov_b32 v214, 0\n"
"v_mov_b32 v215, 0\n"
"v_mov_b32 v234, 0\n"
"v_mov_b32 v235, 0\n"
"v_mov_b32 v254, 0\n"
"v_mov_b32 v255, 0\n"
"v_mov_b32 v7, 0\n"
"s_mov_b32 s57, 0\n"
"SPIN_%=:\n"
"global_load_dword v60, v7, %8 sc1\n"
"s_waitcnt vmcnt(0)\n"
"v_readfirstlane_b32 s56, v60\n"
"s_add_u32 s57, s57, 1\n"
"s_cmp_ge_u32 s56, 16\n"
"s_cbranch_scc1 GO_%=\n"
"s_sleep 1\n"
"s_cmp_lt_u32 s57, 0x400000\n"
"s_cbranch_scc1 SPIN_%=\n"
"GO_%=:\n"
"s_mov_b32 s56, 0\n"
"buffer_load_dwordx4 v[32:35], v6, s[52:55], s56 offen sc1\n"
"s_mov_b32 s56, 4096\n"
"buffer_load_dwordx4 v[36:39], v6, s[52:55], s56 offen sc1\n"
"s_mov_b32 s56, 8192\n"
"buffer_load_dwordx4 v[40:43], v6, s[52:55], s56 offen sc1\n"
"s_mov_b32 s56, 12288\n"
"buffer_load_dwordx4 v[44:47], v6, s[52:55], s56 offen sc1\n"
"s_mov_b32 s56, 16384\n"
"buffer_load_dwordx4 v[48:51], v6, s[52:55], s56 offen sc1\n"
"s_mov_b32 s56, 20480\n"
"buffer_load_dwordx4 v[52:55], v6, s[52:55], s56 offen sc1\n"
"v_lshrrev_b32 v56, 4, v0\n"
"v_lshrrev_b32 v57, 5, v56\n"
"v_bfe_u32 v58, v56, 2, 1\n"
"v_bfe_u32 v59, v56, 3, 2\n"
"v_and_b32 v60, 3, v56\n"
"v_lshl_add_u32 v59, v59, 2, v60\n"
"v_mul_u32_u24 v60, 6080, v57\n"
"v_mov_b32 v61, 80\n"
"v_mad_u32_u24 v60, v59, v61, v60\n"
"v_add_u32 v60, 110592, v60\n"
"v_add_u32 v61, 1280, v60\n"
"v_mov_b32 v62, 122752\n"
"v_mov_b32 v63, 1520\n"
"v_cmp_eq_u32 vcc, v57, v58\n"
"v_cmp_gt_u32 s[56:57], 3, v59\n"
"s_nop 1\n"
"v_cndmask_b32 v2, v62, v60, vcc\n"
"v_cndmask_b32 v3, 0, v63, vcc\n"
"s_and_b64 vcc, vcc, s[56:57]\n"
"s_nop 1\n"
"v_cndmask_b32 v4, v62, v61, vcc\n"
"v_cndmask_b32 v5, 0, v63, vcc\n"
"v_lshrrev_b32 v6, 1, v6\n"
"v_add_u32 v6, 110592, v6\n"
"s_waitcnt vmcnt(5)\n"
"v_cvt_pk_bf16_f32 v64, v32, v33\n"
"v_cvt_pk_bf16_f32 v65, v34, v35\n"
"ds_write_b64 v6, v[64:65] offset:0\n"
"s_waitcnt vmcnt(4)\n"
"v_cvt_pk_bf16_f32 v66, v36, v37\n"
"v_cvt_pk_bf16_f32 v67, v38, v39\n"
"ds_write_b64 v6, v[66:67] offset:2048\n"
"s_waitcnt vmcnt(3)\n"
"v_cvt_pk_bf16_f32 v68, v40, v41\n"
"v_cvt_pk_bf16_f32 v69, v42, v43\n"
"ds_write_b64 v6, v[68:69] offset:4096\n"
"s_waitcnt vmcnt(2)\n"
"v_cvt_pk_bf16_f32 v70, v44, v45\n"
"v_cvt_pk_bf16_f32 v71, v46, v47\n"
"ds_write_b64 v6, v[70:71] offset:6144\n"
"s_waitcnt vmcnt(1)\n"
"v_cvt_pk_bf16_f32 v72, v48, v49\n"
"v_cvt_pk_bf16_f32 v73, v50, v51\n"
"ds_write_b64 v6, v[72:73] offset:8192\n"
"s_waitcnt vmcnt(0)\n"
"v_cvt_pk_bf16_f32 v74, v52, v53\n"
"v_cvt_pk_bf16_f32 v75, v54, v55\n"
"ds_write_b64 v6, v[74:75] offset:10240\n"
"s_waitcnt lgkmcnt(0)\n"
"s_barrier\n"
"ds_read_b128 a[0:3], v2 offset:0\n"
"ds_read_b128 a[4:7], v4 offset:0\n"
"ds_read_b128 a[8:11], v2 offset:16\n"
"ds_read_b128 a[12:15], v4 offset:16\n"
"ds_read_b128 a[16:19], v2 offset:32\n"
"ds_read_b128 a[20:23], v4 offset:32\n"
"ds_read_b128 a[24:27], v2 offset:48\n"
"ds_read_b128 a[28:31], v4 offset:48\n"
"ds_read_b128 a[32:35], v2 offset:64\n"
"ds_read_b128 a[36:39], v4 offset:64\n"
"v_add_u32 v2, v2, v3\n"
"v_add_u32 v4, v4, v5\n"
"ds_read_b128 a[40:43], v2 offset:0\n"
"ds_read_b128 a[44:47], v4 offset:0\n"
"ds_read_b128 a[48:51], v2 offset:16\n"
"ds_read_b128 a[52:55], v4 offset:16\n"
"ds_read_b128 a[56:59], v2 offset:32\n"
"ds_read_b128 a[60:63], v4 offset:32\n"
"ds_read_b128 a[64:67], v2 offset:48\n"
"ds_read_b128 a[68:71], v4 offset:48\n"
"ds_read_b128 a[72:75], v2 offset:64\n"
"ds_read_b128 a[76:79], v4 offset:64\n"
"v_add_u32 v2, v2, v3\n"
"v_add_u32 v4, v4, v5\n"
"s_waitcnt lgkmcnt(15)\n"
"v_mfma_f32_32x32x16_bf16 v[32:47], a[0:3], v[176:179], 0\n"
"v_mfma_f32_32x32x16_bf16 v[48:63], a[4:7], v[176:179], 0\n"
"v_mfma_f32_32x32x16_bf16 v[32:47], a[8:11], v[180:183], v[32:47]\n"
"v_mfma_f32_32x32x16_bf16 v[48:63], a[12:15], v[180:183], v[48:63]\n"
"s_waitcnt lgkmcnt(14)\n"
"v_mfma_f32_32x32x16_bf16 v[32:47], a[16:19], v[184:187], v[32:47]\n"
"v_mfma_f32_32x32x16_bf16 v[48:63], a[20:23], v[184:187], v[48:63]\n"
"s_waitcnt lgkmcnt(12)\n"
"v_mfma_f32_32x32x16_bf16 v[32:47], a[24:27], v[188:191], v[32:47]\n"
"v_mfma_f32_32x32x16_bf16 v[48:63], a[28:31], v[188:191], v[48:63]\n"
"s_waitcnt lgkmcnt(10)\n"
"v_mfma_f32_32x32x16_bf16 v[32:47], a[32:35], v[192:195], v[32:47]\n"
"v_mfma_f32_32x32x16_bf16 v[48:63], a[36:39], v[192:195], v[48:63]\n"
"ds_read_b128 a[80:83], v2 offset:0\n"
"ds_read_b128 a[84:87], v4 offset:0\n"
"ds_read_b128 a[88:91], v2 offset:16\n"
"ds_read_b128 a[92:95], v4 offset:16\n"
"ds_read_b128 a[96:99], v2 offset:32\n"
"ds_read_b128 a[100:103], v4 offset:32\n"
"ds_read_b128 a[104:107], v2 offset:48\n"
"ds_read_b128 a[108:111], v4 offset:48\n"
"ds_read_b128 a[112:115], v2 offset:64\n"
"ds_read_b128 a[116:119], v4 offset:64\n"
"v_add_u32 v2, v2, v3\n"
"v_add_u32 v4, v4, v5\n"
"s_waitcnt lgkmcnt(15)\n"
"v_mfma_f32_32x32x16_bf16 v[64:79], a[40:43], v[196:199], 0\n"
"v_mfma_f32_32x32x16_bf16 v[80:95], a[44:47], v[196:199], 0\n"
"v_mfma_f32_32x32x16_bf16 v[64:79], a[48:51], v[200:203], v[64:79]\n"
"v_mfma_f32_32x32x16_bf16 v[80:95], a[52:55], v[200:203], v[80:95]\n"
"v_add_f32 v7, 0x358637bd, v50\n"
"v_rcp_f32 v7, v7\n"
"s_nop 0\n"
"s_waitcnt lgkmcnt(14)\n"
"v_mfma_f32_32x32x16_bf16 v[64:79], a[56:59], v[204:207], v[64:79]\n"
"v_mul_f32 v32, v32, v7\n"
"v_mul_f32 v33, v33, v7\n"
"v_mul_f32 v34, v34, v7\n"
"v_mul_f32 v35, v35, v7\n"
"v_mul_f32 v36, v36, v7\n"
"v_mfma_f32_32x32x16_bf16 v[80:95], a[60:63], v[204:207], v[80:95]\n"
"v_mul_f32 v37, v37, v7\n"
"v_mul_f32 v38, v38, v7\n"
"v_mul_f32 v39, v39, v7\n"
"v_mul_f32 v40, v40, v7\n"
"s_waitcnt lgkmcnt(12)\n"
"v_mfma_f32_32x32x16_bf16 v[64:79], a[64:67], v[208:211], v[64:79]\n"
"v_mul_f32 v41, v41, v7\n"
"v_mul_f32 v42, v42, v7\n"
"v_mul_f32 v43, v43, v7\n"
"v_mul_f32 v44, v44, v7\n"
"v_mul_f32 v45, v45, v7\n"
"v_mfma_f32_32x32x16_bf16 v[80:95], a[68:71], v[208:211], v[80:95]\n"
"v_mul_f32 v46, v46, v7\n"
"v_mul_f32 v47, v47, v7\n"
"v_mul_f32 v48, v48, v7\n"
"v_mul_f32 v49, v49, v7\n"
"s_waitcnt lgkmcnt(10)\n"
"v_mfma_f32_32x32x16_bf16 v[64:79], a[72:75], v[212:215], v[64:79]\n"
"v_cvt_pk_bf16_f32 v176, v32, v33\n"
"v_cvt_pk_bf16_f32 v177, v34, v35\n"
"v_cvt_pk_bf16_f32 v178, v36, v37\n"
"v_cvt_pk_bf16_f32 v179, v38, v39\n"
"v_cvt_pk_bf16_f32 v180, v40, v41\n"
"v_mfma_f32_32x32x16_bf16 v[80:95], a[76:79], v[212:215], v[80:95]\n"
"v_cvt_pk_bf16_f32 v181, v42, v43\n"
"v_cvt_pk_bf16_f32 v182, v44, v45\n"
"v_cvt_pk_bf16_f32 v183, v46, v47\n"
"v_cvt_pk_bf16_f32 v184, v48, v49\n"
"ds_read_b128 a[120:123], v2 offset:0\n"
"ds_read_b128 a[124:127], v4 offset:0\n"
"ds_read_b128 a[128:131], v2 offset:16\n"
"ds_read_b128 a[132:135], v4 offset:16\n"
"ds_read_b128 a[136:139], v2 offset:32\n"
"ds_read_b128 a[140:143], v4 offset:32\n"
"ds_read_b128 a[144:147], v2 offset:48\n"
"ds_read_b128 a[148:151], v4 offset:48\n"
"ds_read_b128 a[152:155], v2 offset:64\n"
"ds_read_b128 a[156:159], v4 offset:64\n"
"v_add_u32 v2, v2, v3\n"
"v_add_u32 v4, v4, v5\n"
"s_waitcnt lgkmcnt(15)\n"
"v_mfma_f32_32x32x16_bf16 v[96:111], a[80:83], v[216:219], 0\n"
"v_mfma_f32_32x32x16_bf16 v[112:127], a[84:87], v[216:219], 0\n"
"v_mfma_f32_32x32x16_bf16 v[96:111], a[88:91], v[220:223], v[96:111]\n"
"v_mfma_f32_32x32x16_bf16 v[112:127], a[92:95], v[220:223], v[112:127]\n"
"v_add_f32 v7, 0x358637bd, v82\n"
"v_rcp_f32 v7, v7\n"
"s_nop 0\n"
"s_waitcnt lgkmcnt(14)\n"
"v_mfma_f32_32x32x16_bf16 v[96:111], a[96:99], v[224:227], v[96:111]\n"
"v_mul_f32 v64, v64, v7\n"
"v_mul_f32 v65, v65, v7\n"
"v_mul_f32 v66, v66, v7\n"
"v_mul_f32 v67, v67, v7\n"
"v_mul_f32 v68, v68, v7\n"
"v_mfma_f32_32x32x16_bf16 v[112:127], a[100:103], v[224:227], v[112:127]\n"
"v_mul_f32 v69, v69, v7\n"
"v_mul_f32 v70, v70, v7\n"
"v_mul_f32 v71, v71, v7\n"
"v_mul_f32 v72, v72, v7\n"
"s_waitcnt lgkmcnt(12)\n"
"v_mfma_f32_32x32x16_bf16 v[96:111], a[104:107], v[228:231], v[96:111]\n"
"v_mul_f32 v73, v73, v7\n"
"v_mul_f32 v74, v74, v7\n"
"v_mul_f32 v75, v75, v7\n"
"v_mul_f32 v76, v76, v7\n"
"v_mul_f32 v77, v77, v7\n"
"v_mfma_f32_32x32x16_bf16 v[112:127], a[108:111], v[228:231], v[112:127]\n"
"v_mul_f32 v78, v78, v7\n"
"v_mul_f32 v79, v79, v7\n"
"v_mul_f32 v80, v80, v7\n"
"v_mul_f32 v81, v81, v7\n"
"s_waitcnt lgkmcnt(10)\n"
"v_mfma_f32_32x32x16_bf16 v[96:111], a[112:115], v[232:235], v[96:111]\n"
"v_cvt_pk_bf16_f32 v185, v64, v65\n"
"v_cvt_pk_bf16_f32 v186, v66, v67\n"
"v_cvt_pk_bf16_f32 v187, v68, v69\n"
"v_cvt_pk_bf16_f32 v188, v70, v71\n"
"v_cvt_pk_bf16_f32 v189, v72, v73\n"
"v_mfma_f32_32x32x16_bf16 v[112:127], a[116:119], v[232:235], v[112:127]\n"
"v_cvt_pk_bf16_f32 v190, v74, v75\n"
"v_cvt_pk_bf16_f32 v191, v76, v77\n"
"v_cvt_pk_bf16_f32 v192, v78, v79\n"
"v_cvt_pk_bf16_f32 v193, v80, v81\n"
"s_waitcnt lgkmcnt(8)\n"
"v_mfma_f32_32x32x16_bf16 v[128:143], a[120:123], v[236:239], 0\n"
"s_nop 7\n"
"v_mfma_f32_32x32x16_bf16 v[144:159], a[124:127], v[236:239], 0\n"
"s_waitcnt lgkmcnt(6)\n"
"v_mfma_f32_32x32x16_bf16 v[128:143], a[128:131], v[240:243], v[128:143]\n"
"v_mfma_f32_32x32x16_bf16 v[144:159], a[132:135], v[240:243], v[144:159]\n"
"v_add_f32 v7, 0x358637bd, v114\n"
"v_rcp_f32 v7, v7\n"
"s_nop 0\n"
"s_waitcnt lgkmcnt(4)\n"
"v_mfma_f32_32x32x16_bf16 v[128:143], a[136:139], v[244:247], v[128:143]\n"
"v_mul_f32 v96, v96, v7\n"
"v_mul_f32 v97, v97, v7\n"
"v_mul_f32 v98, v98, v7\n"
"v_mul_f32 v99, v99, v7\n"
"v_mul_f32 v100, v100, v7\n"
"v_mfma_f32_32x32x16_bf16 v[144:159], a[140:143], v[244:247], v[144:159]\n"
"v_mul_f32 v101, v101, v7\n"
"v_mul_f32 v102, v102, v7\n"
"v_mul_f32 v103, v103, v7\n"
"v_mul_f32 v104, v104, v7\n"
"s_waitcnt lgkmcnt(2)\n"
"v_mfma_f32_32x32x16_bf16 v[128:143], a[144:147], v[248:251], v[128:143]\n"
"v_mul_f32 v105, v105, v7\n"
"v_mul_f32 v106, v106, v7\n"
"v_mul_f32 v107, v107, v7\n"
"v_mul_f32 v108, v108, v7\n"
"v_mul_f32 v109, v109, v7\n"
"v_mfma_f32_32x32x16_bf16 v[144:159], a[148:151], v[248:251], v[144:159]\n"
"v_mul_f32 v110, v110, v7\n"
"v_mul_f32 v111, v111, v7\n"
"v_mul_f32 v112, v112, v7\n"
"v_mul_f32 v113, v113, v7\n"
"s_waitcnt lgkmcnt(0)\n"
"v_mfma_f32_32x32x16_bf16 v[128:143], a[152:155], v[252:255], v[128:143]\n"
"v_cvt_pk_bf16_f32 v194, v96, v97\n"
"v_cvt_pk_bf16_f32 v195, v98, v99\n"
"v_cvt_pk_bf16_f32 v196, v100, v101\n"
"v_cvt_pk_bf16_f32 v197, v102, v103\n"
"v_cvt_pk_bf16_f32 v198, v104, v105\n"
"v_mfma_f32_32x32x16_bf16 v[144:159], a[156:159], v[252:255], v[144:159]\n"
"v_cvt_pk_bf16_f32 v199, v106, v107\n"
"v_cvt_pk_bf16_f32 v200, v108, v109\n"
"v_cvt_pk_bf16_f32 v201, v110, v111\n"
"v_cvt_pk_bf16_f32 v202, v112, v113\n"
"s_nop 7\n"
"s_nop 3\n"
"v_add_f32 v7, 0x358637bd, v146\n"
"v_rcp_f32 v7, v7\n"
"s_nop 0\n"
"v_mul_f32 v128, v128, v7\n"
"v_mul_f32 v129, v129, v7\n"
"v_mul_f32 v130, v130, v7\n"
"v_mul_f32 v131, v131, v7\n"
"v_mul_f32 v132, v132, v7\n"
"v_mul_f32 v133, v133, v7\n"
"v_mul_f32 v134, v134, v7\n"
"v_mul_f32 v135, v135, v7\n"
"v_mul_f32 v136, v136, v7\n"
"v_mul_f32 v137, v137, v7\n"
"v_mul_f32 v138, v138, v7\n"
"v_mul_f32 v139, v139, v7\n"
"v_mul_f32 v140, v140, v7\n"
"v_mul_f32 v141, v141, v7\n"
"v_mul_f32 v142, v142, v7\n"
"v_mul_f32 v143, v143, v7\n"
"v_mul_f32 v144, v144, v7\n"
"v_mul_f32 v145, v145, v7\n"
"v_cvt_pk_bf16_f32 v203, v128, v129\n"
"v_cvt_pk_bf16_f32 v204, v130, v131\n"
"v_cvt_pk_bf16_f32 v205, v132, v133\n"
"v_cvt_pk_bf16_f32 v206, v134, v135\n"
"v_cvt_pk_bf16_f32 v207, v136, v137\n"
"v_cvt_pk_bf16_f32 v208, v138, v139\n"
"v_cvt_pk_bf16_f32 v209, v140, v141\n"
"v_cvt_pk_bf16_f32 v210, v142, v143\n"
"v_cvt_pk_bf16_f32 v211, v144, v145\n"
"s_mov_b64 s[50:51], %9\n"
"s_nop 0\n"
"v_mfma_f32_32x32x16_bf16 v[32:47], v[8:11], v[176:179], 0\n"
"ds_read_b128 v[8:11], v0 offset:6144\n"
"v_mfma_f32_32x32x16_bf16 v[32:47], v[12:15], v[180:183], v[32:47]\n"
"ds_read_b128 v[12:15], v0 offset:7168\n"
"v_mfma_f32_32x32x16_bf16 v[32:47], v[16:19], v[184:187], v[32:47]\n"
"ds_read_b128 v[16:19], v0 offset:8192\n"
"v_mfma_f32_32x32x16_bf16 v[32:47], v[20:23], v[188:191], v[32:47]\n"
"ds_read_b128 v[20:23], v0 offset:9216\n"
"v_mfma_f32_32x32x16_bf16 v[32:47], v[24:27], v[192:195], v[32:47]\n"
"ds_read_b128 v[24:27], v0 offset:10240\n"
"v_mfma_f32_32x32x16_bf16 v[32:47], v[28:31], v[196:199], v[32:47]\n"
"ds_read_b128 v[28:31], v0 offset:11264\n"
"s_waitcnt lgkmcnt(4)\n"
"v_mfma_f32_32x32x16_bf16 v[32:47], v[8:11], v[200:203], v[32:47]\n"
"ds_read_b128 v[8:11], v0 offset:12288\n"
"v_mfma_f32_32x32x16_bf16 v[32:47], v[12:15], v[204:207], v[32:47]\n"
"ds_read_b128 v[12:15], v0 offset:13312\n"
"s_waitcnt lgkmcnt(4)\n"
"v_mfma_f32_32x32x16_bf16 v[32:47], v[16:19], v[208:211], v[32:47]\n"
"ds_read_b128 v[16:19], v0 offset:14336\n"
"v_mfma_f32_32x32x16_bf16 v[48:63], v[20:23], v[176:179], 0\n"
"ds_read_b128 v[20:23], v0 offset:15360\n"
"s_waitcnt lgkmcnt(4)\n"
"v_mfma_f32_32x32x16_bf16 v[48:63], v[24:27], v[180:183], v[48:63]\n"
"ds_read_b128 v[24:27], v0 offset:16384\n"
"v_mfma_f32_32x32x16_bf16 v[48:63], v[28:31], v[184:187], v[48:63]\n"
"ds_read_b128 v[28:31], v0 offset:17408\n"
"s_waitcnt lgkmcnt(4)\n"
"v_mfma_f32_32x32x16_bf16 v[48:63], v[8:11], v[188:191], v[48:63]\n"
"ds_read_b128 v[8:11], v0 offset:18432\n"
"v_mfma_f32_32x32x16_bf16 v[48:63], v[12:15], v[192:195], v[48:63]\n"
"ds_read_b128 v[12:15], v0 offset:19456\n"
"s_waitcnt lgkmcnt(4)\n"
"v_mfma_f32_32x32x16_bf16 v[48:63], v[16:19], v[196:199], v[48:63]\n"
"ds_read_b128 v[16:19], v0 offset:20480\n"
"v_cvt_pk_bf16_f32 v64, v32, v33\n"
"v_cvt_pk_bf16_f32 v65, v34, v35\n"
"v_cvt_pk_bf16_f32 v66, v36, v37\n"
"v_mfma_f32_32x32x16_bf16 v[48:63], v[20:23], v[200:203], v[48:63]\n"
"ds_read_b128 v[20:23], v0 offset:21504\n"
"v_cvt_pk_bf16_f32 v67, v38, v39\n"
"global_store_dwordx4 v0, v[64:67], s[50:51] offset:0 sc1\n"
"s_waitcnt lgkmcnt(4)\n"
"v_mfma_f32_32x32x16_bf16 v[48:63], v[24:27], v[204:207], v[48:63]\n"
"ds_read_b128 v[24:27], v0 offset:22528\n"
"v_cvt_pk_bf16_f32 v68, v40, v41\n"
"v_cvt_pk_bf16_f32 v69, v42, v43\n"
"v_cvt_pk_bf16_f32 v70, v44, v45\n"
"v_mfma_f32_32x32x16_bf16 v[48:63], v[28:31], v[208:211], v[48:63]\n"
"ds_read_b128 v[28:31], v0 offset:23552\n"
"v_cvt_pk_bf16_f32 v71, v46, v47\n"
"global_store_dwordx4 v0, v[68:71], s[50:51] offset:1024 sc1\n"
"s_waitcnt lgkmcnt(4)\n"
"v_mfma_f32_32x32x16_bf16 v[32:47], v[8:11], v[176:179], 0\n"
"ds_read_b128 v[8:11], v0 offset:24576\n"
"v_mfma_f32_32x32x16_bf16 v[32:47], v[12:15], v[180:183], v[32:47]\n"
"ds_read_b128 v[12:15], v0 offset:25600\n"
"s_waitcnt lgkmcnt(4)\n"
"v_mfma_f32_32x32x16_bf16 v[32:47], v[16:19], v[184:187], v[32:47]\n"
"ds_read_b128 v[16:19], v0 offset:26624\n"
"v_mfma_f32_32x32x16_bf16 v[32:47], v[20:23], v[188:191], v[32:47]\n"
"ds_read_b128 v[20:23], v0 offset:27648\n"
"s_waitcnt lgkmcnt(4)\n"
"v_mfma_f32_32x32x16_bf16 v[32:47], v[24:27], v[192:195], v[32:47]\n"
"ds_read_b128 v[24:27], v0 offset:28672\n"
"v_mfma_f32_32x32x16_bf16 v[32:47], v[28:31], v[196:199], v[32:47]\n"
"ds_read_b128 v[28:31], v0 offset:29696\n"
"v_cvt_pk_bf16_f32 v72, v48, v49\n"
"v_cvt_pk_bf16_f32 v73, v50, v51\n"
"v_cvt_pk_bf16_f32 v74, v52, v53\n"
"s_waitcnt lgkmcnt(4)\n"
"v_mfma_f32_32x32x16_bf16 v[32:47], v[8:11], v[200:203], v[32:47]\n"
"ds_read_b128 v[8:11], v0 offset:30720\n"
"v_cvt_pk_bf16_f32 v75, v54, v55\n"
"global_store_dwordx4 v0, v[72:75], s[50:51] offset:2048 sc1\n"
"v_mfma_f32_32x32x16_bf16 v[32:47], v[12:15], v[204:207], v[32:47]\n"
"ds_read_b128 v[12:15], v0 offset:31744\n"
"v_cvt_pk_bf16_f32 v76, v56, v57\n"
"v_cvt_pk_bf16_f32 v77, v58, v59\n"
"v_cvt_pk_bf16_f32 v78, v60, v61\n"
"s_waitcnt lgkmcnt(4)\n"
"v_mfma_f32_32x32x16_bf16 v[32:47], v[16:19], v[208:211], v[32:47]\n"
"ds_read_b128 v[16:19], v0 offset:32768\n"
"v_cvt_pk_bf16_f32 v79, v62, v63\n"
"global_store_dwordx4 v0, v[76:79], s[50:51] offset:3072 sc1\n"
"s_add_u32 s50, s50, 4096\n"
"s_addc_u32 s51, s51, 0\n"
"v_mfma_f32_32x32x16_bf16 v[48:63], v[20:23], v[176:179], 0\n"
"ds_read_b128 v[20:23], v0 offset:33792\n"
"s_waitcnt lgkmcnt(4)\n"
"v_mfma_f32_32x32x16_bf16 v[48:63], v[24:27], v[180:183], v[48:63]\n"
"ds_read_b128 v[24:27], v0 offset:34816\n"
"v_mfma_f32_32x32x16_bf16 v[48:63], v[28:31], v[184:187], v[48:63]\n"
"ds_read_b128 v[28:31], v0 offset:35840\n"
"s_waitcnt lgkmcnt(4)\n"
"v_mfma_f32_32x32x16_bf16 v[48:63], v[8:11], v[188:191], v[48:63]\n"
"s_barrier\n"
"ds_read_b128 v[8:11], v0 offset:36864\n"
"v_mfma_f32_32x32x16_bf16 v[48:63], v[12:15], v[192:195], v[48:63]\n"
"ds_read_b128 v[12:15], v0 offset:37888\n"
"s_waitcnt lgkmcnt(4)\n"
"v_mfma_f32_32x32x16_bf16 v[48:63], v[16:19], v[196:199], v[48:63]\n"
"ds_read_b128 v[16:19], v0 offset:38912\n"
"v_cvt_pk_bf16_f32 v80, v32, v33\n"
"v_cvt_pk_bf16_f32 v81, v34, v35\n"
"v_cvt_pk_bf16_f32 v82, v36, v37\n"
"v_mfma_f32_32x32x16_bf16 v[48:63], v[20:23], v[200:203], v[48:63]\n"
"ds_read_b128 v[20:23], v0 offset:39936\n"
"v_cvt_pk_bf16_f32 v83, v38, v39\n"
"global_store_dwordx4 v0, v[80:83], s[50:51] offset:0 sc1\n"
"s_waitcnt lgkmcnt(4)\n"
"v_mfma_f32_32x32x16_bf16 v[48:63], v[24:27], v[204:207], v[48:63]\n"
"ds_read_b128 v[24:27], v0 offset:40960\n"
"v_cvt_pk_bf16_f32 v84, v40, v41\n"
"v_cvt_pk_bf16_f32 v85, v42, v43\n"
"v_cvt_pk_bf16_f32 v86, v44, v45\n"
"v_mfma_f32_32x32x16_bf16 v[48:63], v[28:31], v[208:211], v[48:63]\n"
"ds_read_b128 v[28:31], v0 offset:41984\n"
"v_cvt_pk_bf16_f32 v87, v46, v47\n"
"global_store_dwordx4 v0, v[84:87], s[50:51] offset:1024 sc1\n"
"s_waitcnt lgkmcnt(4)\n"
"v_mfma_f32_32x32x16_bf16 v[32:47], v[8:11], v[176:179], 0\n"
"ds_read_b128 v[8:11], v0 offset:43008\n"
"v_mfma_f32_32x32x16_bf16 v[32:47], v[12:15], v[180:183], v[32:47]\n"
"ds_read_b128 v[12:15], v0 offset:44032\n"
"s_waitcnt lgkmcnt(4)\n"
"v_mfma_f32_32x32x16_bf16 v[32:47], v[16:19], v[184:187], v[32:47]\n"
"ds_read_b128 v[16:19], v0 offset:45056\n"
"s_add_u32 m0, s45, 0\n"
"s_add_u32 s44, s45, 663552\n"
"buffer_load_dwordx4 v0, s[40:43], s44 offen offset:0 lds\n"
"v_mfma_f32_32x32x16_bf16 v[32:47], v[20:23], v[188:191], v[32:47]\n"
"ds_read_b128 v[20:23], v0 offset:46080\n"
"s_waitcnt lgkmcnt(4)\n"
"v_mfma_f32_32x32x16_bf16 v[32:47], v[24:27], v[192:195], v[32:47]\n"
"ds_read_b128 v[24:27], v0 offset:47104\n"
"v_mfma_f32_32x32x16_bf16 v[32:47], v[28:31], v[196:199], v[32:47]\n"
"ds_read_b128 v[28:31], v0 offset:48128\n"
"buffer_load_dwordx4 v0, s[40:43], s44 offen offset:1024 lds\n"
"v_cvt_pk_bf16_f32 v88, v48, v49\n"
"v_cvt_pk_bf16_f32 v89, v50, v51\n"
"v_cvt_pk_bf16_f32 v90, v52, v53\n"
"s_waitcnt lgkmcnt(4)\n"
"v_mfma_f32_32x32x16_bf16 v[32:47], v[8:11], v[200:203], v[32:47]\n"
"ds_read_b128 v[8:11], v0 offset:49152\n"
"v_cvt_pk_bf16_f32 v91, v54, v55\n"
"global_store_dwordx4 v0, v[88:91], s[50:51] offset:2048 sc1\n"
"v_mfma_f32_32x32x16_bf16 v[32:47], v[12:15], v[204:207], v[32:47]\n"
"ds_read_b128 v[12:15], v0 offset:50176\n"
"v_cvt_pk_bf16_f32 v92, v56, v57\n"
"v_cvt_pk_bf16_f32 v93, v58, v59\n"
"v_cvt_pk_bf16_f32 v94, v60, v61\n"
"s_waitcnt lgkmcnt(4)\n"
"v_mfma_f32_32x32x16_bf16 v[32:47], v[16:19], v[208:211], v[32:47]\n"
"ds_read_b128 v[16:19], v0 offset:51200\n"
"buffer_load_dwordx4 v0, s[40:43], s44 offen offset:2048 lds\n"
"v_cvt_pk_bf16_f32 v95, v62, v63\n"
"global_store_dwordx4 v0, v[92:95], s[50:51] offset:3072 sc1\n"
"s_add_u32 s50, s50, 4096\n"
"s_addc_u32 s51, s51, 0\n"
"v_mfma_f32_32x32x16_bf16 v[48:63], v[20:23], v[176:179], 0\n"
"ds_read_b128 v[20:23], v0 offset:52224\n"
"s_waitcnt lgkmcnt(4)\n"
"v_mfma_f32_32x32x16_bf16 v[48:63], v[24:27], v[180:183], v[48:63]\n"
"ds_read_b128 v[24:27], v0 offset:53248\n"
"v_mfma_f32_32x32x16_bf16 v[48:63], v[28:31], v[184:187], v[48:63]\n"
"ds_read_b128 v[28:31], v0 offset:54272\n"
"buffer_load_dwordx4 v0, s[40:43], s44 offen offset:3072 lds\n"
"s_waitcnt lgkmcnt(4)\n"
"v_mfma_f32_32x32x16_bf16 v[48:63], v[8:11], v[188:191], v[48:63]\n"
"ds_read_b128 v[8:11], v0 offset:55296\n"
"v_mfma_f32_32x32x16_bf16 v[48:63], v[12:15], v[192:195], v[48:63]\n"
"ds_read_b128 v[12:15], v0 offset:56320\n"
"s_waitcnt lgkmcnt(4)\n"
"v_mfma_f32_32x32x16_bf16 v[48:63], v[16:19], v[196:199], v[48:63]\n"
"ds_read_b128 v[16:19], v0 offset:57344\n"
"s_add_u32 m0, s45, 4096\n"
"s_add_u32 s44, s45, 667648\n"
"buffer_load_dwordx4 v0, s[40:43], s44 offen offset:0 lds\n"
"v_cvt_pk_bf16_f32 v96, v32, v33\n"
"v_cvt_pk_bf16_f32 v97, v34, v35\n"
"v_cvt_pk_bf16_f32 v98, v36, v37\n"
"v_mfma_f32_32x32x16_bf16 v[48:63], v[20:23], v[200:203], v[48:63]\n"
"ds_read_b128 v[20:23], v0 offset:58368\n"
"v_cvt_pk_bf16_f32 v99, v38, v39\n"
"global_store_dwordx4 v0, v[96:99], s[50:51] offset:0 sc1\n"
"s_waitcnt lgkmcnt(4)\n"
"v_mfma_f32_32x32x16_bf16 v[48:63], v[24:27], v[204:207], v[48:63]\n"
"ds_read_b128 v[24:27], v0 offset:59392\n"
"v_cvt_pk_bf16_f32 v100, v40, v41\n"
"v_cvt_pk_bf16_f32 v101, v42, v43\n"
"v_cvt_pk_bf16_f32 v102, v44, v45\n"
"v_mfma_f32_32x32x16_bf16 v[48:63], v[28:31], v[208:211], v[48:63]\n"
"ds_read_b128 v[28:31], v0 offset:60416\n"
"buffer_load_dwordx4 v0, s[40:43], s44 offen offset:1024 lds\n"
"v_cvt_pk_bf16_f32 v103, v46, v47\n"
"global_store_dwordx4 v0, v[100:103], s[50:51] offset:1024 sc1\n"
"s_waitcnt lgkmcnt(4)\n"
"v_mfma_f32_32x32x16_bf16 v[32:47], v[8:11], v[176:179], 0\n"
"ds_read_b128 v[8:11], v0 offset:61440\n"
"v_mfma_f32_32x32x16_bf16 v[32:47], v[12:15], v[180:183], v[32:47]\n"
"ds_read_b128 v[12:15], v0 offset:62464\n"
"s_waitcnt lgkmcnt(4)\n"
"v_mfma_f32_32x32x16_bf16 v[32:47], v[16:19], v[184:187], v[32:47]\n"
"ds_read_b128 v[16:19], v0 offset:63488\n"
"buffer_load_dwordx4 v0, s[40:43], s44 offen offset:2048 lds\n"
"v_mfma_f32_32x32x16_bf16 v[32:47], v[20:23], v[188:191], v[32:47]\n"
"ds_read_b128 v[20:23], v0 offset:64512\n"
"s_waitcnt lgkmcnt(4)\n"
"v_mfma_f32_32x32x16_bf16 v[32:47], v[24:27], v[192:195], v[32:47]\n"
"ds_read_b128 v[24:27], v1 offset:0\n"
"v_mfma_f32_32x32x16_bf16 v[32:47], v[28:31], v[196:199], v[32:47]\n"
"ds_read_b128 v[28:31], v1 offset:1024\n"
"buffer_load_dwordx4 v0, s[40:43], s44 offen offset:3072 lds\n"
"v_cvt_pk_bf16_f32 v104, v48, v49\n"
"v_cvt_pk_bf16_f32 v105, v50, v51\n"
"v_cvt_pk_bf16_f32 v106, v52, v53\n"
"s_waitcnt lgkmcnt(4)\n"
"v_mfma_f32_32x32x16_bf16 v[32:47], v[8:11], v[200:203], v[32:47]\n"
"ds_read_b128 v[8:11], v1 offset:2048\n"
"v_cvt_pk_bf16_f32 v107, v54, v55\n"
"global_store_dwordx4 v0, v[104:107], s[50:51] offset:2048 sc1\n"
"v_mfma_f32_32x32x16_bf16 v[32:47], v[12:15], v[204:207], v[32:47]\n"
"ds_read_b128 v[12:15], v1 offset:3072\n"
"v_cvt_pk_bf16_f32 v108, v56, v57\n"
"v_cvt_pk_bf16_f32 v109, v58, v59\n"
"v_cvt_pk_bf16_f32 v110, v60, v61\n"
"s_waitcnt lgkmcnt(4)\n"
"v_mfma_f32_32x32x16_bf16 v[32:47], v[16:19], v[208:211], v[32:47]\n"
"ds_read_b128 v[16:19], v1 offset:4096\n"
"s_add_u32 m0, s45, 8192\n"
"s_add_u32 s44, s45, 671744\n"
"buffer_load_dwordx4 v0, s[40:43], s44 offen offset:0 lds\n"
"v_cvt_pk_bf16_f32 v111, v62, v63\n"
"global_store_dwordx4 v0, v[108:111], s[50:51] offset:3072 sc1\n"
"s_add_u32 s50, s50, 4096\n"
"s_addc_u32 s51, s51, 0\n"
"v_mfma_f32_32x32x16_bf16 v[48:63], v[20:23], v[176:179], 0\n"
"ds_read_b128 v[20:23], v1 offset:5120\n"
"s_waitcnt lgkmcnt(4)\n"
"v_mfma_f32_32x32x16_bf16 v[48:63], v[24:27], v[180:183], v[48:63]\n"
"ds_read_b128 v[24:27], v1 offset:6144\n"
"v_mfma_f32_32x32x16_bf16 v[48:63], v[28:31], v[184:187], v[48:63]\n"
"ds_read_b128 v[28:31], v1 offset:7168\n"
"s_waitcnt lgkmcnt(4)\n"
"v_mfma_f32_32x32x16_bf16 v[48:63], v[8:11], v[188:191], v[48:63]\n"
"s_barrier\n"
"ds_read_b128 v[8:11], v1 offset:8192\n"
"v_mfma_f32_32x32x16_bf16 v[48:63], v[12:15], v[192:195], v[48:63]\n"
"ds_read_b128 v[12:15], v1 offset:9216\n"
"s_waitcnt lgkmcnt(4)\n"
"v_mfma_f32_32x32x16_bf16 v[48:63], v[16:19], v[196:199], v[48:63]\n"
"ds_read_b128 v[16:19], v1 offset:10240\n"
"v_cvt_pk_bf16_f32 v64, v32, v33\n"
"v_cvt_pk_bf16_f32 v65, v34, v35\n"
"v_cvt_pk_bf16_f32 v66, v36, v37\n"
"v_mfma_f32_32x32x16_bf16 v[48:63], v[20:23], v[200:203], v[48:63]\n"
"ds_read_b128 v[20:23], v1 offset:11264\n"
"v_cvt_pk_bf16_f32 v67, v38, v39\n"
"global_store_dwordx4 v0, v[64:67], s[50:51] offset:0 sc1\n"
"s_waitcnt lgkmcnt(4)\n"
"v_mfma_f32_32x32x16_bf16 v[48:63], v[24:27], v[204:207], v[48:63]\n"
"ds_read_b128 v[24:27], v1 offset:12288\n"
"v_cvt_pk_bf16_f32 v68, v40, v41\n"
"v_cvt_pk_bf16_f32 v69, v42, v43\n"
"v_cvt_pk_bf16_f32 v70, v44, v45\n"
"v_mfma_f32_32x32x16_bf16 v[48:63], v[28:31], v[208:211], v[48:63]\n"
"ds_read_b128 v[28:31], v1 offset:13312\n"
"v_cvt_pk_bf16_f32 v71, v46, v47\n"
"global_store_dwordx4 v0, v[68:71], s[50:51] offset:1024 sc1\n"
"s_waitcnt lgkmcnt(4)\n"
"v_mfma_f32_32x32x16_bf16 v[32:47], v[8:11], v[176:179], 0\n"
"ds_read_b128 v[8:11], v1 offset:14336\n"
"v_mfma_f32_32x32x16_bf16 v[32:47], v[12:15], v[180:183], v[32:47]\n"
"ds_read_b128 v[12:15], v1 offset:15360\n"
"s_waitcnt lgkmcnt(4)\n"
"v_mfma_f32_32x32x16_bf16 v[32:47], v[16:19], v[184:187], v[32:47]\n"
"ds_read_b128 v[16:19], v1 offset:16384\n"
"s_add_u32 m0, s45, 36864\n"
"s_add_u32 s44, s45, 700416\n"
"buffer_load_dwordx4 v0, s[40:43], s44 offen offset:0 lds\n"
"v_mfma_f32_32x32x16_bf16 v[32:47], v[20:23], v[188:191], v[32:47]\n"
"ds_read_b128 v[20:23], v1 offset:17408\n"
"s_waitcnt lgkmcnt(4)\n"
"v_mfma_f32_32x32x16_bf16 v[32:47], v[24:27], v[192:195], v[32:47]\n"
"ds_read_b128 v[24:27], v1 offset:18432\n"
"v_mfma_f32_32x32x16_bf16 v[32:47], v[28:31], v[196:199], v[32:47]\n"
"ds_read_b128 v[28:31], v1 offset:19456\n"
"buffer_load_dwordx4 v0, s[40:43], s44 offen offset:1024 lds\n"
"v_cvt_pk_bf16_f32 v72, v48, v49\n"
"v_cvt_pk_bf16_f32 v73, v50, v51\n"
"v_cvt_pk_bf16_f32 v74, v52, v53\n"
"s_waitcnt lgkmcnt(4)\n"
"v_mfma_f32_32x32x16_bf16 v[32:47], v[8:11], v[200:203], v[32:47]\n"
"ds_read_b128 v[8:11], v1 offset:20480\n"
"v_cvt_pk_bf16_f32 v75, v54, v55\n"
"global_store_dwordx4 v0, v[72:75], s[50:51] offset:2048 sc1\n"
"v_mfma_f32_32x32x16_bf16 v[32:47], v[12:15], v[204:207], v[32:47]\n"
"ds_read_b128 v[12:15], v1 offset:21504\n"
"v_cvt_pk_bf16_f32 v76, v56, v57\n"
"v_cvt_pk_bf16_f32 v77, v58, v59\n"
"v_cvt_pk_bf16_f32 v78, v60, v61\n"
"s_waitcnt lgkmcnt(4)\n"
"v_mfma_f32_32x32x16_bf16 v[32:47], v[16:19], v[208:211], v[32:47]\n"
"ds_read_b128 v[16:19], v1 offset:22528\n"
"buffer_load_dwordx4 v0, s[40:43], s44 offen offset:2048 lds\n"
"v_cvt_pk_bf16_f32 v79, v62, v63\n"
"global_store_dwordx4 v0, v[76:79], s[50:51] offset:3072 sc1\n"
"s_add_u32 s50, s50, 4096\n"
"s_addc_u32 s51, s51, 0\n"
"v_mfma_f32_32x32x16_bf16 v[48:63], v[20:23], v[176:179], 0\n"
"ds_read_b128 v[20:23], v1 offset:23552\n"
"s_waitcnt lgkmcnt(4)\n"
"v_mfma_f32_32x32x16_bf16 v[48:63], v[24:27], v[180:183], v[48:63]\n"
"ds_read_b128 v[24:27], v1 offset:24576\n"
"v_mfma_f32_32x32x16_bf16 v[48:63], v[28:31], v[184:187], v[48:63]\n"
"ds_read_b128 v[28:31], v1 offset:25600\n"
"buffer_load_dwordx4 v0, s[40:43], s44 offen offset:3072 lds\n"
"s_waitcnt lgkmcnt(4)\n"
"v_mfma_f32_32x32x16_bf16 v[48:63], v[8:11], v[188:191], v[48:63]\n"
"ds_read_b128 v[8:11], v1 offset:26624\n"
"v_mfma_f32_32x32x16_bf16 v[48:63], v[12:15], v[192:195], v[48:63]\n"
"ds_read_b128 v[12:15], v1 offset:27648\n"
"s_waitcnt lgkmcnt(4)\n"
"v_mfma_f32_32x32x16_bf16 v[48:63], v[16:19], v[196:199], v[48:63]\n"
"ds_read_b128 v[16:19], v1 offset:28672\n"
"s_add_u32 m0, s45, 40960\n"
"s_add_u32 s44, s45, 704512\n"
"buffer_load_dwordx4 v0, s[40:43], s44 offen offset:0 lds\n"
"v_cvt_pk_bf16_f32 v80, v32, v33\n"
"v_cvt_pk_bf16_f32 v81, v34, v35\n"
"v_cvt_pk_bf16_f32 v82, v36, v37\n"
"v_mfma_f32_32x32x16_bf16 v[48:63], v[20:23], v[200:203], v[48:63]\n"
"ds_read_b128 v[20:23], v1 offset:29696\n"
"v_cvt_pk_bf16_f32 v83, v38, v39\n"
"global_store_dwordx4 v0, v[80:83], s[50:51] offset:0 sc1\n"
"s_waitcnt lgkmcnt(4)\n"
"v_mfma_f32_32x32x16_bf16 v[48:63], v[24:27], v[204:207], v[48:63]\n"
"ds_read_b128 v[24:27], v1 offset:30720\n"
"v_cvt_pk_bf16_f32 v84, v40, v41\n"
"v_cvt_pk_bf16_f32 v85, v42, v43\n"
"v_cvt_pk_bf16_f32 v86, v44, v45\n"
"v_mfma_f32_32x32x16_bf16 v[48:63], v[28:31], v[208:211], v[48:63]\n"
"ds_read_b128 v[28:31], v1 offset:31744\n"
"buffer_load_dwordx4 v0, s[40:43], s44 offen offset:1024 lds\n"
"v_cvt_pk_bf16_f32 v87, v46, v47\n"
"global_store_dwordx4 v0, v[84:87], s[50:51] offset:1024 sc1\n"
"s_waitcnt lgkmcnt(4)\n"
"v_mfma_f32_32x32x16_bf16 v[32:47], v[8:11], v[176:179], 0\n"
"ds_read_b128 v[8:11], v1 offset:32768\n"
"v_mfma_f32_32x32x16_bf16 v[32:47], v[12:15], v[180:183], v[32:47]\n"
"ds_read_b128 v[12:15], v1 offset:33792\n"
"s_waitcnt lgkmcnt(4)\n"
"v_mfma_f32_32x32x16_bf16 v[32:47], v[16:19], v[184:187], v[32:47]\n"
"ds_read_b128 v[16:19], v1 offset:34816\n"
"buffer_load_dwordx4 v0, s[40:43], s44 offen offset:2048 lds\n"
"v_mfma_f32_32x32x16_bf16 v[32:47], v[20:23], v[188:191], v[32:47]\n"
"ds_read_b128 v[20:23], v1 offset:35840\n"
"s_waitcnt lgkmcnt(4)\n"
"v_mfma_f32_32x32x16_bf16 v[32:47], v[24:27], v[192:195], v[32:47]\n"
"ds_read_b128 v[24:27], v1 offset:36864\n"
"v_mfma_f32_32x32x16_bf16 v[32:47], v[28:31], v[196:199], v[32:47]\n"
"ds_read_b128 v[28:31], v1 offset:37888\n"
"buffer_load_dwordx4 v0, s[40:43], s44 offen offset:3072 lds\n"
"v_cvt_pk_bf16_f32 v88, v48, v49\n"
"v_cvt_pk_bf16_f32 v89, v50, v51\n"
"v_cvt_pk_bf16_f32 v90, v52, v53\n"
"s_waitcnt lgkmcnt(4)\n"
"v_mfma_f32_32x32x16_bf16 v[32:47], v[8:11], v[200:203], v[32:47]\n"
"ds_read_b128 v[8:11], v1 offset:38912\n"
"v_cvt_pk_bf16_f32 v91, v54, v55\n"
"global_store_dwordx4 v0, v[88:91], s[50:51] offset:2048 sc1\n"
"v_mfma_f32_32x32x16_bf16 v[32:47], v[12:15], v[204:207], v[32:47]\n"
"ds_read_b128 v[12:15], v1 offset:39936\n"
"v_cvt_pk_bf16_f32 v92, v56, v57\n"
"v_cvt_pk_bf16_f32 v93, v58, v59\n"
"v_cvt_pk_bf16_f32 v94, v60, v61\n"
"s_waitcnt lgkmcnt(4)\n"
"v_mfma_f32_32x32x16_bf16 v[32:47], v[16:19], v[208:211], v[32:47]\n"
"ds_read_b128 v[16:19], v1 offset:40960\n"
"s_add_u32 m0, s45, 45056\n"
"s_add_u32 s44, s45, 708608\n"
"buffer_load_dwordx4 v0, s[40:43], s44 offen offset:0 lds\n"
"v_cvt_pk_bf16_f32 v95, v62, v63\n"
"global_store_dwordx4 v0, v[92:95], s[50:51] offset:3072 sc1\n"
"s_add_u32 s50, s50, 4096\n"
"s_addc_u32 s51, s51, 0\n"
"v_mfma_f32_32x32x16_bf16 v[48:63], v[20:23], v[176:179], 0\n"
"ds_read_b128 v[20:23], v1 offset:41984\n"
"s_waitcnt lgkmcnt(4)\n"
"v_mfma_f32_32x32x16_bf16 v[48:63], v[24:27], v[180:183], v[48:63]\n"
"ds_read_b128 v[24:27], v1 offset:43008\n"
"v_mfma_f32_32x32x16_bf16 v[48:63], v[28:31], v[184:187], v[48:63]\n"
"ds_read_b128 v[28:31], v1 offset:44032\n"
"s_waitcnt lgkmcnt(4)\n"
"v_mfma_f32_32x32x16_bf16 v[48:63], v[8:11], v[188:191], v[48:63]\n"
"s_waitcnt vmcnt(18)\n"
"s_barrier\n"
"ds_read_b128 v[8:11], v0 offset:0\n"
"v_mfma_f32_32x32x16_bf16 v[48:63], v[12:15], v[192:195], v[48:63]\n"
"ds_read_b128 v[12:15], v0 offset:1024\n"
"s_waitcnt lgkmcnt(4)\n"
"v_mfma_f32_32x32x16_bf16 v[48:63], v[16:19], v[196:199], v[48:63]\n"
"ds_read_b128 v[16:19], v0 offset:2048\n"
"v_cvt_pk_bf16_f32 v96, v32, v33\n"
"v_cvt_pk_bf16_f32 v97, v34, v35\n"
"v_cvt_pk_bf16_f32 v98, v36, v37\n"
"v_mfma_f32_32x32x16_bf16 v[48:63], v[20:23], v[200:203], v[48:63]\n"
"ds_read_b128 v[20:23], v0 offset:3072\n"
"v_cvt_pk_bf16_f32 v99, v38, v39\n"
"global_store_dwordx4 v0, v[96:99], s[50:51] offset:0 sc1\n"
"s_waitcnt lgkmcnt(4)\n"
"v_mfma_f32_32x32x16_bf16 v[48:63], v[24:27], v[204:207], v[48:63]\n"
"ds_read_b128 v[24:27], v0 offset:4096\n"
"v_cvt_pk_bf16_f32 v100, v40, v41\n"
"v_cvt_pk_bf16_f32 v101, v42, v43\n"
"v_cvt_pk_bf16_f32 v102, v44, v45\n"
"v_mfma_f32_32x32x16_bf16 v[48:63], v[28:31], v[208:211], v[48:63]\n"
"ds_read_b128 v[28:31], v0 offset:5120\n"
"v_cvt_pk_bf16_f32 v103, v46, v47\n"
"global_store_dwordx4 v0, v[100:103], s[50:51] offset:1024 sc1\n"
"s_waitcnt lgkmcnt(4)\n"
"v_mfma_f32_32x32x16_bf16 v[32:47], v[8:11], v[176:179], 0\n"
"ds_read_b128 v[8:11], v0 offset:6144\n"
"v_mfma_f32_32x32x16_bf16 v[32:47], v[12:15], v[180:183], v[32:47]\n"
"ds_read_b128 v[12:15], v0 offset:7168\n"
"s_waitcnt lgkmcnt(4)\n"
"v_mfma_f32_32x32x16_bf16 v[32:47], v[16:19], v[184:187], v[32:47]\n"
"ds_read_b128 v[16:19], v0 offset:8192\n"
"v_mfma_f32_32x32x16_bf16 v[32:47], v[20:23], v[188:191], v[32:47]\n"
"ds_read_b128 v[20:23], v0 offset:9216\n"
"s_waitcnt lgkmcnt(4)\n"
"v_mfma_f32_32x32x16_bf16 v[32:47], v[24:27], v[192:195], v[32:47]\n"
"ds_read_b128 v[24:27], v0 offset:10240\n"
"v_mfma_f32_32x32x16_bf16 v[32:47], v[28:31], v[196:199], v[32:47]\n"
"ds_read_b128 v[28:31], v0 offset:11264\n"
"v_cvt_pk_bf16_f32 v104, v48, v49\n"
"v_cvt_pk_bf16_f32 v105, v50, v51\n"
"v_cvt_pk_bf16_f32 v106, v52, v53\n"
"s_waitcnt lgkmcnt(4)\n"
"v_mfma_f32_32x32x16_bf16 v[32:47], v[8:11], v[200:203], v[32:47]\n"
"ds_read_b128 v[8:11], v0 offset:12288\n"
"v_cvt_pk_bf16_f32 v107, v54, v55\n"
"global_store_dwordx4 v0, v[104:107], s[50:51] offset:2048 sc1\n"
"v_mfma_f32_32x32x16_bf16 v[32:47], v[12:15], v[204:207], v[32:47]\n"
"ds_read_b128 v[12:15], v0 offset:13312\n"
"v_cvt_pk_bf16_f32 v108, v56, v57\n"
"v_cvt_pk_bf16_f32 v109, v58, v59\n"
"v_cvt_pk_bf16_f32 v110, v60, v61\n"
"s_waitcnt lgkmcnt(4)\n"
"v_mfma_f32_32x32x16_bf16 v[32:47], v[16:19], v[208:211], v[32:47]\n"
"ds_read_b128 v[16:19], v0 offset:14336\n"
"v_cvt_pk_bf16_f32 v111, v62, v63\n"
"global_store_dwordx4 v0, v[108:111], s[50:51] offset:3072 sc1\n"
"s_add_u32 s50, s50, 4096\n"
"s_addc_u32 s51, s51, 0\n"
"v_mfma_f32_32x32x16_bf16 v[48:63], v[20:23], v[176:179], 0\n"
"ds_read_b128 v[20:23], v0 offset:15360\n"
"s_waitcnt lgkmcnt(4)\n"
"v_mfma_f32_32x32x16_bf16 v[48:63], v[24:27], v[180:183], v[48:63]\n"
"ds_read_b128 v[24:27], v0 offset:16384\n"
"v_mfma_f32_32x32x16_bf16 v[48:63], v[28:31], v[184:187], v[48:63]\n"
"ds_read_b128 v[28:31], v0 offset:17408\n"
"s_waitcnt lgkmcnt(4)\n"
"v_mfma_f32_32x32x16_bf16 v[48:63], v[8:11], v[188:191], v[48:63]\n"
"ds_read_b128 v[8:11], v0 offset:18432\n"
"v_mfma_f32_32x32x16_bf16 v[48:63], v[12:15], v[192:195], v[48:63]\n"
"ds_read_b128 v[12:15], v0 offset:19456\n"
"s_waitcnt lgkmcnt(4)\n"
"v_mfma_f32_32x32x16_bf16 v[48:63], v[16:19], v[196:199], v[48:63]\n"
"ds_read_b128 v[16:19], v0 offset:20480\n"
"v_cvt_pk_bf16_f32 v64, v32, v33\n"
"v_cvt_pk_bf16_f32 v65, v34, v35\n"
"v_cvt_pk_bf16_f32 v66, v36, v37\n"
"v_mfma_f32_32x32x16_bf16 v[48:63], v[20:23], v[200:203], v[48:63]\n"
"ds_read_b128 v[20:23], v0 offset:21504\n"
"v_cvt_pk_bf16_f32 v67, v38, v39\n"
"global_store_dwordx4 v0, v[64:67], s[50:51] offset:0 sc1\n"
"s_waitcnt lgkmcnt(4)\n"
"v_mfma_f32_32x32x16_bf16 v[48:63], v[24:27], v[204:207], v[48:63]\n"
"ds_read_b128 v[24:27], v0 offset:22528\n"
"v_cvt_pk_bf16_f32 v68, v40, v41\n"
"v_cvt_pk_bf16_f32 v69, v42, v43\n"
"v_cvt_pk_bf16_f32 v70, v44, v45\n"
"v_mfma_f32_32x32x16_bf16 v[48:63], v[28:31], v[208:211], v[48:63]\n"
"ds_read_b128 v[28:31], v0 offset:23552\n"
"v_cvt_pk_bf16_f32 v71, v46, v47\n"
"global_store_dwordx4 v0, v[68:71], s[50:51] offset:1024 sc1\n"
"s_waitcnt lgkmcnt(4)\n"
"v_mfma_f32_32x32x16_bf16 v[32:47], v[8:11], v[176:179], 0\n"
"ds_read_b128 v[8:11], v0 offset:24576\n"
"v_mfma_f32_32x32x16_bf16 v[32:47], v[12:15], v[180:183], v[32:47]\n"
"ds_read_b128 v[12:15], v0 offset:25600\n"
"s_waitcnt lgkmcnt(4)\n"
"v_mfma_f32_32x32x16_bf16 v[32:47], v[16:19], v[184:187], v[32:47]\n"
"ds_read_b128 v[16:19], v0 offset:26624\n"
"v_mfma_f32_32x32x16_bf16 v[32:47], v[20:23], v[188:191], v[32:47]\n"
"ds_read_b128 v[20:23], v0 offset:27648\n"
"s_waitcnt lgkmcnt(4)\n"
"v_mfma_f32_32x32x16_bf16 v[32:47], v[24:27], v[192:195], v[32:47]\n"
"ds_read_b128 v[24:27], v0 offset:28672\n"
"v_mfma_f32_32x32x16_bf16 v[32:47], v[28:31], v[196:199], v[32:47]\n"
"ds_read_b128 v[28:31], v0 offset:29696\n"
"v_cvt_pk_bf16_f32 v72, v48, v49\n"
"v_cvt_pk_bf16_f32 v73, v50, v51\n"
"v_cvt_pk_bf16_f32 v74, v52, v53\n"
"s_waitcnt lgkmcnt(4)\n"
"v_mfma_f32_32x32x16_bf16 v[32:47], v[8:11], v[200:203], v[32:47]\n"
"ds_read_b128 v[8:11], v0 offset:30720\n"
"v_cvt_pk_bf16_f32 v75, v54, v55\n"
"global_store_dwordx4 v0, v[72:75], s[50:51] offset:2048 sc1\n"
"v_mfma_f32_32x32x16_bf16 v[32:47], v[12:15], v[204:207], v[32:47]\n"
"ds_read_b128 v[12:15], v0 offset:31744\n"
"v_cvt_pk_bf16_f32 v76, v56, v57\n"
"v_cvt_pk_bf16_f32 v77, v58, v59\n"
"v_cvt_pk_bf16_f32 v78, v60, v61\n"
"s_waitcnt lgkmcnt(4)\n"
"v_mfma_f32_32x32x16_bf16 v[32:47], v[16:19], v[208:211], v[32:47]\n"
"ds_read_b128 v[16:19], v0 offset:32768\n"
"v_cvt_pk_bf16_f32 v79, v62, v63\n"
"global_store_dwordx4 v0, v[76:79], s[50:51] offset:3072 sc1\n"
"s_add_u32 s50, s50, 4096\n"
"s_addc_u32 s51, s51, 0\n"
"v_mfma_f32_32x32x16_bf16 v[48:63], v[20:23], v[176:179], 0\n"
"ds_read_b128 v[20:23], v0 offset:33792\n"
"s_waitcnt lgkmcnt(4)\n"
"v_mfma_f32_32x32x16_bf16 v[48:63], v[24:27], v[180:183], v[48:63]\n"
"ds_read_b128 v[24:27], v0 offset:34816\n"
"v_mfma_f32_32x32x16_bf16 v[48:63], v[28:31], v[184:187], v[48:63]\n"
"ds_read_b128 v[28:31], v0 offset:35840\n"
"s_waitcnt lgkmcnt(4)\n"
"v_mfma_f32_32x32x16_bf16 v[48:63], v[8:11], v[188:191], v[48:63]\n"
"s_waitcnt vmcnt(9)\n"
"s_barrier\n"
"ds_read_b128 v[8:11], v0 offset:36864\n"
"v_mfma_f32_32x32x16_bf16 v[48:63], v[12:15], v[192:195], v[48:63]\n"
"ds_read_b128 v[12:15], v0 offset:37888\n"
"s_waitcnt lgkmcnt(4)\n"
"v_mfma_f32_32x32x16_bf16 v[48:63], v[16:19], v[196:199], v[48:63]\n"
"ds_read_b128 v[16:19], v0 offset:38912\n"
"v_cvt_pk_bf16_f32 v80, v32, v33\n"
"v_cvt_pk_bf16_f32 v81, v34, v35\n"
"v_cvt_pk_bf16_f32 v82, v36, v37\n"
"v_mfma_f32_32x32x16_bf16 v[48:63], v[20:23], v[200:203], v[48:63]\n"
"ds_read_b128 v[20:23], v0 offset:39936\n"
"v_cvt_pk_bf16_f32 v83, v38, v39\n"
"global_store_dwordx4 v0, v[80:83], s[50:51] offset:0 sc1\n"
"s_waitcnt lgkmcnt(4)\n"
"v_mfma_f32_32x32x16_bf16 v[48:63], v[24:27], v[204:207], v[48:63]\n"
"ds_read_b128 v[24:27], v0 offset:40960\n"
"v_cvt_pk_bf16_f32 v84, v40, v41\n"
"v_cvt_pk_bf16_f32 v85, v42, v43\n"
"v_cvt_pk_bf16_f32 v86, v44, v45\n"
"v_mfma_f32_32x32x16_bf16 v[48:63], v[28:31], v[208:211], v[48:63]\n"
"ds_read_b128 v[28:31], v0 offset:41984\n"
"v_cvt_pk_bf16_f32 v87, v46, v47\n"
"global_store_dwordx4 v0, v[84:87], s[50:51] offset:1024 sc1\n"
"s_waitcnt lgkmcnt(4)\n"
"v_mfma_f32_32x32x16_bf16 v[32:47], v[8:11], v[176:179], 0\n"
"ds_read_b128 v[8:11], v0 offset:43008\n"
"v_mfma_f32_32x32x16_bf16 v[32:47], v[12:15], v[180:183], v[32:47]\n"
"ds_read_b128 v[12:15], v0 offset:44032\n"
"s_waitcnt lgkmcnt(4)\n"
"v_mfma_f32_32x32x16_bf16 v[32:47], v[16:19], v[184:187], v[32:47]\n"
"ds_read_b128 v[16:19], v0 offset:45056\n"
"v_mfma_f32_32x32x16_bf16 v[32:47], v[20:23], v[188:191], v[32:47]\n"
"ds_read_b128 v[20:23], v0 offset:46080\n"
"s_waitcnt lgkmcnt(4)\n"
"v_mfma_f32_32x32x16_bf16 v[32:47], v[24:27], v[192:195], v[32:47]\n"
"ds_read_b128 v[24:27], v0 offset:47104\n"
"v_mfma_f32_32x32x16_bf16 v[32:47], v[28:31], v[196:199], v[32:47]\n"
"ds_read_b128 v[28:31], v0 offset:48128\n"
"v_cvt_pk_bf16_f32 v88, v48, v49\n"
"v_cvt_pk_bf16_f32 v89, v50, v51\n"
"v_cvt_pk_bf16_f32 v90, v52, v53\n"
"s_waitcnt lgkmcnt(4)\n"
"v_mfma_f32_32x32x16_bf16 v[32:47], v[8:11], v[200:203], v[32:47]\n"
"ds_read_b128 v[8:11], v0 offset:49152\n"
"v_cvt_pk_bf16_f32 v91, v54, v55\n"
"global_store_dwordx4 v0, v[88:91], s[50:51] offset:2048 sc1\n"
"v_mfma_f32_32x32x16_bf16 v[32:47], v[12:15], v[204:207], v[32:47]\n"
"ds_read_b128 v[12:15], v0 offset:50176\n"
"v_cvt_pk_bf16_f32 v92, v56, v57\n"
"v_cvt_pk_bf16_f32 v93, v58, v59\n"
"v_cvt_pk_bf16_f32 v94, v60, v61\n"
"s_waitcnt lgkmcnt(4)\n"
"v_mfma_f32_32x32x16_bf16 v[32:47], v[16:19], v[208:211], v[32:47]\n"
"ds_read_b128 v[16:19], v0 offset:51200\n"
"v_cvt_pk_bf16_f32 v95, v62, v63\n"
"global_store_dwordx4 v0, v[92:95], s[50:51] offset:3072 sc1\n"
"s_add_u32 s50, s50, 4096\n"
"s_addc_u32 s51, s51, 0\n"
"v_mfma_f32_32x32x16_bf16 v[48:63], v[20:23], v[176:179], 0\n"
"ds_read_b128 v[20:23], v0 offset:52224\n"
"s_waitcnt lgkmcnt(4)\n"
"v_mfma_f32_32x32x16_bf16 v[48:63], v[24:27], v[180:183], v[48:63]\n"
"ds_read_b128 v[24:27], v0 offset:53248\n"
"v_mfma_f32_32x32x16_bf16 v[48:63], v[28:31], v[184:187], v[48:63]\n"
"ds_read_b128 v[28:31], v0 offset:54272\n"
"s_waitcnt lgkmcnt(4)\n"
"v_mfma_f32_32x32x16_bf16 v[48:63], v[8:11], v[188:191], v[48:63]\n"
"v_mfma_f32_32x32x16_bf16 v[48:63], v[12:15], v[192:195], v[48:63]\n"
"s_waitcnt lgkmcnt(2)\n"
"v_mfma_f32_32x32x16_bf16 v[48:63], v[16:19], v[196:199], v[48:63]\n"
"v_cvt_pk_bf16_f32 v96, v32, v33\n"
"v_cvt_pk_bf16_f32 v97, v34, v35\n"
"v_cvt_pk_bf16_f32 v98, v36, v37\n"
"v_mfma_f32_32x32x16_bf16 v[48:63], v[20:23], v[200:203], v[48:63]\n"
"v_cvt_pk_bf16_f32 v99, v38, v39\n"
"global_store_dwordx4 v0, v[96:99], s[50:51] offset:0 sc1\n"
"s_waitcnt lgkmcnt(0)\n"
"v_mfma_f32_32x32x16_bf16 v[48:63], v[24:27], v[204:207], v[48:63]\n"
"v_cvt_pk_bf16_f32 v100, v40, v41\n"
"v_cvt_pk_bf16_f32 v101, v42, v43\n"
"v_cvt_pk_bf16_f32 v102, v44, v45\n"
"v_mfma_f32_32x32x16_bf16 v[48:63], v[28:31], v[208:211], v[48:63]\n"
"v_cvt_pk_bf16_f32 v103, v46, v47\n"
"global_store_dwordx4 v0, v[100:103], s[50:51] offset:1024 sc1\n"
"s_nop 7\n"
"s_nop 3\n"
"v_cvt_pk_bf16_f32 v104, v48, v49\n"
"v_cvt_pk_bf16_f32 v105, v50, v51\n"
"v_cvt_pk_bf16_f32 v106, v52, v53\n"
"v_cvt_pk_bf16_f32 v107, v54, v55\n"
"global_store_dwordx4 v0, v[104:107], s[50:51] offset:2048 sc1\n"
"v_cvt_pk_bf16_f32 v108, v56, v57\n"
"v_cvt_pk_bf16_f32 v109, v58, v59\n"
"v_cvt_pk_bf16_f32 v110, v60, v61\n"
"v_cvt_pk_bf16_f32 v111, v62, v63\n"
"global_store_dwordx4 v0, v[108:111], s[50:51] offset:3072 sc1\n"
"s_add_u32 s50, s50, 4096\n"
"s_addc_u32 s51, s51, 0\n"
"s_nop 1\n"
"v_mbcnt_lo_u32_b32 v0, -1, 0\n"
"v_mbcnt_hi_u32_b32 v0, -1, v0\n"
"v_and_b32 v1, 31, v0\n"
"v_mul_u32_u24 v1, 1152, v1\n"
"v_and_b32 v2, 32, v0\n"
"v_add_u32 v1, v1, v2\n"
"v_lshlrev_b32 v0, 4, v0\n"
"s_lshl_b32 s44, %15, 2\n"
"s_add_u32 s44, s44, %10\n"
"s_add_u32 s45, s44, 16\n"
"s_add_u32 s46, %15, 32\n"
"s_lshr_b32 s47, s44, 1\n"
"s_lshl_b32 s47, s47, 6\n"
"s_and_b32 s48, s44, 1\n"
"s_lshl_b32 s48, s48, 4\n"
"s_add_u32 s47, s47, s48\n"
"v_add_u32 v2, s47, v1\n"
"s_lshr_b32 s47, s45, 1\n"
"s_lshl_b32 s47, s47, 6\n"
"s_and_b32 s48, s45, 1\n"
"s_lshl_b32 s48, s48, 4\n"
"s_add_u32 s47, s47, s48\n"
"v_add_u32 v3, s47, v1\n"
"s_lshr_b32 s47, s46, 1\n"
"s_lshl_b32 s47, s47, 6\n"
"s_and_b32 s48, s46, 1\n"
"s_lshl_b32 s48, s48, 4\n"
"s_add_u32 s47, s47, s48\n"
"s_lshl_b32 s48, %10, 2\n"
"s_add_u32 s47, s47, s48\n"
"v_add_u32 v4, s47, v1\n"
"s_lshl_b32 s47, s44, 10\n"
"v_add_u32 v5, s47, v0\n"
"v_add_u32 v10, 0x4000, v5\n"
"s_lshl_b32 s47, s46, 10\n"
"s_add_u32 s48, s48, s47\n"
"v_add_u32 v6, s48, v0\n"
"s_waitcnt vmcnt(0)\n"
"v_mov_b32 v7, 0\n"
"v_mov_b32 v8, 1\n"
"s_mov_b64 exec, 1\n"
"global_atomic_add v7, v8, %14 offset:0 sc1\n"
"s_mov_b64 exec, -1\n"
"global_load_dwordx4 v[160:163], v2, %12\n"
"global_load_dwordx4 v[164:167], v3, %12\n"
"global_load_dword v168, v4, %12\n"
"s_mov_b32 s44, 0x3f80\n"
"s_mov_b32 s45, 0x3f800000\n"
"s_mov_b32 s46, 0x3ef5fdf0\n"
"s_cmp_lg_u32 %10, 0\n"
"s_cbranch_scc1 TGO0_%=\n"
"TSP0_%=:\n"
"global_load_dword v9, v7, %14 offset:0 sc1\n"
"s_waitcnt vmcnt(0)\n"
"v_readfirstlane_b32 s47, v9\n"
"s_cmp_ge_u32 s47, 64\n"
"s_cbranch_scc1 TGO0_%=\n"
"s_sleep 2\n"
"s_branch TSP0_%=\n"
"TGO0_%=:\n"
"s_barrier\n"
"s_mov_b64 s[40:41], %11\n"
"global_load_dwordx4 v[16:19], v5, s[40:41] sc1\n"
"s_add_u32 s40, s40, 2359296\n"
"s_addc_u32 s41, s41, 0\n"
"global_load_dwordx4 v[20:23], v5, s[40:41] sc1\n"
"s_add_u32 s40, s40, 2359296\n"
"s_addc_u32 s41, s41, 0\n"
"global_load_dwordx4 v[24:27], v5, s[40:41] sc1\n"
"s_add_u32 s40, s40, 2359296\n"
"s_addc_u32 s41, s41, 0\n"
"global_load_dwordx4 v[28:31], v5, s[40:41] sc1\n"
"s_add_u32 s40, s40, 2359296\n"
"s_addc_u32 s41, s41, 0\n"
"global_load_dwordx4 v[32:35], v5, s[40:41] sc1\n"
"s_add_u32 s40, s40, 2359296\n"
"s_addc_u32 s41, s41, 0\n"
"global_load_dwordx4 v[36:39], v5, s[40:41] sc1\n"
"s_add_u32 s40, s40, 2359296\n"
"s_addc_u32 s41, s41, 0\n"
"global_load_dwordx4 v[40:43], v5, s[40:41] sc1\n"
"s_add_u32 s40, s40, 2359296\n"
"s_addc_u32 s41, s41, 0\n"
"global_load_dwordx4 v[44:47], v5, s[40:41] sc1\n"
"s_add_u32 s40, s40, 2359296\n"
"s_addc_u32 s41, s41, 0\n"
"global_load_dwordx4 v[48:51], v5, s[40:41] sc1\n"
"s_add_u32 s40, s40, 2359296\n"
"s_addc_u32 s41, s41, 0\n"
"global_load_dwordx4 v[52:55], v5, s[40:41] sc1\n"
"s_add_u32 s40, s40, 2359296\n"
"s_addc_u32 s41, s41, 0\n"
"global_load_dwordx4 v[56:59], v5, s[40:41] sc1\n"
"s_add_u32 s40, s40, 2359296\n"
"s_addc_u32 s41, s41, 0\n"
"global_load_dwordx4 v[60:63], v5, s[40:41] sc1\n"
"s_add_u32 s40, s40, 2359296\n"
"s_addc_u32 s41, s41, 0\n"
"global_load_dwordx4 v[64:67], v5, s[40:41] sc1\n"
"s_add_u32 s40, s40, 2359296\n"
"s_addc_u32 s41, s41, 0\n"
"global_load_dwordx4 v[68:71], v5, s[40:41] sc1\n"
"s_add_u32 s40, s40, 2359296\n"
"s_addc_u32 s41, s41, 0\n"
"global_load_dwordx4 v[72:75], v5, s[40:41] sc1\n"
"s_add_u32 s40, s40, 2359296\n"
"s_addc_u32 s41, s41, 0\n"
"global_load_dwordx4 v[76:79], v5, s[40:41] sc1\n"
"s_mov_b64 s[40:41], %11\n"
"global_load_dwordx4 v[80:83], v10, s[40:41] sc1\n"
"s_add_u32 s40, s40, 2359296\n"
"s_addc_u32 s41, s41, 0\n"
"global_load_dwordx4 v[84:87], v10, s[40:41] sc1\n"
"s_add_u32 s40, s40, 2359296\n"
"s_addc_u32 s41, s41, 0\n"
"global_load_dwordx4 v[88:91], v10, s[40:41] sc1\n"
"s_add_u32 s40, s40, 2359296\n"
"s_addc_u32 s41, s41, 0\n"
"global_load_dwordx4 v[92:95], v10, s[40:41] sc1\n"
"s_add_u32 s40, s40, 2359296\n"
"s_addc_u32 s41, s41, 0\n"
"global_load_dwordx4 v[96:99], v10, s[40:41] sc1\n"
"s_add_u32 s40, s40, 2359296\n"
"s_addc_u32 s41, s41, 0\n"
"global_load_dwordx4 v[100:103], v10, s[40:41] sc1\n"
"s_add_u32 s40, s40, 2359296\n"
"s_addc_u32 s41, s41, 0\n"
"global_load_dwordx4 v[104:107], v10, s[40:41] sc1\n"
"s_add_u32 s40, s40, 2359296\n"
"s_addc_u32 s41, s41, 0\n"
"global_load_dwordx4 v[108:111], v10, s[40:41] sc1\n"
"s_add_u32 s40, s40, 2359296\n"
"s_addc_u32 s41, s41, 0\n"
"global_load_dwordx4 v[112:115], v10, s[40:41] sc1\n"
"s_add_u32 s40, s40, 2359296\n"
"s_addc_u32 s41, s41, 0\n"
"global_load_dwordx4 v[116:119], v10, s[40:41] sc1\n"
"s_add_u32 s40, s40, 2359296\n"
"s_addc_u32 s41, s41, 0\n"
"global_load_dwordx4 v[120:123], v10, s[40:41] sc1\n"
"s_add_u32 s40, s40, 2359296\n"
"s_addc_u32 s41, s41, 0\n"
"global_load_dwordx4 v[124:127], v10, s[40:41] sc1\n"
"s_add_u32 s40, s40, 2359296\n"
"s_addc_u32 s41, s41, 0\n"
"global_load_dwordx4 v[128:131], v10, s[40:41] sc1\n"
"s_add_u32 s40, s40, 2359296\n"
"s_addc_u32 s41, s41, 0\n"
"global_load_dwordx4 v[132:135], v10, s[40:41] sc1\n"
"s_add_u32 s40, s40, 2359296\n"
"s_addc_u32 s41, s41, 0\n"
"global_load_dwordx4 v[136:139], v10, s[40:41] sc1\n"
"s_add_u32 s40, s40, 2359296\n"
"s_addc_u32 s41, s41, 0\n"
"global_load_dwordx4 v[140:143], v10, s[40:41] sc1\n"
"s_mov_b64 s[40:41], %11\n"
"global_load_dword v144, v6, s[40:41] sc1\n"
"s_add_u32 s40, s40, 2359296\n"
"s_addc_u32 s41, s41, 0\n"
"global_load_dword v145, v6, s[40:41] sc1\n"
"s_add_u32 s40, s40, 2359296\n"
"s_addc_u32 s41, s41, 0\n"
"global_load_dword v146, v6, s[40:41] sc1\n"
"s_add_u32 s40, s40, 2359296\n"
"s_addc_u32 s41, s41, 0\n"
"global_load_dword v147, v6, s[40:41] sc1\n"
"s_add_u32 s40, s40, 2359296\n"
"s_addc_u32 s41, s41, 0\n"
"global_load_dword v148, v6, s[40:41] sc1\n"
"s_add_u32 s40, s40, 2359296\n"
"s_addc_u32 s41, s41, 0\n"
"global_load_dword v149, v6, s[40:41] sc1\n"
"s_add_u32 s40, s40, 2359296\n"
"s_addc_u32 s41, s41, 0\n"
"global_load_dword v150, v6, s[40:41] sc1\n"
"s_add_u32 s40, s40, 2359296\n"
"s_addc_u32 s41, s41, 0\n"
"global_load_dword v151, v6, s[40:41] sc1\n"
"s_add_u32 s40, s40, 2359296\n"
"s_addc_u32 s41, s41, 0\n"
"global_load_dword v152, v6, s[40:41] sc1\n"
"s_add_u32 s40, s40, 2359296\n"
"s_addc_u32 s41, s41, 0\n"
"global_load_dword v153, v6, s[40:41] sc1\n"
"s_add_u32 s40, s40, 2359296\n"
"s_addc_u32 s41, s41, 0\n"
"global_load_dword v154, v6, s[40:41] sc1\n"
"s_add_u32 s40, s40, 2359296\n"
"s_addc_u32 s41, s41, 0\n"
"global_load_dword v155, v6, s[40:41] sc1\n"
"s_add_u32 s40, s40, 2359296\n"
"s_addc_u32 s41, s41, 0\n"
"global_load_dword v156, v6, s[40:41] sc1\n"
"s_add_u32 s40, s40, 2359296\n"
"s_addc_u32 s41, s41, 0\n"
"global_load_dword v157, v6, s[40:41] sc1\n"
"s_add_u32 s40, s40, 2359296\n"
"s_addc_u32 s41, s41, 0\n"
"global_load_dword v158, v6, s[40:41] sc1\n"
"s_add_u32 s40, s40, 2359296\n"
"s_addc_u32 s41, s41, 0\n"
"global_load_dword v159, v6, s[40:41] sc1\n"
"s_waitcnt vmcnt(46)\n"
"v_dot2_f32_bf16 v208, v16, s44, 0\n"
"v_dot2_f32_bf16 v209, v16, s45, 0\n"
"v_dot2_f32_bf16 v210, v17, s44, 0\n"
"v_dot2_f32_bf16 v211, v17, s45, 0\n"
"v_dot2_f32_bf16 v212, v18, s44, 0\n"
"v_dot2_f32_bf16 v213, v18, s45, 0\n"
"v_dot2_f32_bf16 v214, v19, s44, 0\n"
"v_dot2_f32_bf16 v215, v19, s45, 0\n"
"v_dot2_f32_bf16 v208, v20, s44, v208\n"
"v_dot2_f32_bf16 v209, v20, s45, v209\n"
"v_dot2_f32_bf16 v210, v21, s44, v210\n"
"v_dot2_f32_bf16 v211, v21, s45, v211\n"
"v_dot2_f32_bf16 v212, v22, s44, v212\n"
"v_dot2_f32_bf16 v213, v22, s45, v213\n"
"v_dot2_f32_bf16 v214, v23, s44, v214\n"
"v_dot2_f32_bf16 v215, v23, s45, v215\n"
"s_waitcnt vmcnt(44)\n"
"v_dot2_f32_bf16 v208, v24, s44, v208\n"
"v_dot2_f32_bf16 v209, v24, s45, v209\n"
"v_dot2_f32_bf16 v210, v25, s44, v210\n"
"v_dot2_f32_bf16 v211, v25, s45, v211\n"
"v_dot2_f32_bf16 v212, v26, s44, v212\n"
"v_dot2_f32_bf16 v213, v26, s45, v213\n"
"v_dot2_f32_bf16 v214, v27, s44, v214\n"
"v_dot2_f32_bf16 v215, v27, s45, v215\n"
"v_dot2_f32_bf16 v208, v28, s44, v208\n"
"v_dot2_f32_bf16 v209, v28, s45, v209\n"
"v_dot2_f32_bf16 v210, v29, s44, v210\n"
"v_dot2_f32_bf16 v211, v29, s45, v211\n"
"v_dot2_f32_bf16 v212, v30, s44, v212\n"
"v_dot2_f32_bf16 v213, v30, s45, v213\n"
"v_dot2_f32_bf16 v214, v31, s44, v214\n"
"v_dot2_f32_bf16 v215, v31, s45, v215\n"
"s_waitcnt vmcnt(42)\n"
"v_dot2_f32_bf16 v208, v32, s44, v208\n"
"v_dot2_f32_bf16 v209, v32, s45, v209\n"
"v_dot2_f32_bf16 v210, v33, s44, v210\n"
"v_dot2_f32_bf16 v211, v33, s45, v211\n"
"v_dot2_f32_bf16 v212, v34, s44, v212\n"
"v_dot2_f32_bf16 v213, v34, s45, v213\n"
"v_dot2_f32_bf16 v214, v35, s44, v214\n"
"v_dot2_f32_bf16 v215, v35, s45, v215\n"
"v_dot2_f32_bf16 v208, v36, s44, v208\n"
"v_dot2_f32_bf16 v209, v36, s45, v209\n"
"v_dot2_f32_bf16 v210, v37, s44, v210\n"
"v_dot2_f32_bf16 v211, v37, s45, v211\n"
"v_dot2_f32_bf16 v212, v38, s44, v212\n"
"v_dot2_f32_bf16 v213, v38, s45, v213\n"
"v_dot2_f32_bf16 v214, v39, s44, v214\n"
"v_dot2_f32_bf16 v215, v39, s45, v215\n"
"s_waitcnt vmcnt(40)\n"
"v_dot2_f32_bf16 v208, v40, s44, v208\n"
"v_dot2_f32_bf16 v209, v40, s45, v209\n"
"v_dot2_f32_bf16 v210, v41, s44, v210\n"
"v_dot2_f32_bf16 v211, v41, s45, v211\n"
"v_dot2_f32_bf16 v212, v42, s44, v212\n"
"v_dot2_f32_bf16 v213, v42, s45, v213\n"
"v_dot2_f32_bf16 v214, v43, s44, v214\n"
"v_dot2_f32_bf16 v215, v43, s45, v215\n"
"v_dot2_f32_bf16 v208, v44, s44, v208\n"
"v_dot2_f32_bf16 v209, v44, s45, v209\n"
"v_dot2_f32_bf16 v210, v45, s44, v210\n"
"v_dot2_f32_bf16 v211, v45, s45, v211\n"
"v_dot2_f32_bf16 v212, v46, s44, v212\n"
"v_dot2_f32_bf16 v213, v46, s45, v213\n"
"v_dot2_f32_bf16 v214, v47, s44, v214\n"
"v_dot2_f32_bf16 v215, v47, s45, v215\n"
"s_waitcnt vmcnt(38)\n"
"v_dot2_f32_bf16 v208, v48, s44, v208\n"
"v_dot2_f32_bf16 v209, v48, s45, v209\n"
"v_dot2_f32_bf16 v210, v49, s44, v210\n"
"v_dot2_f32_bf16 v211, v49, s45, v211\n"
"v_dot2_f32_bf16 v212, v50, s44, v212\n"
"v_dot2_f32_bf16 v213, v50, s45, v213\n"
"v_dot2_f32_bf16 v214, v51, s44, v214\n"
"v_dot2_f32_bf16 v215, v51, s45, v215\n"
"v_dot2_f32_bf16 v208, v52, s44, v208\n"
"v_dot2_f32_bf16 v209, v52, s45, v209\n"
"v_dot2_f32_bf16 v210, v53, s44, v210\n"
"v_dot2_f32_bf16 v211, v53, s45, v211\n"
"v_dot2_f32_bf16 v212, v54, s44, v212\n"
"v_dot2_f32_bf16 v213, v54, s45, v213\n"
"v_dot2_f32_bf16 v214, v55, s44, v214\n"
"v_dot2_f32_bf16 v215, v55, s45, v215\n"
"s_waitcnt vmcnt(36)\n"
"v_dot2_f32_bf16 v208, v56, s44, v208\n"
"v_dot2_f32_bf16 v209, v56, s45, v209\n"
"v_dot2_f32_bf16 v210, v57, s44, v210\n"
"v_dot2_f32_bf16 v211, v57, s45, v211\n"
"v_dot2_f32_bf16 v212, v58, s44, v212\n"
"v_dot2_f32_bf16 v213, v58, s45, v213\n"
"v_dot2_f32_bf16 v214, v59, s44, v214\n"
"v_dot2_f32_bf16 v215, v59, s45, v215\n"
"v_dot2_f32_bf16 v208, v60, s44, v208\n"
"v_dot2_f32_bf16 v209, v60, s45, v209\n"
"v_dot2_f32_bf16 v210, v61, s44, v210\n"
"v_dot2_f32_bf16 v211, v61, s45, v211\n"
"v_dot2_f32_bf16 v212, v62, s44, v212\n"
"v_dot2_f32_bf16 v213, v62, s45, v213\n"
"v_dot2_f32_bf16 v214, v63, s44, v214\n"
"v_dot2_f32_bf16 v215, v63, s45, v215\n"
"s_waitcnt vmcnt(34)\n"
"v_dot2_f32_bf16 v180, v64, s44, 0\n"
"v_dot2_f32_bf16 v181, v64, s45, 0\n"
"v_dot2_f32_bf16 v182, v65, s44, 0\n"
"v_dot2_f32_bf16 v183, v65, s45, 0\n"
"v_dot2_f32_bf16 v184, v66, s44, 0\n"
"v_dot2_f32_bf16 v185, v66, s45, 0\n"
"v_dot2_f32_bf16 v186, v67, s44, 0\n"
"v_dot2_f32_bf16 v187, v67, s45, 0\n"
"v_dot2_f32_bf16 v180, v68, s44, v180\n"
"v_dot2_f32_bf16 v181, v68, s45, v181\n"
"v_dot2_f32_bf16 v182, v69, s44, v182\n"
"v_dot2_f32_bf16 v183, v69, s45, v183\n"
"v_dot2_f32_bf16 v184, v70, s44, v184\n"
"v_dot2_f32_bf16 v185, v70, s45, v185\n"
"v_dot2_f32_bf16 v186, v71, s44, v186\n"
"v_dot2_f32_bf16 v187, v71, s45, v187\n"
"s_waitcnt vmcnt(32)\n"
"v_dot2_f32_bf16 v180, v72, s44, v180\n"
"v_dot2_f32_bf16 v181, v72, s45, v181\n"
"v_dot2_f32_bf16 v182, v73, s44, v182\n"
"v_dot2_f32_bf16 v183, v73, s45, v183\n"
"v_dot2_f32_bf16 v184, v74, s44, v184\n"
"v_dot2_f32_bf16 v185, v74, s45, v185\n"
"v_dot2_f32_bf16 v186, v75, s44, v186\n"
"v_dot2_f32_bf16 v187, v75, s45, v187\n"
"v_dot2_f32_bf16 v180, v76, s44, v180\n"
"v_dot2_f32_bf16 v181, v76, s45, v181\n"
"v_dot2_f32_bf16 v182, v77, s44, v182\n"
"v_dot2_f32_bf16 v183, v77, s45, v183\n"
"v_dot2_f32_bf16 v184, v78, s44, v184\n"
"v_dot2_f32_bf16 v185, v78, s45, v185\n"
"v_dot2_f32_bf16 v186, v79, s44, v186\n"
"v_dot2_f32_bf16 v187, v79, s45, v187\n"
"s_nop 3\n"
"v_exp_f32 v188, -v208\n"
"v_exp_f32 v189, -v209\n"
"v_exp_f32 v190, -v210\n"
"v_exp_f32 v191, -v211\n"
"v_exp_f32 v192, -v212\n"
"v_exp_f32 v193, -v213\n"
"v_exp_f32 v194, -v214\n"
"v_exp_f32 v195, -v215\n"
"v_mul_f32 v208, v208, v180\n"
"v_mul_f32 v209, v209, v181\n"
"v_mul_f32 v210, v210, v182\n"
"v_mul_f32 v211, v211, v183\n"
"v_mul_f32 v212, v212, v184\n"
"v_mul_f32 v213, v213, v185\n"
"v_mul_f32 v214, v214, v186\n"
"v_mul_f32 v215, v215, v187\n"
"v_add_f32 v188, 1.0, v188\n"
"v_add_f32 v189, 1.0, v189\n"
"v_add_f32 v190, 1.0, v190\n"
"v_add_f32 v191, 1.0, v191\n"
"v_add_f32 v192, 1.0, v192\n"
"v_add_f32 v193, 1.0, v193\n"
"v_add_f32 v194, 1.0, v194\n"
"v_add_f32 v195, 1.0, v195\n"
"v_rcp_f32 v188, v188\n"
"v_rcp_f32 v189, v189\n"
"v_rcp_f32 v190, v190\n"
"v_rcp_f32 v191, v191\n"
"v_rcp_f32 v192, v192\n"
"v_rcp_f32 v193, v193\n"
"v_rcp_f32 v194, v194\n"
"v_rcp_f32 v195, v195\n"
"v_lshlrev_b32 v180, 16, v160\n"
"v_and_b32 v181, 0xffff0000, v160\n"
"v_lshlrev_b32 v182, 16, v161\n"
"v_and_b32 v183, 0xffff0000, v161\n"
"v_lshlrev_b32 v184, 16, v162\n"
"v_and_b32 v185, 0xffff0000, v162\n"
"v_lshlrev_b32 v186, 16, v163\n"
"v_and_b32 v187, 0xffff0000, v163\n"
"v_mul_f32 v208, v208, v188\n"
"v_mul_f32 v209, v209, v189\n"
"v_mul_f32 v210, v210, v190\n"
"v_mul_f32 v211, v211, v191\n"
"v_mul_f32 v212, v212, v192\n"
"v_mul_f32 v213, v213, v193\n"
"v_mul_f32 v214, v214, v194\n"
"v_mul_f32 v215, v215, v195\n"
"v_fma_f32 v208, v208, s46, v180\n"
"v_fma_f32 v209, v209, s46, v181\n"
"v_fma_f32 v210, v210, s46, v182\n"
"v_fma_f32 v211, v211, s46, v183\n"
"v_fma_f32 v212, v212, s46, v184\n"
"v_fma_f32 v213, v213, s46, v185\n"
"v_fma_f32 v214, v214, s46, v186\n"
"v_fma_f32 v215, v215, s46, v187\n"
"v_cvt_pk_bf16_f32 v196, v208, v209\n"
"v_cvt_pk_bf16_f32 v197, v210, v211\n"
"v_cvt_pk_bf16_f32 v198, v212, v213\n"
"v_cvt_pk_bf16_f32 v199, v214, v215\n"
"s_waitcnt vmcnt(30)\n"
"v_dot2_f32_bf16 v216, v80, s44, 0\n"
"v_dot2_f32_bf16 v217, v80, s45, 0\n"
"v_dot2_f32_bf16 v218, v81, s44, 0\n"
"v_dot2_f32_bf16 v219, v81, s45, 0\n"
"v_dot2_f32_bf16 v220, v82, s44, 0\n"
"v_dot2_f32_bf16 v221, v82, s45, 0\n"
"v_dot2_f32_bf16 v222, v83, s44, 0\n"
"v_dot2_f32_bf16 v223, v83, s45, 0\n"
"v_dot2_f32_bf16 v216, v84, s44, v216\n"
"v_dot2_f32_bf16 v217, v84, s45, v217\n"
"v_dot2_f32_bf16 v218, v85, s44, v218\n"
"v_dot2_f32_bf16 v219, v85, s45, v219\n"
"v_dot2_f32_bf16 v220, v86, s44, v220\n"
"v_dot2_f32_bf16 v221, v86, s45, v221\n"
"v_dot2_f32_bf16 v222, v87, s44, v222\n"
"v_dot2_f32_bf16 v223, v87, s45, v223\n"
"s_waitcnt vmcnt(28)\n"
"v_dot2_f32_bf16 v216, v88, s44, v216\n"
"v_dot2_f32_bf16 v217, v88, s45, v217\n"
"v_dot2_f32_bf16 v218, v89, s44, v218\n"
"v_dot2_f32_bf16 v219, v89, s45, v219\n"
"v_dot2_f32_bf16 v220, v90, s44, v220\n"
"v_dot2_f32_bf16 v221, v90, s45, v221\n"
"v_dot2_f32_bf16 v222, v91, s44, v222\n"
"v_dot2_f32_bf16 v223, v91, s45, v223\n"
"v_dot2_f32_bf16 v216, v92, s44, v216\n"
"v_dot2_f32_bf16 v217, v92, s45, v217\n"
"v_dot2_f32_bf16 v218, v93, s44, v218\n"
"v_dot2_f32_bf16 v219, v93, s45, v219\n"
"v_dot2_f32_bf16 v220, v94, s44, v220\n"
"v_dot2_f32_bf16 v221, v94, s45, v221\n"
"v_dot2_f32_bf16 v222, v95, s44, v222\n"
"v_dot2_f32_bf16 v223, v95, s45, v223\n"
"s_waitcnt vmcnt(26)\n"
"v_dot2_f32_bf16 v216, v96, s44, v216\n"
"v_dot2_f32_bf16 v217, v96, s45, v217\n"
"v_dot2_f32_bf16 v218, v97, s44, v218\n"
"v_dot2_f32_bf16 v219, v97, s45, v219\n"
"v_dot2_f32_bf16 v220, v98, s44, v220\n"
"v_dot2_f32_bf16 v221, v98, s45, v221\n"
"v_dot2_f32_bf16 v222, v99, s44, v222\n"
"v_dot2_f32_bf16 v223, v99, s45, v223\n"
"v_dot2_f32_bf16 v216, v100, s44, v216\n"
"v_dot2_f32_bf16 v217, v100, s45, v217\n"
"v_dot2_f32_bf16 v218, v101, s44, v218\n"
"v_dot2_f32_bf16 v219, v101, s45, v219\n"
"v_dot2_f32_bf16 v220, v102, s44, v220\n"
"v_dot2_f32_bf16 v221, v102, s45, v221\n"
"v_dot2_f32_bf16 v222, v103, s44, v222\n"
"v_dot2_f32_bf16 v223, v103, s45, v223\n"
"s_waitcnt vmcnt(24)\n"
"v_dot2_f32_bf16 v216, v104, s44, v216\n"
"v_dot2_f32_bf16 v217, v104, s45, v217\n"
"v_dot2_f32_bf16 v218, v105, s44, v218\n"
"v_dot2_f32_bf16 v219, v105, s45, v219\n"
"v_dot2_f32_bf16 v220, v106, s44, v220\n"
"v_dot2_f32_bf16 v221, v106, s45, v221\n"
"v_dot2_f32_bf16 v222, v107, s44, v222\n"
"v_dot2_f32_bf16 v223, v107, s45, v223\n"
"v_dot2_f32_bf16 v216, v108, s44, v216\n"
"v_dot2_f32_bf16 v217, v108, s45, v217\n"
"v_dot2_f32_bf16 v218, v109, s44, v218\n"
"v_dot2_f32_bf16 v219, v109, s45, v219\n"
"v_dot2_f32_bf16 v220, v110, s44, v220\n"
"v_dot2_f32_bf16 v221, v110, s45, v221\n"
"v_dot2_f32_bf16 v222, v111, s44, v222\n"
"v_dot2_f32_bf16 v223, v111, s45, v223\n"
"s_waitcnt vmcnt(22)\n"
"v_dot2_f32_bf16 v216, v112, s44, v216\n"
"v_dot2_f32_bf16 v217, v112, s45, v217\n"
"v_dot2_f32_bf16 v218, v113, s44, v218\n"
"v_dot2_f32_bf16 v219, v113, s45, v219\n"
"v_dot2_f32_bf16 v220, v114, s44, v220\n"
"v_dot2_f32_bf16 v221, v114, s45, v221\n"
"v_dot2_f32_bf16 v222, v115, s44, v222\n"
"v_dot2_f32_bf16 v223, v115, s45, v223\n"
"v_dot2_f32_bf16 v216, v116, s44, v216\n"
"v_dot2_f32_bf16 v217, v116, s45, v217\n"
"v_dot2_f32_bf16 v218, v117, s44, v218\n"
"v_dot2_f32_bf16 v219, v117, s45, v219\n"
"v_dot2_f32_bf16 v220, v118, s44, v220\n"
"v_dot2_f32_bf16 v221, v118, s45, v221\n"
"v_dot2_f32_bf16 v222, v119, s44, v222\n"
"v_dot2_f32_bf16 v223, v119, s45, v223\n"
"s_waitcnt vmcnt(20)\n"
"v_dot2_f32_bf16 v216, v120, s44, v216\n"
"v_dot2_f32_bf16 v217, v120, s45, v217\n"
"v_dot2_f32_bf16 v218, v121, s44, v218\n"
"v_dot2_f32_bf16 v219, v121, s45, v219\n"
"v_dot2_f32_bf16 v220, v122, s44, v220\n"
"v_dot2_f32_bf16 v221, v122, s45, v221\n"
"v_dot2_f32_bf16 v222, v123, s44, v222\n"
"v_dot2_f32_bf16 v223, v123, s45, v223\n"
"v_dot2_f32_bf16 v216, v124, s44, v216\n"
"v_dot2_f32_bf16 v217, v124, s45, v217\n"
"v_dot2_f32_bf16 v218, v125, s44, v218\n"
"v_dot2_f32_bf16 v219, v125, s45, v219\n"
"v_dot2_f32_bf16 v220, v126, s44, v220\n"
"v_dot2_f32_bf16 v221, v126, s45, v221\n"
"v_dot2_f32_bf16 v222, v127, s44, v222\n"
"v_dot2_f32_bf16 v223, v127, s45, v223\n"
"s_waitcnt vmcnt(18)\n"
"v_dot2_f32_bf16 v180, v128, s44, 0\n"
"v_dot2_f32_bf16 v181, v128, s45, 0\n"
"v_dot2_f32_bf16 v182, v129, s44, 0\n"
"v_dot2_f32_bf16 v183, v129, s45, 0\n"
"v_dot2_f32_bf16 v184, v130, s44, 0\n"
"v_dot2_f32_bf16 v185, v130, s45, 0\n"
"v_dot2_f32_bf16 v186, v131, s44, 0\n"
"v_dot2_f32_bf16 v187, v131, s45, 0\n"
"v_dot2_f32_bf16 v180, v132, s44, v180\n"
"v_dot2_f32_bf16 v181, v132, s45, v181\n"
"v_dot2_f32_bf16 v182, v133, s44, v182\n"
"v_dot2_f32_bf16 v183, v133, s45, v183\n"
"v_dot2_f32_bf16 v184, v134, s44, v184\n"
"v_dot2_f32_bf16 v185, v134, s45, v185\n"
"v_dot2_f32_bf16 v186, v135, s44, v186\n"
"v_dot2_f32_bf16 v187, v135, s45, v187\n"
"s_waitcnt vmcnt(16)\n"
"v_dot2_f32_bf16 v180, v136, s44, v180\n"
"v_dot2_f32_bf16 v181, v136, s45, v181\n"
"v_dot2_f32_bf16 v182, v137, s44, v182\n"
"v_dot2_f32_bf16 v183, v137, s45, v183\n"
"v_dot2_f32_bf16 v184, v138, s44, v184\n"
"v_dot2_f32_bf16 v185, v138, s45, v185\n"
"v_dot2_f32_bf16 v186, v139, s44, v186\n"
"v_dot2_f32_bf16 v187, v139, s45, v187\n"
"v_dot2_f32_bf16 v180, v140, s44, v180\n"
"v_dot2_f32_bf16 v181, v140, s45, v181\n"
"v_dot2_f32_bf16 v182, v141, s44, v182\n"
"v_dot2_f32_bf16 v183, v141, s45, v183\n"
"v_dot2_f32_bf16 v184, v142, s44, v184\n"
"v_dot2_f32_bf16 v185, v142, s45, v185\n"
"v_dot2_f32_bf16 v186, v143, s44, v186\n"
"v_dot2_f32_bf16 v187, v143, s45, v187\n"
"s_nop 3\n"
"v_exp_f32 v188, -v216\n"
"v_exp_f32 v189, -v217\n"
"v_exp_f32 v190, -v218\n"
"v_exp_f32 v191, -v219\n"
"v_exp_f32 v192, -v220\n"
"v_exp_f32 v193, -v221\n"
"v_exp_f32 v194, -v222\n"
"v_exp_f32 v195, -v223\n"
"v_mul_f32 v216, v216, v180\n"
"v_mul_f32 v217, v217, v181\n"
"v_mul_f32 v218, v218, v182\n"
"v_mul_f32 v219, v219, v183\n"
"v_mul_f32 v220, v220, v184\n"
"v_mul_f32 v221, v221, v185\n"
"v_mul_f32 v222, v222, v186\n"
"v_mul_f32 v223, v223, v187\n"
"v_add_f32 v188, 1.0, v188\n"
"v_add_f32 v189, 1.0, v189\n"
"v_add_f32 v190, 1.0, v190\n"
"v_add_f32 v191, 1.0, v191\n"
"v_add_f32 v192, 1.0, v192\n"
"v_add_f32 v193, 1.0, v193\n"
"v_add_f32 v194, 1.0, v194\n"
"v_add_f32 v195, 1.0, v195\n"
"v_rcp_f32 v188, v188\n"
"v_rcp_f32 v189, v189\n"
"v_rcp_f32 v190, v190\n"
"v_rcp_f32 v191, v191\n"
"v_rcp_f32 v192, v192\n"
"v_rcp_f32 v193, v193\n"
"v_rcp_f32 v194, v194\n"
"v_rcp_f32 v195, v195\n"
"v_lshlrev_b32 v180, 16, v164\n"
"v_and_b32 v181, 0xffff0000, v164\n"
"v_lshlrev_b32 v182, 16, v165\n"
"v_and_b32 v183, 0xffff0000, v165\n"
"v_lshlrev_b32 v184, 16, v166\n"
"v_and_b32 v185, 0xffff0000, v166\n"
"v_lshlrev_b32 v186, 16, v167\n"
"v_and_b32 v187, 0xffff0000, v167\n"
"v_mul_f32 v216, v216, v188\n"
"v_mul_f32 v217, v217, v189\n"
"v_mul_f32 v218, v218, v190\n"
"v_mul_f32 v219, v219, v191\n"
"v_mul_f32 v220, v220, v192\n"
"v_mul_f32 v221, v221, v193\n"
"v_mul_f32 v222, v222, v194\n"
"v_mul_f32 v223, v223, v195\n"
"v_fma_f32 v216, v216, s46, v180\n"
"v_fma_f32 v217, v217, s46, v181\n"
"v_fma_f32 v218, v218, s46, v182\n"
"v_fma_f32 v219, v219, s46, v183\n"
"v_fma_f32 v220, v220, s46, v184\n"
"v_fma_f32 v221, v221, s46, v185\n"
"v_fma_f32 v222, v222, s46, v186\n"
"v_fma_f32 v223, v223, s46, v187\n"
"v_cvt_pk_bf16_f32 v200, v216, v217\n"
"v_cvt_pk_bf16_f32 v201, v218, v219\n"
"v_cvt_pk_bf16_f32 v202, v220, v221\n"
"v_cvt_pk_bf16_f32 v203, v222, v223\n"
"s_waitcnt vmcnt(14)\n"
"v_dot2_f32_bf16 v224, v144, s44, 0\n"
"v_dot2_f32_bf16 v225, v144, s45, 0\n"
"s_nop 1\n"
"v_dot2_f32_bf16 v224, v145, s44, v224\n"
"v_dot2_f32_bf16 v225, v145, s45, v225\n"
"s_nop 1\n"
"s_waitcnt vmcnt(12)\n"
"v_dot2_f32_bf16 v224, v146, s44, v224\n"
"v_dot2_f32_bf16 v225, v146, s45, v225\n"
"s_nop 1\n"
"v_dot2_f32_bf16 v224, v147, s44, v224\n"
"v_dot2_f32_bf16 v225, v147, s45, v225\n"
"s_nop 1\n"
"s_waitcnt vmcnt(10)\n"
"v_dot2_f32_bf16 v224, v148, s44, v224\n"
"v_dot2_f32_bf16 v225, v148, s45, v225\n"
"s_nop 1\n"
"v_dot2_f32_bf16 v224, v149, s44, v224\n"
"v_dot2_f32_bf16 v225, v149, s45, v225\n"
"s_nop 1\n"
"s_waitcnt vmcnt(8)\n"
"v_dot2_f32_bf16 v224, v150, s44, v224\n"
"v_dot2_f32_bf16 v225, v150, s45, v225\n"
"s_nop 1\n"
"v_dot2_f32_bf16 v224, v151, s44, v224\n"
"v_dot2_f32_bf16 v225, v151, s45, v225\n"
"s_nop 1\n"
"s_waitcnt vmcnt(6)\n"
"v_dot2_f32_bf16 v224, v152, s44, v224\n"
"v_dot2_f32_bf16 v225, v152, s45, v225\n"
"s_nop 1\n"
"v_dot2_f32_bf16 v224, v153, s44, v224\n"
"v_dot2_f32_bf16 v225, v153, s45, v225\n"
"s_nop 1\n"
"s_waitcnt vmcnt(4)\n"
"v_dot2_f32_bf16 v224, v154, s44, v224\n"
"v_dot2_f32_bf16 v225, v154, s45, v225\n"
"s_nop 1\n"
"v_dot2_f32_bf16 v224, v155, s44, v224\n"
"v_dot2_f32_bf16 v225, v155, s45, v225\n"
"s_nop 1\n"
"s_waitcnt vmcnt(2)\n"
"v_dot2_f32_bf16 v180, v156, s44, 0\n"
"v_dot2_f32_bf16 v181, v156, s45, 0\n"
"s_nop 1\n"
"v_dot2_f32_bf16 v180, v157, s44, v180\n"
"v_dot2_f32_bf16 v181, v157, s45, v181\n"
"s_nop 1\n"
"s_waitcnt vmcnt(0)\n"
"v_dot2_f32_bf16 v180, v158, s44, v180\n"
"v_dot2_f32_bf16 v181, v158, s45, v181\n"
"s_nop 1\n"
"v_dot2_f32_bf16 v180, v159, s44, v180\n"
"v_dot2_f32_bf16 v181, v159, s45, v181\n"
"s_nop 1\n"
"s_nop 3\n"
"v_exp_f32 v188, -v224\n"
"v_exp_f32 v189, -v225\n"
"v_mul_f32 v224, v224, v180\n"
"v_mul_f32 v225, v225, v181\n"
"v_add_f32 v188, 1.0, v188\n"
"v_add_f32 v189, 1.0, v189\n"
"v_rcp_f32 v188, v188\n"
"v_rcp_f32 v189, v189\n"
"v_lshlrev_b32 v180, 16, v168\n"
"v_and_b32 v181, 0xffff0000, v168\n"
"v_mul_f32 v224, v224, v188\n"
"v_mul_f32 v225, v225, v189\n"
"v_fma_f32 v224, v224, s46, v180\n"
"v_fma_f32 v225, v225, s46, v181\n"
"v_cvt_pk_bf16_f32 v204, v224, v225\n"
"global_store_dwordx4 v2, v[196:199], %13\n"
"global_store_dwordx4 v3, v[200:203], %13\n"
"global_store_dword v4, v204, %13\n"
"s_nop 1\n"
"CEN_%=:\n"
      "s_endpgm\n"
      :: "v"(l * 16), "v"(img_w), "v"(img_r), "v"(atv), "v"(tid * 16), "s"(ws + WS_WF + (size_t)hq * 720 * 1024),
         "s"(ws + WS_XN + (size_t)(mt * 4 + wv) * 36864), "s"(ws + WS_KV + (size_t)hq * 8 * 3040), "s"(ws + WS_CNT + hq * 256),
         "s"(ws + WS_PART + (size_t)role * PART_B + (size_t)(mt * 4 + wv) * 36864), "s"(wv), TAIL_OPS
      : "vcc",
"v8","v9","v10","v11","v12","v13","v14","v15","v16","v17","v18","v19","v20","v21","v22","v23",
"v24","v25","v26","v27","v28","v29","v30","v31","v32","v33","v34","v35","v36","v37","v38","v39",
"v40","v41","v42","v43","v44","v45","v46","v47","v48","v49","v50","v51","v52","v53","v54","v55",
"v56","v57","v58","v59","v60","v61","v62","v63","v64","v65","v66","v67","v68","v69","v70","v71",
"v72","v73","v74","v75","v76","v77","v78","v79","v80","v81","v82","v83","v84","v85","v86","v87",
"v88","v89","v90","v91","v92","v93","v94","v95","v96","v97","v98","v99","v100","v101","v102","v103",
"v104","v105","v106","v107","v108","v109","v110","v111","v112","v113","v114","v115","v116","v117","v118","v119",
"v120","v121","v122","v123","v124","v125","v126","v127","v128","v129","v130","v131","v132","v133","v134","v135",
"v136","v137","v138","v139","v140","v141","v142","v143","v144","v145","v146","v147","v148","v149","v150","v151",
"v152","v153","v154","v155","v156","v157","v158","v159","v160","v161","v162","v163","v164","v165","v166","v167",
"v168","v169","v170","v171","v172","v173","v174","v175","v176","v177","v178","v179","v180","v181","v182","v183",
"v184","v185","v186","v187","v188","v189","v190","v191","v192","v193","v194","v195","v196","v197","v198","v199",
"v200","v201","v202","v203","v204","v205","v206","v207","v208","v209","v210","v211","v212","v213","v214","v215",
"v216","v217","v218","v219","v220","v221","v222","v223","v224","v225","v226","v227","v228","v229","v230","v231",
"v232","v233","v234","v235","v236","v237","v238","v239","v240","v241","v242","v243","v244","v245","v246","v247",
"v248","v249","v250","v251","v252","v253","v254","v255","a0","a1","a2","a3","a4","a5","a6","a7",
"a8","a9","a10","a11","a12","a13","a14","a15","a16","a17","a18","a19","a20","a21","a22","a23",
"a24","a25","a26","a27","a28","a29","a30","a31","a32","a33","a34","a35","a36","a37","a38","a39",
"a40","a41","a42","a43","a44","a45","a46","a47","a48","a49","a50","a51","a52","a53","a54","a55",
"a56","a57","a58","a59","a60","a61","a62","a63","a64","a65","a66","a67","a68","a69","a70","a71",
"a72","a73","a74","a75","a76","a77","a78","a79","a80","a81","a82","a83","a84","a85","a86","a87",
"a88","a89","a90","a91","a92","a93","a94","a95","a96","a97","a98","a99","a100","a101","a102","a103",
"a104","a105","a106","a107","a108","a109","a110","a111","a112","a113","a114","a115","a116","a117","a118","a119",
"a120","a121","a122","a123","a124","a125","a126","a127","a128","a129","a130","a131","a132","a133","a134","a135",
"a136","a137","a138","a139","a140","a141","a142","a143","a144","a145","a146","a147","a148","a149","a150","a151",
"a152","a153","a154","a155","a156","a157","a158","a159","a160","a161","a162","a163","a164","a165","a166","a167",
"a168","a169","a170","a171","a172","a173","a174","a175","a176","a177","a178","a179","a180","a181","a182","a183",
"a184","a185","a186","a187","a188","a189","a190","a191","a192","a193","a194","a195","a196","a197","a198","a199",
"a200","a201","a202","a203","a204","a205","a206","a207","a208","a209","a210","a211","a212","a213","a214","a215",
"a216","a217","a218","a219","a220","a221","a222","a223","a224","a225","a226","a227","a228","a229","a230","a231",
"a232","a233","a234","a235","a236","a237","a238","a239","a240","a241","a242","a243","a244","a245","a246","a247",
"a248","a249","a250","a251","a252","a253","a254","a255","s24","s25","s26","s27","s28","s29","s30","s31",
"s32","s33","s34","s35","s40","s41","s42","s43","s44","s45","s46","s47","s48","s49","s50","s51",
"s52","s53","s54","s55","s56","s57","s58","s59","s60","s61","s62","scc","memory"
    );
    __builtin_unreachable();
  }
  const int h = l >> 5, g = (l >> 4) & 1, q = (l >> 2) & 3, p = l & 3, c0 = (mt & 1) * 3;
  int g1 = h * 1024 + q * 64 + g * 32 + p * 8;
  int g2 = 73728 + (4 * h + q) * 64 + 32 * (p & 1) + 16 * g + 8 * (p >> 1);
  int dw1 = ((l >> 2) & 7) * 9216 + h * 64 + p * 16, dw2 = ((l >> 2) & 7) * 1152 + h * 64 + p * 16;
  int brd = 147456 + c0 * 256 + h * 16, bi = (tid < 192 ? tid : 191) * 4;
  const char* xp = ws + WS_XN + (size_t)(mt * 4 + wv) * 36864;
  char* op = ws + WS_PART + (size_t)role * PART_B + (size_t)(mt * 4 + wv) * 36864;
  asm volatile(
"s_nop 4\n"
"s_getpc_b64 s[40:41]\n"
"CST_%=:\n"
"v_mbcnt_lo_u32_b32 v220, -1, 0\n"
"v_mbcnt_hi_u32_b32 v220, -1, v220\n"
"v_lshlrev_b32 v220, 6, v220\n"
"s_lshl_b32 s42, %13, 12\n"
"v_add_u32 v220, s42, v220\n"
"s_mov_b32 s42, CEN_%=-CST_%=-4\n"
"v_min_u32 v221, s42, v220\n"
"global_load_dword v223, v221, s[40:41]\n"
"v_add_u32 v220, 0x4000, v220\n"
"s_nop 1\n"
"v_min_u32 v221, s42, v220\n"
"global_load_dword v223, v221, s[40:41]\n"
"v_add_u32 v220, 0x4000, v220\n"
"s_nop 1\n"
"v_mov_b32 v8, %0\n"
"v_mov_b32 v9, %1\n"
"v_mov_b32 v10, %2\n"
"v_mov_b32 v11, %3\n"
"v_mov_b32 v12, %4\n"
"v_mov_b32 v13, %5\n"
"v_mov_b32 v14, %6\n"
"global_load_dword v15, v14, %7\n"
"v_mov_b32 v0, v8\n"
"v_add_u32 v1, 36864, v8\n"
"v_mov_b32 v2, v9\n"
"v_add_u32 v3, 36864, v9\n"
"v_mov_b32 v4, v10\n"
"v_mov_b32 v5, v11\n"
"v_mov_b32 v6, v12\n"
"v_mov_b32 v7, v13\n"
"s_mov_b64 s[34:35], %8\n"
"s_mov_b32 s42, 2654208\n"
"s_mov_b32 s43, 0x20000\n"
"s_mov_b64 s[44:45], %9\n"
"s_mov_b32 s46, 5308416\n"
"s_mov_b32 s47, 0x20000\n"
"s_mov_b32 s52, %12\n"
"s_mul_i32 s53, %13, 9216\n"
"s_mul_i32 s54, %13, 663552\n"
"s_mov_b32 s55, s53\n"
"s_mul_i32 s57, %14, 768\n"
"s_mul_i32 s58, %14, 442368\n"
"s_lshl_b32 s50, s52, 7\n"
"s_add_u32 s50, s50, s57\n"
"s_mul_i32 s51, s52, 73728\n"
"s_add_u32 s51, s51, s58\n"
"s_mov_b64 s[60:61], %10\n"
"s_mov_b32 s24, s54\n"
"s_add_u32 s25, s24, 73728\n"
"s_add_u32 s26, s25, 73728\n"
"s_add_u32 s27, s26, 73728\n"
"s_add_u32 s28, s27, 73728\n"
"s_add_u32 s29, s28, 73728\n"
"s_add_u32 s30, s29, 73728\n"
"s_add_u32 s31, s30, 73728\n"
"s_add_u32 s32, s31, 73728\n"
"s_add_u32 s40, s34, s50\n"
"s_addc_u32 s41, s35, 0\n"
"s_add_u32 m0, s53, 0\n"
"s_nop 0\n"
"buffer_load_dwordx4 v4, s[40:43], s24 offen lds\n"
"global_load_dwordx4 v[80:83], v7, s[60:61] offset:0\n"
"global_load_dwordx4 v[84:87], v7, s[60:61] offset:1024\n"
"s_add_u32 m0, m0, 1024\n"
"s_nop 0\n"
"buffer_load_dwordx4 v4, s[40:43], s25 offen lds\n"
"global_load_dwordx4 v[88:91], v7, s[60:61] offset:2048\n"
"global_load_dwordx4 v[92:95], v7, s[60:61] offset:3072\n"
"s_add_u32 s60, s60, 4096\n"
"s_addc_u32 s61, s61, 0\n"
"s_add_u32 m0, m0, 1024\n"
"s_nop 0\n"
"buffer_load_dwordx4 v4, s[40:43], s26 offen lds\n"
"global_load_dwordx4 v[96:99], v7, s[60:61] offset:0\n"
"global_load_dwordx4 v[100:103], v7, s[60:61] offset:1024\n"
"s_add_u32 m0, m0, 1024\n"
"s_nop 0\n"
"buffer_load_dwordx4 v4, s[40:43], s27 offen lds\n"
"global_load_dwordx4 v[104:107], v7, s[60:61] offset:2048\n"
"v_mov_b32 v79, 0\n"
"v_mov_b32 v78, 0\n"
"v_mov_b32 v77, 0\n"
"v_mov_b32 v76, 0\n"
"v_mov_b32 v75, 0\n"
"v_mov_b32 v74, 0\n"
"v_mov_b32 v73, 0\n"
"v_mov_b32 v72, 0\n"
"v_mov_b32 v71, 0\n"
"v_mov_b32 v70, 0\n"
"v_mov_b32 v69, 0\n"
"v_mov_b32 v68, 0\n"
"global_load_dwordx4 v[108:111], v7, s[60:61] offset:3072\n"
"s_add_u32 s60, s60, 4096\n"
"s_addc_u32 s61, s61, 0\n"
"v_mov_b32 v67, 0\n"
"v_mov_b32 v66, 0\n"
"v_mov_b32 v65, 0\n"
"v_mov_b32 v64, 0\n"
"v_mov_b32 v63, 0\n"
"v_mov_b32 v62, 0\n"
"v_mov_b32 v61, 0\n"
"v_mov_b32 v60, 0\n"
"v_mov_b32 v59, 0\n"
"v_mov_b32 v58, 0\n"
"v_mov_b32 v57, 0\n"
"v_mov_b32 v56, 0\n"
"s_add_u32 m0, m0, 1024\n"
"s_nop 0\n"
"buffer_load_dwordx4 v4, s[40:43], s28 offen lds\n"
"global_load_dwordx4 v[112:115], v7, s[60:61] offset:0\n"
"v_mov_b32 v55, 0\n"
"v_mov_b32 v54, 0\n"
"v_mov_b32 v53, 0\n"
"v_mov_b32 v52, 0\n"
"v_mov_b32 v51, 0\n"
"v_mov_b32 v50, 0\n"
"v_mov_b32 v49, 0\n"
"v_mov_b32 v48, 0\n"
"v_mov_b32 v47, 0\n"
"v_mov_b32 v46, 0\n"
"v_mov_b32 v45, 0\n"
"v_mov_b32 v44, 0\n"
"global_load_dwordx4 v[116:119], v7, s[60:61] offset:1024\n"
"v_mov_b32 v43, 0\n"
"v_mov_b32 v42, 0\n"
"v_mov_b32 v41, 0\n"
"v_mov_b32 v40, 0\n"
"v_mov_b32 v39, 0\n"
"v_mov_b32 v38, 0\n"
"v_mov_b32 v37, 0\n"
"v_mov_b32 v36, 0\n"
"v_mov_b32 v35, 0\n"
"v_mov_b32 v34, 0\n"
"v_mov_b32 v33, 0\n"
"v_mov_b32 v32, 0\n"
"s_add_u32 m0, m0, 1024\n"
"s_nop 0\n"
"buffer_load_dwordx4 v4, s[40:43], s29 offen lds\n"
"global_load_dwordx4 v[120:123], v7, s[60:61] offset:2048\n"
"v_mov_b32 v31, 0\n"
"v_mov_b32 v30, 0\n"
"v_mov_b32 v29, 0\n"
"v_mov_b32 v28, 0\n"
"v_mov_b32 v27, 0\n"
"v_mov_b32 v26, 0\n"
"v_mov_b32 v25, 0\n"
"v_mov_b32 v24, 0\n"
"v_mov_b32 v23, 0\n"
"v_mov_b32 v22, 0\n"
"v_mov_b32 v21, 0\n"
"v_mov_b32 v20, 0\n"
"global_load_dwordx4 v[124:127], v7, s[60:61] offset:3072\n"
"s_add_u32 s60, s60, 4096\n"
"s_addc_u32 s61, s61, 0\n"
"v_mov_b32 v19, 0\n"
"v_mov_b32 v18, 0\n"
"v_mov_b32 v17, 0\n"
"v_mov_b32 v16, 0\n"
"v_mov_b32 v255, 0\n"
"v_mov_b32 v254, 0\n"
"v_mov_b32 v253, 0\n"
"v_mov_b32 v252, 0\n"
"v_mov_b32 v251, 0\n"
"v_mov_b32 v250, 0\n"
"v_mov_b32 v249, 0\n"
"v_mov_b32 v248, 0\n"
"s_add_u32 m0, m0, 1024\n"
"s_nop 0\n"
"buffer_load_dwordx4 v4, s[40:43], s30 offen lds\n"
"global_load_dwordx4 v[128:131], v7, s[60:61] offset:0\n"
"v_mov_b32 v247, 0\n"
"v_mov_b32 v246, 0\n"
"v_mov_b32 v245, 0\n"
"v_mov_b32 v244, 0\n"
"v_mov_b32 v243, 0\n"
"v_mov_b32 v242, 0\n"
"v_mov_b32 v241, 0\n"
"v_mov_b32 v240, 0\n"
"v_mov_b32 v239, 0\n"
"v_mov_b32 v238, 0\n"
"v_mov_b32 v237, 0\n"
"v_mov_b32 v236, 0\n"
"global_load_dwordx4 v[132:135], v7, s[60:61] offset:1024\n"
"v_mov_b32 v235, 0\n"
"v_mov_b32 v234, 0\n"
"v_mov_b32 v233, 0\n"
"v_mov_b32 v232, 0\n"
"v_mov_b32 v231, 0\n"
"v_mov_b32 v230, 0\n"
"v_mov_b32 v229, 0\n"
"v_mov_b32 v228, 0\n"
"v_mov_b32 v227, 0\n"
"v_mov_b32 v226, 0\n"
"v_mov_b32 v225, 0\n"
"v_mov_b32 v224, 0\n"
"s_add_u32 m0, m0, 1024\n"
"s_nop 0\n"
"buffer_load_dwordx4 v4, s[40:43], s31 offen lds\n"
"global_load_dwordx4 v[136:139], v7, s[60:61] offset:2048\n"
"v_accvgpr_write_b32 a255, 0\n"
"v_accvgpr_write_b32 a254, 0\n"
"v_accvgpr_write_b32 a253, 0\n"
"v_accvgpr_write_b32 a252, 0\n"
"v_accvgpr_write_b32 a251, 0\n"
"v_accvgpr_write_b32 a250, 0\n"
"v_accvgpr_write_b32 a249, 0\n"
"v_accvgpr_write_b32 a248, 0\n"
"v_accvgpr_write_b32 a247, 0\n"
"v_accvgpr_write_b32 a246, 0\n"
"v_accvgpr_write_b32 a245, 0\n"
"v_accvgpr_write_b32 a244, 0\n"
"global_load_dwordx4 v[140:143], v7, s[60:61] offset:3072\n"
"s_add_u32 s60, s60, 4096\n"
"s_addc_u32 s61, s61, 0\n"
"v_accvgpr_write_b32 a243, 0\n"
"v_accvgpr_write_b32 a242, 0\n"
"v_accvgpr_write_b32 a241, 0\n"
"v_accvgpr_write_b32 a240, 0\n"
"v_accvgpr_write_b32 a239, 0\n"
"v_accvgpr_write_b32 a238, 0\n"
"v_accvgpr_write_b32 a237, 0\n"
"v_accvgpr_write_b32 a236, 0\n"
"v_accvgpr_write_b32 a235, 0\n"
"v_accvgpr_write_b32 a234, 0\n"
"v_accvgpr_write_b32 a233, 0\n"
"v_accvgpr_write_b32 a232, 0\n"
"s_add_u32 m0, m0, 1024\n"
"s_nop 0\n"
"buffer_load_dwordx4 v4, s[40:43], s32 offen lds\n"
"global_load_dwordx4 v[144:147], v7, s[60:61] offset:0\n"
"v_accvgpr_write_b32 a231, 0\n"
"v_accvgpr_write_b32 a230, 0\n"
"v_accvgpr_write_b32 a229, 0\n"
"v_accvgpr_write_b32 a228, 0\n"
"v_accvgpr_write_b32 a227, 0\n"
"v_accvgpr_write_b32 a226, 0\n"
"v_accvgpr_write_b32 a225, 0\n"
"v_accvgpr_write_b32 a224, 0\n"
"v_accvgpr_write_b32 a223, 0\n"
"v_accvgpr_write_b32 a222, 0\n"
"v_accvgpr_write_b32 a221, 0\n"
"v_accvgpr_write_b32 a220, 0\n"
"global_load_dwordx4 v[148:151], v7, s[60:61] offset:1024\n"
"v_accvgpr_write_b32 a219, 0\n"
"v_accvgpr_write_b32 a218, 0\n"
"v_accvgpr_write_b32 a217, 0\n"
"v_accvgpr_write_b32 a216, 0\n"
"v_accvgpr_write_b32 a215, 0\n"
"v_accvgpr_write_b32 a214, 0\n"
"v_accvgpr_write_b32 a213, 0\n"
"v_accvgpr_write_b32 a212, 0\n"
"v_accvgpr_write_b32 a211, 0\n"
"v_accvgpr_write_b32 a210, 0\n"
"v_accvgpr_write_b32 a209, 0\n"
"v_accvgpr_write_b32 a208, 0\n"
"s_add_u32 s40, s40, 2654208\n"
"s_addc_u32 s41, s41, 0\n"
"s_add_u32 m0, s53, 36864\n"
"s_nop 0\n"
"buffer_load_dwordx4 v4, s[40:43], s24 offen lds\n"
"global_load_dwordx4 v[152:155], v7, s[60:61] offset:2048\n"
"v_accvgpr_write_b32 a207, 0\n"
"v_accvgpr_write_b32 a206, 0\n"
"v_accvgpr_write_b32 a205, 0\n"
"v_accvgpr_write_b32 a204, 0\n"
"v_accvgpr_write_b32 a203, 0\n"
"v_accvgpr_write_b32 a202, 0\n"
"v_accvgpr_write_b32 a201, 0\n"
"v_accvgpr_write_b32 a200, 0\n"
"v_accvgpr_write_b32 a199, 0\n"
"v_accvgpr_write_b32 a198, 0\n"
"v_accvgpr_write_b32 a197, 0\n"
"v_accvgpr_write_b32 a196, 0\n"
"global_load_dwordx4 v[156:159], v7, s[60:61] offset:3072\n"
"s_add_u32 s60, s60, 4096\n"
"s_addc_u32 s61, s61, 0\n"
"v_accvgpr_write_b32 a195, 0\n"
"v_accvgpr_write_b32 a194, 0\n"
"v_accvgpr_write_b32 a193, 0\n"
"v_accvgpr_write_b32 a192, 0\n"
"v_accvgpr_write_b32 a191, 0\n"
"v_accvgpr_write_b32 a190, 0\n"
"v_accvgpr_write_b32 a189, 0\n"
"v_accvgpr_write_b32 a188, 0\n"
"v_accvgpr_write_b32 a187, 0\n"
"v_accvgpr_write_b32 a186, 0\n"
"v_accvgpr_write_b32 a185, 0\n"
"v_accvgpr_write_b32 a184, 0\n"
"s_add_u32 m0, m0, 1024\n"
"s_nop 0\n"
"buffer_load_dwordx4 v4, s[40:43], s25 offen lds\n"
"global_load_dwordx4 v[160:163], v7, s[60:61] offset:0\n"
"v_accvgpr_write_b32 a183, 0\n"
"v_accvgpr_write_b32 a182, 0\n"
"v_accvgpr_write_b32 a181, 0\n"
"v_accvgpr_write_b32 a180, 0\n"
"v_accvgpr_write_b32 a179, 0\n"
"v_accvgpr_write_b32 a178, 0\n"
"v_accvgpr_write_b32 a177, 0\n"
"v_accvgpr_write_b32 a176, 0\n"
"v_accvgpr_write_b32 a175, 0\n"
"v_accvgpr_write_b32 a174, 0\n"
"v_accvgpr_write_b32 a173, 0\n"
"v_accvgpr_write_b32 a172, 0\n"
"global_load_dwordx4 v[164:167], v7, s[60:61] offset:1024\n"
"v_accvgpr_write_b32 a171, 0\n"
"v_accvgpr_write_b32 a170, 0\n"
"v_accvgpr_write_b32 a169, 0\n"
"v_accvgpr_write_b32 a168, 0\n"
"v_accvgpr_write_b32 a167, 0\n"
"v_accvgpr_write_b32 a166, 0\n"
"v_accvgpr_write_b32 a165, 0\n"
"v_accvgpr_write_b32 a164, 0\n"
"v_accvgpr_write_b32 a163, 0\n"
"v_accvgpr_write_b32 a162, 0\n"
"v_accvgpr_write_b32 a161, 0\n"
"v_accvgpr_write_b32 a160, 0\n"
"s_add_u32 m0, m0, 1024\n"
"s_nop 0\n"
"buffer_load_dwordx4 v4, s[40:43], s26 offen lds\n"
"global_load_dwordx4 v[168:171], v7, s[60:61] offset:2048\n"
"v_accvgpr_write_b32 a159, 0\n"
"v_accvgpr_write_b32 a158, 0\n"
"v_accvgpr_write_b32 a157, 0\n"
"v_accvgpr_write_b32 a156, 0\n"
"v_accvgpr_write_b32 a155, 0\n"
"v_accvgpr_write_b32 a154, 0\n"
"v_accvgpr_write_b32 a153, 0\n"
"v_accvgpr_write_b32 a152, 0\n"
"v_accvgpr_write_b32 a151, 0\n"
"v_accvgpr_write_b32 a150, 0\n"
"v_accvgpr_write_b32 a149, 0\n"
"v_accvgpr_write_b32 a148, 0\n"
"global_load_dwordx4 v[172:175], v7, s[60:61] offset:3072\n"
"s_add_u32 s60, s60, 4096\n"
"s_addc_u32 s61, s61, 0\n"
"v_accvgpr_write_b32 a147, 0\n"
"v_accvgpr_write_b32 a146, 0\n"
"v_accvgpr_write_b32 a145, 0\n"
"v_accvgpr_write_b32 a144, 0\n"
"v_accvgpr_write_b32 a143, 0\n"
"v_accvgpr_write_b32 a142, 0\n"
"v_accvgpr_write_b32 a141, 0\n"
"v_accvgpr_write_b32 a140, 0\n"
"v_accvgpr_write_b32 a139, 0\n"
"v_accvgpr_write_b32 a138, 0\n"
"v_accvgpr_write_b32 a137, 0\n"
"v_accvgpr_write_b32 a136, 0\n"
"s_add_u32 m0, m0, 1024\n"
"s_nop 0\n"
"buffer_load_dwordx4 v4, s[40:43], s27 offen lds\n"
"global_load_dwordx4 v[176:179], v7, s[60:61] offset:0\n"
"v_accvgpr_write_b32 a135, 0\n"
"v_accvgpr_write_b32 a134, 0\n"
"v_accvgpr_write_b32 a133, 0\n"
"v_accvgpr_write_b32 a132, 0\n"
"v_accvgpr_write_b32 a131, 0\n"
"v_accvgpr_write_b32 a130, 0\n"
"v_accvgpr_write_b32 a129, 0\n"
"v_accvgpr_write_b32 a128, 0\n"
"v_accvgpr_write_b32 a127, 0\n"
"v_accvgpr_write_b32 a126, 0\n"
"v_accvgpr_write_b32 a125, 0\n"
"v_accvgpr_write_b32 a124, 0\n"
"global_load_dwordx4 v[180:183], v7, s[60:61] offset:1024\n"
"v_accvgpr_write_b32 a123, 0\n"
"v_accvgpr_write_b32 a122, 0\n"
"v_accvgpr_write_b32 a121, 0\n"
"v_accvgpr_write_b32 a120, 0\n"
"v_accvgpr_write_b32 a119, 0\n"
"v_accvgpr_write_b32 a118, 0\n"
"v_accvgpr_write_b32 a117, 0\n"
"v_accvgpr_write_b32 a116, 0\n"
"v_accvgpr_write_b32 a115, 0\n"
"v_accvgpr_write_b32 a114, 0\n"
"v_accvgpr_write_b32 a113, 0\n"
"v_accvgpr_write_b32 a112, 0\n"
"s_add_u32 m0, m0, 1024\n"
"s_nop 0\n"
"buffer_load_dwordx4 v4, s[40:43], s28 offen lds\n"
"global_load_dwordx4 v[184:187], v7, s[60:61] offset:2048\n"
"v_accvgpr_write_b32 a111, 0\n"
"v_accvgpr_write_b32 a110, 0\n"
"v_accvgpr_write_b32 a109, 0\n"
"v_accvgpr_write_b32 a108, 0\n"
"v_accvgpr_write_b32 a107, 0\n"
"v_accvgpr_write_b32 a106, 0\n"
"v_accvgpr_write_b32 a105, 0\n"
"v_accvgpr_write_b32 a104, 0\n"
"v_accvgpr_write_b32 a103, 0\n"
"v_accvgpr_write_b32 a102, 0\n"
"v_accvgpr_write_b32 a101, 0\n"
"v_accvgpr_write_b32 a100, 0\n"
"global_load_dwordx4 v[188:191], v7, s[60:61] offset:3072\n"
"s_add_u32 s60, s60, 4096\n"
"s_addc_u32 s61, s61, 0\n"
"v_accvgpr_write_b32 a99, 0\n"
"v_accvgpr_write_b32 a98, 0\n"
"v_accvgpr_write_b32 a97, 0\n"
"v_accvgpr_write_b32 a96, 0\n"
"v_accvgpr_write_b32 a95, 0\n"
"v_accvgpr_write_b32 a94, 0\n"
"v_accvgpr_write_b32 a93, 0\n"
"v_accvgpr_write_b32 a92, 0\n"
"v_accvgpr_write_b32 a91, 0\n"
"v_accvgpr_write_b32 a90, 0\n"
"v_accvgpr_write_b32 a89, 0\n"
"v_accvgpr_write_b32 a88, 0\n"
"s_add_u32 m0, m0, 1024\n"
"s_nop 0\n"
"buffer_load_dwordx4 v4, s[40:43], s29 offen lds\n"
"global_load_dwordx4 v[192:195], v7, s[60:61] offset:0\n"
"v_accvgpr_write_b32 a87, 0\n"
"v_accvgpr_write_b32 a86, 0\n"
"v_accvgpr_write_b32 a85, 0\n"
"v_accvgpr_write_b32 a84, 0\n"
"v_accvgpr_write_b32 a83, 0\n"
"v_accvgpr_write_b32 a82, 0\n"
"v_accvgpr_write_b32 a81, 0\n"
"v_accvgpr_write_b32 a80, 0\n"
"v_accvgpr_write_b32 a79, 0\n"
"v_accvgpr_write_b32 a78, 0\n"
"v_accvgpr_write_b32 a77, 0\n"
"v_accvgpr_write_b32 a76, 0\n"
"global_load_dwordx4 v[196:199], v7, s[60:61] offset:1024\n"
"v_accvgpr_write_b32 a75, 0\n"
"v_accvgpr_write_b32 a74, 0\n"
"v_accvgpr_write_b32 a73, 0\n"
"v_accvgpr_write_b32 a72, 0\n"
"v_accvgpr_write_b32 a71, 0\n"
"v_accvgpr_write_b32 a70, 0\n"
"v_accvgpr_write_b32 a69, 0\n"
"v_accvgpr_write_b32 a68, 0\n"
"v_accvgpr_write_b32 a67, 0\n"
"v_accvgpr_write_b32 a66, 0\n"
"v_accvgpr_write_b32 a65, 0\n"
"v_accvgpr_write_b32 a64, 0\n"
"s_add_u32 m0, m0, 1024\n"
"s_nop 0\n"
"buffer_load_dwordx4 v4, s[40:43], s30 offen lds\n"
"global_load_dwordx4 v[200:203], v7, s[60:61] offset:2048\n"
"v_accvgpr_write_b32 a63, 0\n"
"v_accvgpr_write_b32 a62, 0\n"
"v_accvgpr_write_b32 a61, 0\n"
"v_accvgpr_write_b32 a60, 0\n"
"v_accvgpr_write_b32 a59, 0\n"
"v_accvgpr_write_b32 a58, 0\n"
"v_accvgpr_write_b32 a57, 0\n"
"v_accvgpr_write_b32 a56, 0\n"
"v_accvgpr_write_b32 a55, 0\n"
"v_accvgpr_write_b32 a54, 0\n"
"v_accvgpr_write_b32 a53, 0\n"
"v_accvgpr_write_b32 a52, 0\n"
"global_load_dwordx4 v[204:207], v7, s[60:61] offset:3072\n"
"s_add_u32 s60, s60, 4096\n"
"s_addc_u32 s61, s61, 0\n"
"v_accvgpr_write_b32 a51, 0\n"
"v_accvgpr_write_b32 a50, 0\n"
"v_accvgpr_write_b32 a49, 0\n"
"v_accvgpr_write_b32 a48, 0\n"
"v_accvgpr_write_b32 a47, 0\n"
"v_accvgpr_write_b32 a46, 0\n"
"v_accvgpr_write_b32 a45, 0\n"
"v_accvgpr_write_b32 a44, 0\n"
"v_accvgpr_write_b32 a43, 0\n"
"v_accvgpr_write_b32 a42, 0\n"
"v_accvgpr_write_b32 a41, 0\n"
"v_accvgpr_write_b32 a40, 0\n"
"s_add_u32 m0, m0, 1024\n"
"s_nop 0\n"
"buffer_load_dwordx4 v4, s[40:43], s31 offen lds\n"
"global_load_dwordx4 v[208:211], v7, s[60:61] offset:0\n"
"v_accvgpr_write_b32 a39, 0\n"
"v_accvgpr_write_b32 a38, 0\n"
"v_accvgpr_write_b32 a37, 0\n"
"v_accvgpr_write_b32 a36, 0\n"
"v_accvgpr_write_b32 a35, 0\n"
"v_accvgpr_write_b32 a34, 0\n"
"v_accvgpr_write_b32 a33, 0\n"
"v_accvgpr_write_b32 a32, 0\n"
"v_accvgpr_write_b32 a31, 0\n"
"v_accvgpr_write_b32 a30, 0\n"
"v_accvgpr_write_b32 a29, 0\n"
"v_accvgpr_write_b32 a28, 0\n"
"global_load_dwordx4 v[212:215], v7, s[60:61] offset:1024\n"
"v_accvgpr_write_b32 a27, 0\n"
"v_accvgpr_write_b32 a26, 0\n"
"v_accvgpr_write_b32 a25, 0\n"
"v_accvgpr_write_b32 a24, 0\n"
"v_accvgpr_write_b32 a23, 0\n"
"v_accvgpr_write_b32 a22, 0\n"
"v_accvgpr_write_b32 a21, 0\n"
"v_accvgpr_write_b32 a20, 0\n"
"v_accvgpr_write_b32 a19, 0\n"
"v_accvgpr_write_b32 a18, 0\n"
"v_accvgpr_write_b32 a17, 0\n"
"v_accvgpr_write_b32 a16, 0\n"
"s_add_u32 m0, m0, 1024\n"
"s_nop 0\n"
"buffer_load_dwordx4 v4, s[40:43], s32 offen lds\n"
"global_load_dwordx4 v[216:219], v7, s[60:61] offset:2048\n"
"v_accvgpr_write_b32 a15, 0\n"
"v_accvgpr_write_b32 a14, 0\n"
"v_accvgpr_write_b32 a13, 0\n"
"v_accvgpr_write_b32 a12, 0\n"
"v_accvgpr_write_b32 a11, 0\n"
"v_accvgpr_write_b32 a10, 0\n"
"v_accvgpr_write_b32 a9, 0\n"
"v_accvgpr_write_b32 a8, 0\n"
"v_accvgpr_write_b32 a7, 0\n"
"v_accvgpr_write_b32 a6, 0\n"
"v_accvgpr_write_b32 a5, 0\n"
"v_accvgpr_write_b32 a4, 0\n"
"global_load_dwordx4 v[220:223], v7, s[60:61] offset:3072\n"
"s_add_u32 s60, s60, 4096\n"
"s_addc_u32 s61, s61, 0\n"
"v_accvgpr_write_b32 a3, 0\n"
"v_accvgpr_write_b32 a2, 0\n"
"v_accvgpr_write_b32 a1, 0\n"
"v_accvgpr_write_b32 a0, 0\n"
"s_waitcnt vmcnt(54)\n"
"v_lshlrev_b32 v8, 16, v15\n"
"v_and_b32 v9, 0xffff0000, v15\n"
"v_mul_f32 v8, 0x3fb8aa3b, v8\n"
"v_mul_f32 v9, 0x3fb8aa3b, v9\n"
"v_lshlrev_b32 v14, 1, v14\n"
"v_add_u32 v14, 147456, v14\n"
"ds_write_b64 v14, v[8:9]\n"
"s_waitcnt lgkmcnt(0)\n"
"s_add_u32 s56, s52, 1\n"
"s_cmp_eq_u32 s56, 6\n"
"s_cselect_b32 s56, 0, s56\n"
"s_lshl_b32 s50, s56, 7\n"
"s_add_u32 s50, s50, s57\n"
"s_mov_b32 s49, 0\n"
"s_waitcnt vmcnt(0)\n"
"s_barrier\n"
"ds_read_b128 v[48:51], v6 offset:0\n"
"ds_read_b128 v[52:55], v6 offset:32\n"
"ds_read_b128 v[56:59], v6 offset:64\n"
"ds_read_b128 v[60:63], v6 offset:96\n"
"ds_read_b128 v[64:67], v6 offset:128\n"
"ds_read_b128 v[68:71], v6 offset:160\n"
"ds_read_b64_tr_b16 v[16:17], v0 offset:0\n"
"ds_read_b64_tr_b16 v[18:19], v0 offset:256\n"
"ds_read_b64_tr_b16 v[20:21], v0 offset:2048\n"
"ds_read_b64_tr_b16 v[22:23], v0 offset:2304\n"
"ds_read_b64_tr_b16 v[24:25], v0 offset:4096\n"
"ds_read_b64_tr_b16 v[26:27], v0 offset:4352\n"
"ds_read_b64_tr_b16 v[28:29], v0 offset:6144\n"
"ds_read_b64_tr_b16 v[30:31], v0 offset:6400\n"
"s_waitcnt lgkmcnt(0)\n"
"LOOP_%=:\n"
"s_waitcnt lgkmcnt(4)\n"
"v_mfma_f32_32x32x16_bf16 v[48:63], v[16:19], v[80:83], v[48:63]\n"
"ds_read_b64_tr_b16 v[16:17], v0 offset:8192\n"
"ds_read_b64_tr_b16 v[18:19], v0 offset:8448\n"
"v_exp_f32 v8, v72\n"
"v_mfma_f32_32x32x16_bf16 v[48:63], v[20:23], v[84:87], v[48:63]\n"
"ds_read_b64_tr_b16 v[20:21], v0 offset:10240\n"
"ds_read_b64_tr_b16 v[22:23], v0 offset:10496\n"
"v_exp_f32 v10, v73\n"
"s_waitcnt lgkmcnt(4)\n"
"v_mfma_f32_32x32x16_bf16 v[48:63], v[24:27], v[88:91], v[48:63]\n"
"ds_read_b64_tr_b16 v[24:25], v0 offset:12288\n"
"ds_read_b64_tr_b16 v[26:27], v0 offset:12544\n"
"v_fma_f32 v9, v8, -0.5, -1.0\n"
"v_fma_f32 v11, v10, -0.5, -1.0\n"
"v_fma_f32 v9, v8, v9, -1.0\n"
"v_mfma_f32_32x32x16_bf16 v[48:63], v[28:31], v[92:95], v[48:63]\n"
"ds_read_b64_tr_b16 v[28:29], v0 offset:14336\n"
"ds_read_b64_tr_b16 v[30:31], v0 offset:14592\n"
"v_fma_f32 v11, v10, v11, -1.0\n"
"s_waitcnt lgkmcnt(4)\n"
"v_mfma_f32_32x32x16_bf16 v[48:63], v[16:19], v[96:99], v[48:63]\n"
"s_add_u32 s48, s51, s55\n"
"s_add_u32 m0, s53, 110592\n"
"ds_read_b64_tr_b16 v[16:17], v0 offset:16384\n"
"ds_read_b64_tr_b16 v[18:19], v0 offset:16640\n"
"buffer_load_dwordx4 v5, s[44:47], s48 offen offset:0 lds\n"
"v_rcp_f32 v9, v9\n"
"v_mfma_f32_32x32x16_bf16 v[48:63], v[20:23], v[100:103], v[48:63]\n"
"ds_read_b64_tr_b16 v[20:21], v0 offset:18432\n"
"ds_read_b64_tr_b16 v[22:23], v0 offset:18688\n"
"v_rcp_f32 v11, v11\n"
"s_waitcnt lgkmcnt(4)\n"
"v_mfma_f32_32x32x16_bf16 v[48:63], v[24:27], v[104:107], v[48:63]\n"
"ds_read_b64_tr_b16 v[24:25], v0 offset:20480\n"
"ds_read_b64_tr_b16 v[26:27], v0 offset:20736\n"
"v_fma_f32 v8, v72, v9, v72\n"
"v_fma_f32 v10, v73, v11, v73\n"
"v_mfma_f32_32x32x16_bf16 v[48:63], v[28:31], v[108:111], v[48:63]\n"
"s_add_u32 m0, m0, 896\n"
"ds_read_b64_tr_b16 v[28:29], v0 offset:22528\n"
"ds_read_b64_tr_b16 v[30:31], v0 offset:22784\n"
"buffer_load_dwordx4 v5, s[44:47], s48 offen offset:128 lds\n"
"v_cvt_pk_bf16_f32 v44, v8, v10\n"
"s_waitcnt lgkmcnt(4)\n"
"v_mfma_f32_32x32x16_bf16 v[48:63], v[16:19], v[112:115], v[48:63]\n"
"ds_read_b64_tr_b16 v[16:17], v0 offset:24576\n"
"ds_read_b64_tr_b16 v[18:19], v0 offset:24832\n"
"v_exp_f32 v12, v74\n"
"v_mfma_f32_32x32x16_bf16 v[48:63], v[20:23], v[116:119], v[48:63]\n"
"ds_read_b64_tr_b16 v[20:21], v0 offset:26624\n"
"ds_read_b64_tr_b16 v[22:23], v0 offset:26880\n"
"v_exp_f32 v14, v75\n"
"v_fma_f32 v13, v12, -0.5, -1.0\n"
"s_waitcnt lgkmcnt(4)\n"
"v_mfma_f32_32x32x16_bf16 v[48:63], v[24:27], v[120:123], v[48:63]\n"
"s_add_u32 m0, m0, 896\n"
"ds_read_b64_tr_b16 v[24:25], v0 offset:28672\n"
"ds_read_b64_tr_b16 v[26:27], v0 offset:28928\n"
"buffer_load_dwordx4 v5, s[44:47], s48 offen offset:256 lds\n"
"v_fma_f32 v15, v14, -0.5, -1.0\n"
"v_mfma_f32_32x32x16_bf16 v[48:63], v[28:31], v[124:127], v[48:63]\n"
"ds_read_b64_tr_b16 v[28:29], v0 offset:30720\n"
"ds_read_b64_tr_b16 v[30:31], v0 offset:30976\n"
"v_fma_f32 v13, v12, v13, -1.0\n"
"v_fma_f32 v15, v14, v15, -1.0\n"
"s_waitcnt lgkmcnt(4)\n"
"v_mfma_f32_32x32x16_bf16 v[48:63], v[16:19], v[128:131], v[48:63]\n"
"ds_read_b64_tr_b16 v[16:17], v0 offset:32768\n"
"ds_read_b64_tr_b16 v[18:19], v0 offset:33024\n"
"v_rcp_f32 v13, v13\n"
"v_mfma_f32_32x32x16_bf16 v[48:63], v[20:23], v[132:135], v[48:63]\n"
"ds_read_b64_tr_b16 v[20:21], v0 offset:34816\n"
"ds_read_b64_tr_b16 v[22:23], v0 offset:35072\n"
"v_rcp_f32 v15, v15\n"
"s_waitcnt lgkmcnt(4)\n"
"v_mfma_f32_32x32x16_bf16 v[48:63], v[24:27], v[136:139], v[48:63]\n"
"s_add_u32 m0, m0, 896\n"
"ds_read_b64_tr_b16 v[24:25], v1 offset:0\n"
"ds_read_b64_tr_b16 v[26:27], v1 offset:256\n"
"buffer_load_dwordx4 v5, s[44:47], s48 offen offset:384 lds\n"
"v_fma_f32 v12, v74, v13, v74\n"
"v_mfma_f32_32x32x16_bf16 v[48:63], v[28:31], v[140:143], v[48:63]\n"
"ds_read_b64_tr_b16 v[28:29], v1 offset:2048\n"
"ds_read_b64_tr_b16 v[30:31], v1 offset:2304\n"
"v_fma_f32 v14, v75, v15, v75\n"
"v_cvt_pk_bf16_f32 v45, v12, v14\n"
"s_waitcnt lgkmcnt(4)\n"
"v_mfma_f32_32x32x16_bf16 v[48:63], v[16:19], v[144:147], v[48:63]\n"
"ds_read_b64_tr_b16 v[16:17], v1 offset:4096\n"
"ds_read_b64_tr_b16 v[18:19], v1 offset:4352\n"
"ds_read_b128 v[72:75], v6 offset:192\n"
"v_exp_f32 v8, v76\n"
"v_mfma_f32_32x32x16_bf16 v[48:63], v[20:23], v[148:151], v[48:63]\n"
"s_add_u32 m0, m0, 896\n"
"ds_read_b64_tr_b16 v[20:21], v1 offset:6144\n"
"ds_read_b64_tr_b16 v[22:23], v1 offset:6400\n"
"buffer_load_dwordx4 v5, s[44:47], s48 offen offset:512 lds\n"
"s_waitcnt lgkmcnt(5)\n"
"v_mfma_f32_32x32x16_bf16 v[48:63], v[24:27], v[152:155], v[48:63]\n"
"ds_read_b64_tr_b16 v[24:25], v1 offset:8192\n"
"ds_read_b64_tr_b16 v[26:27], v1 offset:8448\n"
"v_exp_f32 v10, v77\n"
"v_fma_f32 v9, v8, -0.5, -1.0\n"
"v_mfma_f32_32x32x16_bf16 v[48:63], v[28:31], v[156:159], v[48:63]\n"
"ds_read_b64_tr_b16 v[28:29], v1 offset:10240\n"
"ds_read_b64_tr_b16 v[30:31], v1 offset:10496\n"
"v_fma_f32 v11, v10, -0.5, -1.0\n"
"v_fma_f32 v9, v8, v9, -1.0\n"
"s_waitcnt lgkmcnt(4)\n"
"v_mfma_f32_32x32x16_bf16 v[48:63], v[16:19], v[160:163], v[48:63]\n"
"s_add_u32 m0, m0, 896\n"
"ds_read_b64_tr_b16 v[16:17], v1 offset:12288\n"
"ds_read_b64_tr_b16 v[18:19], v1 offset:12544\n"
"buffer_load_dwordx4 v5, s[44:47], s48 offen offset:640 lds\n"
"v_fma_f32 v11, v10, v11, -1.0\n"
"v_mfma_f32_32x32x16_bf16 v[48:63], v[20:23], v[164:167], v[48:63]\n"
"ds_read_b64_tr_b16 v[20:21], v1 offset:14336\n"
"ds_read_b64_tr_b16 v[22:23], v1 offset:14592\n"
"v_rcp_f32 v9, v9\n"
"s_waitcnt lgkmcnt(4)\n"
"v_mfma_f32_32x32x16_bf16 v[48:63], v[24:27], v[168:171], v[48:63]\n"
"ds_read_b64_tr_b16 v[24:25], v1 offset:16384\n"
"ds_read_b64_tr_b16 v[26:27], v1 offset:16640\n"
"v_rcp_f32 v11, v11\n"
"v_fma_f32 v8, v76, v9, v76\n"
"v_mfma_f32_32x32x16_bf16 v[48:63], v[28:31], v[172:175], v[48:63]\n"
"ds_read_b64_tr_b16 v[28:29], v1 offset:18432\n"
"ds_read_b64_tr_b16 v[30:31], v1 offset:18688\n"
"v_fma_f32 v10, v77, v11, v77\n"
"v_cvt_pk_bf16_f32 v46, v8, v10\n"
"s_waitcnt lgkmcnt(4)\n"
"v_mfma_f32_32x32x16_bf16 v[48:63], v[16:19], v[176:179], v[48:63]\n"
"s_add_u32 m0, m0, 896\n"
"ds_read_b64_tr_b16 v[16:17], v1 offset:20480\n"
"ds_read_b64_tr_b16 v[18:19], v1 offset:20736\n"
"buffer_load_dwordx4 v5, s[44:47], s48 offen offset:768 lds\n"
"v_mfma_f32_32x32x16_bf16 v[48:63], v[20:23], v[180:183], v[48:63]\n"
"ds_read_b64_tr_b16 v[20:21], v1 offset:22528\n"
"ds_read_b64_tr_b16 v[22:23], v1 offset:22784\n"
"v_exp_f32 v12, v78\n"
"s_waitcnt lgkmcnt(4)\n"
"v_mfma_f32_32x32x16_bf16 v[48:63], v[24:27], v[184:187], v[48:63]\n"
"ds_read_b64_tr_b16 v[24:25], v1 offset:24576\n"
"ds_read_b64_tr_b16 v[26:27], v1 offset:24832\n"
"v_exp_f32 v14, v79\n"
"v_fma_f32 v13, v12, -0.5, -1.0\n"
"v_mfma_f32_32x32x16_bf16 v[48:63], v[28:31], v[188:191], v[48:63]\n"
"s_add_u32 m0, m0, 896\n"
"ds_read_b64_tr_b16 v[28:29], v1 offset:26624\n"
"ds_read_b64_tr_b16 v[30:31], v1 offset:26880\n"
"buffer_load_dwordx4 v5, s[44:47], s48 offen offset:896 lds\n"
"v_fma_f32 v15, v14, -0.5, -1.0\n"
"s_waitcnt lgkmcnt(4)\n"
"v_mfma_f32_32x32x16_bf16 v[48:63], v[16:19], v[192:195], v[48:63]\n"
"ds_read_b64_tr_b16 v[16:17], v1 offset:28672\n"
"ds_read_b64_tr_b16 v[18:19], v1 offset:28928\n"
"v_fma_f32 v13, v12, v13, -1.0\n"
"v_fma_f32 v15, v14, v15, -1.0\n"
"v_mfma_f32_32x32x16_bf16 v[48:63], v[20:23], v[196:199], v[48:63]\n"
"ds_read_b64_tr_b16 v[20:21], v1 offset:30720\n"
"ds_read_b64_tr_b16 v[22:23], v1 offset:30976\n"
"v_rcp_f32 v13, v13\n"
"s_waitcnt lgkmcnt(4)\n"
"v_mfma_f32_32x32x16_bf16 v[48:63], v[24:27], v[200:203], v[48:63]\n"
"s_add_u32 m0, m0, 896\n"
"ds_read_b64_tr_b16 v[24:25], v1 offset:32768\n"
"ds_read_b64_tr_b16 v[26:27], v1 offset:33024\n"
"buffer_load_dwordx4 v5, s[44:47], s48 offen offset:1024 lds\n"
"v_rcp_f32 v15, v15\n"
"v_mfma_f32_32x32x16_bf16 v[48:63], v[28:31], v[204:207], v[48:63]\n"
"ds_read_b64_tr_b16 v[28:29], v1 offset:34816\n"
"ds_read_b64_tr_b16 v[30:31], v1 offset:35072\n"
"v_fma_f32 v12, v78, v13, v78\n"
"v_fma_f32 v14, v79, v15, v79\n"
"s_waitcnt lgkmcnt(4)\n"
"v_mfma_f32_32x32x16_bf16 v[48:63], v[16:19], v[208:211], v[48:63]\n"
"ds_read_b64_tr_b16 v[16:17], v0 offset:512\n"
"ds_read_b64_tr_b16 v[18:19], v0 offset:768\n"
"v_cvt_pk_bf16_f32 v47, v12, v14\n"
"ds_read_b128 v[76:79], v6 offset:224\n"
"v_mfma_f32_32x32x16_bf16 v[48:63], v[20:23], v[212:215], v[48:63]\n"
"ds_read_b64_tr_b16 v[20:21], v0 offset:2560\n"
"ds_read_b64_tr_b16 v[22:23], v0 offset:2816\n"
"s_waitcnt lgkmcnt(5)\n"
"v_mfma_f32_32x32x16_bf16 v[48:63], v[24:27], v[216:219], v[48:63]\n"
"ds_read_b64_tr_b16 v[24:25], v0 offset:4608\n"
"ds_read_b64_tr_b16 v[26:27], v0 offset:4864\n"
"v_mfma_f32_32x32x16_bf16 v[48:63], v[28:31], v[220:223], v[48:63]\n"
"ds_read_b64_tr_b16 v[28:29], v0 offset:6656\n"
"ds_read_b64_tr_b16 v[30:31], v0 offset:6912\n"
"s_waitcnt lgkmcnt(4)\n"
"v_mfma_f32_32x32x16_bf16 v[64:79], v[16:19], v[80:83], v[64:79]\n"
"ds_read_b64_tr_b16 v[16:17], v0 offset:8704\n"
"ds_read_b64_tr_b16 v[18:19], v0 offset:8960\n"
"v_mfma_f32_32x32x16_bf16 v[64:79], v[20:23], v[84:87], v[64:79]\n"
"ds_read_b64_tr_b16 v[20:21], v0 offset:10752\n"
"ds_read_b64_tr_b16 v[22:23], v0 offset:11008\n"
"s_lshl_b32 s59, s56, 8\n"
"s_lshl_b32 s62, s52, 8\n"
"s_sub_u32 s59, s59, s62\n"
"v_add_u32 v6, s59, v6\n"
"s_waitcnt lgkmcnt(4)\n"
"v_mfma_f32_32x32x16_bf16 v[64:79], v[24:27], v[88:91], v[64:79]\n"
"ds_read_b64_tr_b16 v[24:25], v0 offset:12800\n"
"ds_read_b64_tr_b16 v[26:27], v0 offset:13056\n"
"s_cmp_eq_u32 s49, 5\n"
"s_cselect_b32 s42, 0, s42\n"
"v_mfma_f32_32x32x16_bf16 v[64:79], v[28:31], v[92:95], v[64:79]\n"
"ds_read_b64_tr_b16 v[28:29], v0 offset:14848\n"
"ds_read_b64_tr_b16 v[30:31], v0 offset:15104\n"
"s_waitcnt lgkmcnt(4)\n"
"v_mfma_f32_32x32x16_bf16 v[64:79], v[16:19], v[96:99], v[64:79]\n"
"ds_read_b64_tr_b16 v[16:17], v0 offset:16896\n"
"ds_read_b64_tr_b16 v[18:19], v0 offset:17152\n"
"v_exp_f32 v8, v48\n"
"v_mfma_f32_32x32x16_bf16 v[64:79], v[20:23], v[100:103], v[64:79]\n"
"ds_read_b64_tr_b16 v[20:21], v0 offset:18944\n"
"ds_read_b64_tr_b16 v[22:23], v0 offset:19200\n"
"v_exp_f32 v10, v49\n"
"v_fma_f32 v9, v8, -0.5, -1.0\n"
"s_waitcnt lgkmcnt(4)\n"
"v_mfma_f32_32x32x16_bf16 v[64:79], v[24:27], v[104:107], v[64:79]\n"
"ds_read_b64_tr_b16 v[24:25], v0 offset:20992\n"
"ds_read_b64_tr_b16 v[26:27], v0 offset:21248\n"
"v_fma_f32 v11, v10, -0.5, -1.0\n"
"v_fma_f32 v9, v8, v9, -1.0\n"
"v_mfma_f32_32x32x16_bf16 v[64:79], v[28:31], v[108:111], v[64:79]\n"
"ds_read_b64_tr_b16 v[28:29], v0 offset:23040\n"
"ds_read_b64_tr_b16 v[30:31], v0 offset:23296\n"
"v_fma_f32 v11, v10, v11, -1.0\n"
"v_rcp_f32 v9, v9\n"
"s_waitcnt lgkmcnt(4)\n"
"v_mfma_f32_32x32x16_bf16 v[64:79], v[16:19], v[112:115], v[64:79]\n"
"ds_read_b64_tr_b16 v[16:17], v0 offset:25088\n"
"ds_read_b64_tr_b16 v[18:19], v0 offset:25344\n"
"v_rcp_f32 v11, v11\n"
"v_mfma_f32_32x32x16_bf16 v[64:79], v[20:23], v[116:119], v[64:79]\n"
"ds_read_b64_tr_b16 v[20:21], v0 offset:27136\n"
"ds_read_b64_tr_b16 v[22:23], v0 offset:27392\n"
"v_fma_f32 v8, v48, v9, v48\n"
"v_fma_f32 v10, v49, v11, v49\n"
"s_waitcnt lgkmcnt(4)\n"
"v_mfma_f32_32x32x16_bf16 v[64:79], v[24:27], v[120:123], v[64:79]\n"
"ds_read_b64_tr_b16 v[24:25], v0 offset:29184\n"
"ds_read_b64_tr_b16 v[26:27], v0 offset:29440\n"
"v_cvt_pk_bf16_f32 v32, v8, v10\n"
"v_mfma_f32_32x32x16_bf16 v[64:79], v[28:31], v[124:127], v[64:79]\n"
"ds_read_b64_tr_b16 v[28:29], v0 offset:31232\n"
"ds_read_b64_tr_b16 v[30:31], v0 offset:31488\n"
"v_exp_f32 v12, v50\n"
"v_exp_f32 v14, v51\n"
"s_waitcnt lgkmcnt(4)\n"
"v_mfma_f32_32x32x16_bf16 v[64:79], v[16:19], v[128:131], v[64:79]\n"
"ds_read_b64_tr_b16 v[16:17], v0 offset:33280\n"
"ds_read_b64_tr_b16 v[18:19], v0 offset:33536\n"
"v_fma_f32 v13, v12, -0.5, -1.0\n"
"v_mfma_f32_32x32x16_bf16 v[64:79], v[20:23], v[132:135], v[64:79]\n"
"ds_read_b64_tr_b16 v[20:21], v0 offset:35328\n"
"ds_read_b64_tr_b16 v[22:23], v0 offset:35584\n"
"v_fma_f32 v15, v14, -0.5, -1.0\n"
"v_fma_f32 v13, v12, v13, -1.0\n"
"v_fma_f32 v15, v14, v15, -1.0\n"
"s_waitcnt lgkmcnt(4)\n"
"v_mfma_f32_32x32x16_bf16 v[64:79], v[24:27], v[136:139], v[64:79]\n"
"ds_read_b64_tr_b16 v[24:25], v1 offset:512\n"
"ds_read_b64_tr_b16 v[26:27], v1 offset:768\n"
"v_rcp_f32 v13, v13\n"
"v_mfma_f32_32x32x16_bf16 v[64:79], v[28:31], v[140:143], v[64:79]\n"
"ds_read_b64_tr_b16 v[28:29], v1 offset:2560\n"
"ds_read_b64_tr_b16 v[30:31], v1 offset:2816\n"
"v_rcp_f32 v15, v15\n"
"s_waitcnt lgkmcnt(4)\n"
"v_mfma_f32_32x32x16_bf16 v[64:79], v[16:19], v[144:147], v[64:79]\n"
"ds_read_b64_tr_b16 v[16:17], v1 offset:4608\n"
"ds_read_b64_tr_b16 v[18:19], v1 offset:4864\n"
"v_fma_f32 v12, v50, v13, v50\n"
"v_fma_f32 v14, v51, v15, v51\n"
"v_mfma_f32_32x32x16_bf16 v[64:79], v[20:23], v[148:151], v[64:79]\n"
"ds_read_b64_tr_b16 v[20:21], v1 offset:6656\n"
"ds_read_b64_tr_b16 v[22:23], v1 offset:6912\n"
"v_cvt_pk_bf16_f32 v33, v12, v14\n"
"ds_read_b128 v[48:51], v6 offset:0\n"
"s_waitcnt lgkmcnt(5)\n"
"v_mfma_f32_32x32x16_bf16 v[64:79], v[24:27], v[152:155], v[64:79]\n"
"s_barrier\n"
"ds_read_b64_tr_b16 v[24:25], v1 offset:8704\n"
"ds_read_b64_tr_b16 v[26:27], v1 offset:8960\n"
"v_exp_f32 v8, v52\n"
"v_mfma_f32_32x32x16_bf16 v[64:79], v[28:31], v[156:159], v[64:79]\n"
"s_add_u32 s40, s34, s50\n"
"s_addc_u32 s41, s35, 0\n"
"s_add_u32 m0, s53, 0\n"
"ds_read_b64_tr_b16 v[28:29], v1 offset:10752\n"
"ds_read_b64_tr_b16 v[30:31], v1 offset:11008\n"
"buffer_load_dwordx4 v4, s[40:43], s24 offen lds\n"
"v_exp_f32 v10, v53\n"
"s_waitcnt lgkmcnt(5)\n"
"v_mfma_f32_32x32x16_bf16 v[64:79], v[16:19], v[160:163], v[64:79]\n"
"s_add_u32 m0, m0, 1024\n"
"ds_read_b64_tr_b16 v[16:17], v1 offset:12800\n"
"ds_read_b64_tr_b16 v[18:19], v1 offset:13056\n"
"buffer_load_dwordx4 v4, s[40:43], s25 offen lds\n"
"v_fma_f32 v9, v8, -0.5, -1.0\n"
"v_mfma_f32_32x32x16_bf16 v[64:79], v[20:23], v[164:167], v[64:79]\n"
"s_add_u32 m0, m0, 1024\n"
"ds_read_b64_tr_b16 v[20:21], v1 offset:14848\n"
"ds_read_b64_tr_b16 v[22:23], v1 offset:15104\n"
"buffer_load_dwordx4 v4, s[40:43], s26 offen lds\n"
"v_fma_f32 v11, v10, -0.5, -1.0\n"
"s_waitcnt lgkmcnt(4)\n"
"v_mfma_f32_32x32x16_bf16 v[64:79], v[24:27], v[168:171], v[64:79]\n"
"ds_read_b64_tr_b16 v[24:25], v1 offset:16896\n"
"ds_read_b64_tr_b16 v[26:27], v1 offset:17152\n"
"v_fma_f32 v9, v8, v9, -1.0\n"
"v_fma_f32 v11, v10, v11, -1.0\n"
"v_mfma_f32_32x32x16_bf16 v[64:79], v[28:31], v[172:175], v[64:79]\n"
"s_add_u32 m0, m0, 1024\n"
"ds_read_b64_tr_b16 v[28:29], v1 offset:18944\n"
"ds_read_b64_tr_b16 v[30:31], v1 offset:19200\n"
"buffer_load_dwordx4 v4, s[40:43], s27 offen lds\n"
"v_rcp_f32 v9, v9\n"
"s_waitcnt lgkmcnt(4)\n"
"v_mfma_f32_32x32x16_bf16 v[64:79], v[16:19], v[176:179], v[64:79]\n"
"s_add_u32 m0, m0, 1024\n"
"ds_read_b64_tr_b16 v[16:17], v1 offset:20992\n"
"ds_read_b64_tr_b16 v[18:19], v1 offset:21248\n"
"buffer_load_dwordx4 v4, s[40:43], s28 offen lds\n"
"v_mfma_f32_32x32x16_bf16 v[64:79], v[20:23], v[180:183], v[64:79]\n"
"ds_read_b64_tr_b16 v[20:21], v1 offset:23040\n"
"ds_read_b64_tr_b16 v[22:23], v1 offset:23296\n"
"v_rcp_f32 v11, v11\n"
"v_fma_f32 v8, v52, v9, v52\n"
"s_waitcnt lgkmcnt(4)\n"
"v_mfma_f32_32x32x16_bf16 v[64:79], v[24:27], v[184:187], v[64:79]\n"
"s_add_u32 m0, m0, 1024\n"
"ds_read_b64_tr_b16 v[24:25], v1 offset:25088\n"
"ds_read_b64_tr_b16 v[26:27], v1 offset:25344\n"
"buffer_load_dwordx4 v4, s[40:43], s29 offen lds\n"
"v_fma_f32 v10, v53, v11, v53\n"
"v_mfma_f32_32x32x16_bf16 v[64:79], v[28:31], v[188:191], v[64:79]\n"
"s_add_u32 m0, m0, 1024\n"
"ds_read_b64_tr_b16 v[28:29], v1 offset:27136\n"
"ds_read_b64_tr_b16 v[30:31], v1 offset:27392\n"
"buffer_load_dwordx4 v4, s[40:43], s30 offen lds\n"
"v_cvt_pk_bf16_f32 v34, v8, v10\n"
"s_waitcnt lgkmcnt(4)\n"
"v_mfma_f32_32x32x16_bf16 v[64:79], v[16:19], v[192:195], v[64:79]\n"
"ds_read_b64_tr_b16 v[16:17], v1 offset:29184\n"
"ds_read_b64_tr_b16 v[18:19], v1 offset:29440\n"
"v_exp_f32 v12, v54\n"
"v_mfma_f32_32x32x16_bf16 v[64:79], v[20:23], v[196:199], v[64:79]\n"
"s_add_u32 m0, m0, 1024\n"
"ds_read_b64_tr_b16 v[20:21], v1 offset:31232\n"
"ds_read_b64_tr_b16 v[22:23], v1 offset:31488\n"
"buffer_load_dwordx4 v4, s[40:43], s31 offen lds\n"
"v_exp_f32 v14, v55\n"
"s_waitcnt lgkmcnt(4)\n"
"v_mfma_f32_32x32x16_bf16 v[64:79], v[24:27], v[200:203], v[64:79]\n"
"s_add_u32 m0, m0, 1024\n"
"ds_read_b64_tr_b16 v[24:25], v1 offset:33280\n"
"ds_read_b64_tr_b16 v[26:27], v1 offset:33536\n"
"buffer_load_dwordx4 v4, s[40:43], s32 offen lds\n"
"v_fma_f32 v13, v12, -0.5, -1.0\n"
"v_mfma_f32_32x32x16_bf16 v[64:79], v[28:31], v[204:207], v[64:79]\n"
"ds_read_b64_tr_b16 v[28:29], v1 offset:35328\n"
"ds_read_b64_tr_b16 v[30:31], v1 offset:35584\n"
"v_fma_f32 v15, v14, -0.5, -1.0\n"
"v_fma_f32 v13, v12, v13, -1.0\n"
"s_waitcnt lgkmcnt(4)\n"
"v_mfma_f32_32x32x16_bf16 v[64:79], v[16:19], v[208:211], v[64:79]\n"
"s_waitcnt vmcnt(18)\n"
"s_barrier\n"
"ds_read_b64_tr_b16 v[16:17], v2 offset:0\n"
"ds_read_b64_tr_b16 v[18:19], v2 offset:9216\n"
"v_fma_f32 v15, v14, v15, -1.0\n"
"v_rcp_f32 v13, v13\n"
"v_mfma_f32_32x32x16_bf16 v[64:79], v[20:23], v[212:215], v[64:79]\n"
"ds_read_b64_tr_b16 v[20:21], v2 offset:512\n"
"ds_read_b64_tr_b16 v[22:23], v2 offset:9728\n"
"v_rcp_f32 v15, v15\n"
"s_waitcnt lgkmcnt(4)\n"
"v_mfma_f32_32x32x16_bf16 v[64:79], v[24:27], v[216:219], v[64:79]\n"
"ds_read_b64_tr_b16 v[24:25], v2 offset:1024\n"
"ds_read_b64_tr_b16 v[26:27], v2 offset:10240\n"
"v_fma_f32 v12, v54, v13, v54\n"
"v_fma_f32 v14, v55, v15, v55\n"
"v_mfma_f32_32x32x16_bf16 v[64:79], v[28:31], v[220:223], v[64:79]\n"
"ds_read_b64_tr_b16 v[28:29], v2 offset:1536\n"
"ds_read_b64_tr_b16 v[30:31], v2 offset:10752\n"
"v_cvt_pk_bf16_f32 v35, v12, v14\n"
"ds_read_b128 v[52:55], v6 offset:32\n"
"s_cmp_eq_u32 s49, 0\n"
"s_cbranch_scc1 FIRST_%=\n"
"s_waitcnt lgkmcnt(5)\n"
"v_mfma_f32_32x32x16_bf16 a[0:15], v[16:19], v[40:43], a[0:15]\n"
"ds_read_b64_tr_b16 v[16:17], v2 offset:2048\n"
"ds_read_b64_tr_b16 v[18:19], v2 offset:11264\n"
"v_exp_f32 v8, v56\n"
"v_mfma_f32_32x32x16_bf16 a[16:31], v[20:23], v[40:43], a[16:31]\n"
"ds_read_b64_tr_b16 v[20:21], v2 offset:2560\n"
"ds_read_b64_tr_b16 v[22:23], v2 offset:11776\n"
"v_exp_f32 v10, v57\n"
"s_waitcnt lgkmcnt(5)\n"
"v_mfma_f32_32x32x16_bf16 a[32:47], v[24:27], v[40:43], a[32:47]\n"
"s_add_u32 s40, s40, 2654208\n"
"s_addc_u32 s41, s41, 0\n"
"s_add_u32 m0, s53, 36864\n"
"ds_read_b64_tr_b16 v[24:25], v2 offset:3072\n"
"ds_read_b64_tr_b16 v[26:27], v2 offset:12288\n"
"buffer_load_dwordx4 v4, s[40:43], s24 offen lds\n"
"v_fma_f32 v9, v8, -0.5, -1.0\n"
"v_mfma_f32_32x32x16_bf16 a[48:63], v[28:31], v[40:43], a[48:63]\n"
"ds_read_b64_tr_b16 v[28:29], v2 offset:3584\n"
"ds_read_b64_tr_b16 v[30:31], v2 offset:12800\n"
"v_fma_f32 v11, v10, -0.5, -1.0\n"
"v_fma_f32 v9, v8, v9, -1.0\n"
"s_waitcnt lgkmcnt(4)\n"
"v_mfma_f32_32x32x16_bf16 a[64:79], v[16:19], v[40:43], a[64:79]\n"
"ds_read_b64_tr_b16 v[16:17], v2 offset:4096\n"
"ds_read_b64_tr_b16 v[18:19], v2 offset:13312\n"
"v_fma_f32 v11, v10, v11, -1.0\n"
"v_rcp_f32 v9, v9\n"
"v_mfma_f32_32x32x16_bf16 a[80:95], v[20:23], v[40:43], a[80:95]\n"
"s_add_u32 m0, m0, 1024\n"
"ds_read_b64_tr_b16 v[20:21], v2 offset:4608\n"
"ds_read_b64_tr_b16 v[22:23], v2 offset:13824\n"
"buffer_load_dwordx4 v4, s[40:43], s25 offen lds\n"
"s_waitcnt lgkmcnt(4)\n"
"v_mfma_f32_32x32x16_bf16 a[96:111], v[24:27], v[40:43], a[96:111]\n"
"ds_read_b64_tr_b16 v[24:25], v2 offset:5120\n"
"ds_read_b64_tr_b16 v[26:27], v2 offset:14336\n"
"v_rcp_f32 v11, v11\n"
"v_mfma_f32_32x32x16_bf16 a[112:127], v[28:31], v[40:43], a[112:127]\n"
"ds_read_b64_tr_b16 v[28:29], v2 offset:5632\n"
"ds_read_b64_tr_b16 v[30:31], v2 offset:14848\n"
"v_fma_f32 v8, v56, v9, v56\n"
"v_fma_f32 v10, v57, v11, v57\n"
"s_waitcnt lgkmcnt(4)\n"
"v_mfma_f32_32x32x16_bf16 a[128:143], v[16:19], v[40:43], a[128:143]\n"
"s_add_u32 m0, m0, 1024\n"
"ds_read_b64_tr_b16 v[16:17], v2 offset:6144\n"
"ds_read_b64_tr_b16 v[18:19], v2 offset:15360\n"
"buffer_load_dwordx4 v4, s[40:43], s26 offen lds\n"
"v_cvt_pk_bf16_f32 v36, v8, v10\n"
"v_mfma_f32_32x32x16_bf16 a[144:159], v[20:23], v[40:43], a[144:159]\n"
"ds_read_b64_tr_b16 v[20:21], v2 offset:6656\n"
"ds_read_b64_tr_b16 v[22:23], v2 offset:15872\n"
"v_exp_f32 v12, v58\n"
"s_waitcnt lgkmcnt(4)\n"
"v_mfma_f32_32x32x16_bf16 a[160:175], v[24:27], v[40:43], a[160:175]\n"
"ds_read_b64_tr_b16 v[24:25], v2 offset:7168\n"
"ds_read_b64_tr_b16 v[26:27], v2 offset:16384\n"
"v_exp_f32 v14, v59\n"
"v_mfma_f32_32x32x16_bf16 a[176:191], v[28:31], v[40:43], a[176:191]\n"
"ds_read_b64_tr_b16 v[28:29], v2 offset:7680\n"
"ds_read_b64_tr_b16 v[30:31], v2 offset:16896\n"
"v_fma_f32 v13, v12, -0.5, -1.0\n"
"v_fma_f32 v15, v14, -0.5, -1.0\n"
"s_waitcnt lgkmcnt(4)\n"
"v_mfma_f32_32x32x16_bf16 a[192:207], v[16:19], v[40:43], a[192:207]\n"
"s_add_u32 m0, m0, 1024\n"
"ds_read_b64_tr_b16 v[16:17], v2 offset:8192\n"
"ds_read_b64_tr_b16 v[18:19], v2 offset:17408\n"
"buffer_load_dwordx4 v4, s[40:43], s27 offen lds\n"
"v_fma_f32 v13, v12, v13, -1.0\n"
"v_mfma_f32_32x32x16_bf16 a[208:223], v[20:23], v[40:43], a[208:223]\n"
"ds_read_b64_tr_b16 v[20:21], v2 offset:8704\n"
"ds_read_b64_tr_b16 v[22:23], v2 offset:17920\n"
"v_fma_f32 v15, v14, v15, -1.0\n"
"v_rcp_f32 v13, v13\n"
"s_waitcnt lgkmcnt(4)\n"
"v_mfma_f32_32x32x16_bf16 a[224:239], v[24:27], v[40:43], a[224:239]\n"
"ds_read_b64_tr_b16 v[24:25], v2 offset:18432\n"
"ds_read_b64_tr_b16 v[26:27], v2 offset:27648\n"
"v_rcp_f32 v15, v15\n"
"v_mfma_f32_32x32x16_bf16 a[240:255], v[28:31], v[40:43], a[240:255]\n"
"s_add_u32 m0, m0, 1024\n"
"ds_read_b64_tr_b16 v[28:29], v2 offset:18944\n"
"ds_read_b64_tr_b16 v[30:31], v2 offset:28160\n"
"buffer_load_dwordx4 v4, s[40:43], s28 offen lds\n"
"s_waitcnt lgkmcnt(4)\n"
"v_mfma_f32_32x32x16_bf16 v[224:239], v[16:19], v[40:43], v[224:239]\n"
"ds_read_b64_tr_b16 v[16:17], v2 offset:19456\n"
"ds_read_b64_tr_b16 v[18:19], v2 offset:28672\n"
"v_fma_f32 v12, v58, v13, v58\n"
"v_fma_f32 v14, v59, v15, v59\n"
"v_mfma_f32_32x32x16_bf16 v[240:255], v[20:23], v[40:43], v[240:255]\n"
"ds_read_b64_tr_b16 v[20:21], v2 offset:19968\n"
"ds_read_b64_tr_b16 v[22:23], v2 offset:29184\n"
"v_cvt_pk_bf16_f32 v37, v12, v14\n"
"ds_read_b128 v[56:59], v6 offset:64\n"
"s_waitcnt lgkmcnt(5)\n"
"v_mfma_f32_32x32x16_bf16 a[0:15], v[24:27], v[44:47], a[0:15]\n"
"s_add_u32 m0, m0, 1024\n"
"ds_read_b64_tr_b16 v[24:25], v2 offset:20480\n"
"ds_read_b64_tr_b16 v[26:27], v2 offset:29696\n"
"buffer_load_dwordx4 v4, s[40:43], s29 offen lds\n"
"v_exp_f32 v8, v60\n"
"v_mfma_f32_32x32x16_bf16 a[16:31], v[28:31], v[44:47], a[16:31]\n"
"ds_read_b64_tr_b16 v[28:29], v2 offset:20992\n"
"ds_read_b64_tr_b16 v[30:31], v2 offset:30208\n"
"v_exp_f32 v10, v61\n"
"s_waitcnt lgkmcnt(5)\n"
"v_mfma_f32_32x32x16_bf16 a[32:47], v[16:19], v[44:47], a[32:47]\n"
"ds_read_b64_tr_b16 v[16:17], v2 offset:21504\n"
"ds_read_b64_tr_b16 v[18:19], v2 offset:30720\n"
"v_fma_f32 v9, v8, -0.5, -1.0\n"
"v_fma_f32 v11, v10, -0.5, -1.0\n"
"v_mfma_f32_32x32x16_bf16 a[48:63], v[20:23], v[44:47], a[48:63]\n"
"ds_read_b64_tr_b16 v[20:21], v2 offset:22016\n"
"ds_read_b64_tr_b16 v[22:23], v2 offset:31232\n"
"v_fma_f32 v9, v8, v9, -1.0\n"
"v_fma_f32 v11, v10, v11, -1.0\n"
"s_waitcnt lgkmcnt(4)\n"
"v_mfma_f32_32x32x16_bf16 a[64:79], v[24:27], v[44:47], a[64:79]\n"
"s_add_u32 m0, m0, 1024\n"
"ds_read_b64_tr_b16 v[24:25], v2 offset:22528\n"
"ds_read_b64_tr_b16 v[26:27], v2 offset:31744\n"
"buffer_load_dwordx4 v4, s[40:43], s30 offen lds\n"
"v_mfma_f32_32x32x16_bf16 a[80:95], v[28:31], v[44:47], a[80:95]\n"
"ds_read_b64_tr_b16 v[28:29], v2 offset:23040\n"
"ds_read_b64_tr_b16 v[30:31], v2 offset:32256\n"
"v_rcp_f32 v9, v9\n"
"s_waitcnt lgkmcnt(4)\n"
"v_mfma_f32_32x32x16_bf16 a[96:111], v[16:19], v[44:47], a[96:111]\n"
"ds_read_b64_tr_b16 v[16:17], v2 offset:23552\n"
"ds_read_b64_tr_b16 v[18:19], v2 offset:32768\n"
"v_rcp_f32 v11, v11\n"
"v_fma_f32 v8, v60, v9, v60\n"
"v_mfma_f32_32x32x16_bf16 a[112:127], v[20:23], v[44:47], a[112:127]\n"
"s_add_u32 m0, m0, 1024\n"
"ds_read_b64_tr_b16 v[20:21], v2 offset:24064\n"
"ds_read_b64_tr_b16 v[22:23], v2 offset:33280\n"
"buffer_load_dwordx4 v4, s[40:43], s31 offen lds\n"
"v_fma_f32 v10, v61, v11, v61\n"
"s_waitcnt lgkmcnt(4)\n"
"v_mfma_f32_32x32x16_bf16 a[128:143], v[24:27], v[44:47], a[128:143]\n"
"ds_read_b64_tr_b16 v[24:25], v2 offset:24576\n"
"ds_read_b64_tr_b16 v[26:27], v2 offset:33792\n"
"v_cvt_pk_bf16_f32 v38, v8, v10\n"
"v_mfma_f32_32x32x16_bf16 a[144:159], v[28:31], v[44:47], a[144:159]\n"
"ds_read_b64_tr_b16 v[28:29], v2 offset:25088\n"
"ds_read_b64_tr_b16 v[30:31], v2 offset:34304\n"
"v_exp_f32 v12, v62\n"
"s_waitcnt lgkmcnt(4)\n"
"v_mfma_f32_32x32x16_bf16 a[160:175], v[16:19], v[44:47], a[160:175]\n"
"s_add_u32 m0, m0, 1024\n"
"ds_read_b64_tr_b16 v[16:17], v2 offset:25600\n"
"ds_read_b64_tr_b16 v[18:19], v2 offset:34816\n"
"buffer_load_dwordx4 v4, s[40:43], s32 offen lds\n"
"v_exp_f32 v14, v63\n"
"v_mfma_f32_32x32x16_bf16 a[176:191], v[20:23], v[44:47], a[176:191]\n"
"ds_read_b64_tr_b16 v[20:21], v2 offset:26112\n"
"ds_read_b64_tr_b16 v[22:23], v2 offset:35328\n"
"v_fma_f32 v13, v12, -0.5, -1.0\n"
"v_fma_f32 v15, v14, -0.5, -1.0\n"
"s_waitcnt lgkmcnt(4)\n"
"v_mfma_f32_32x32x16_bf16 a[192:207], v[24:27], v[44:47], a[192:207]\n"
"ds_read_b64_tr_b16 v[24:25], v2 offset:26624\n"
"ds_read_b64_tr_b16 v[26:27], v2 offset:35840\n"
"v_fma_f32 v13, v12, v13, -1.0\n"
"v_fma_f32 v15, v14, v15, -1.0\n"
"v_mfma_f32_32x32x16_bf16 a[208:223], v[28:31], v[44:47], a[208:223]\n"
"ds_read_b64_tr_b16 v[28:29], v2 offset:27136\n"
"ds_read_b64_tr_b16 v[30:31], v2 offset:36352\n"
"v_rcp_f32 v13, v13\n"
"s_waitcnt lgkmcnt(4)\n"
"v_mfma_f32_32x32x16_bf16 a[224:239], v[16:19], v[44:47], a[224:239]\n"
"s_waitcnt vmcnt(18)\n"
"s_barrier\n"
"ds_read_b64_tr_b16 v[16:17], v3 offset:0\n"
"ds_read_b64_tr_b16 v[18:19], v3 offset:9216\n"
"v_rcp_f32 v15, v15\n"
"v_mfma_f32_32x32x16_bf16 a[240:255], v[20:23], v[44:47], a[240:255]\n"
"ds_read_b64_tr_b16 v[20:21], v3 offset:512\n"
"ds_read_b64_tr_b16 v[22:23], v3 offset:9728\n"
"v_fma_f32 v12, v62, v13, v62\n"
"v_fma_f32 v14, v63, v15, v63\n"
"s_waitcnt lgkmcnt(4)\n"
"v_mfma_f32_32x32x16_bf16 v[224:239], v[24:27], v[44:47], v[224:239]\n"
"ds_read_b64_tr_b16 v[24:25], v3 offset:1024\n"
"ds_read_b64_tr_b16 v[26:27], v3 offset:10240\n"
"v_cvt_pk_bf16_f32 v39, v12, v14\n"
"ds_read_b128 v[60:63], v6 offset:96\n"
"v_mfma_f32_32x32x16_bf16 v[240:255], v[28:31], v[44:47], v[240:255]\n"
"ds_read_b64_tr_b16 v[28:29], v3 offset:1536\n"
"ds_read_b64_tr_b16 v[30:31], v3 offset:10752\n"
"S3_%=:\n"
"s_waitcnt lgkmcnt(5)\n"
"v_mfma_f32_32x32x16_bf16 a[0:15], v[16:19], v[32:35], a[0:15]\n"
"s_add_u32 s48, s51, s55\n"
"s_add_u32 s48, s48, 36864\n"
"s_add_u32 m0, s53, 73728\n"
"ds_read_b64_tr_b16 v[16:17], v3 offset:2048\n"
"ds_read_b64_tr_b16 v[18:19], v3 offset:11264\n"
"buffer_load_dwordx4 v5, s[44:47], s48 offen offset:0 lds\n"
"v_exp_f32 v8, v64\n"
"v_mfma_f32_32x32x16_bf16 a[16:31], v[20:23], v[32:35], a[16:31]\n"
"ds_read_b64_tr_b16 v[20:21], v3 offset:2560\n"
"ds_read_b64_tr_b16 v[22:23], v3 offset:11776\n"
"v_exp_f32 v10, v65\n"
"s_waitcnt lgkmcnt(4)\n"
"v_mfma_f32_32x32x16_bf16 a[32:47], v[24:27], v[32:35], a[32:47]\n"
"s_add_u32 m0, m0, 896\n"
"ds_read_b64_tr_b16 v[24:25], v3 offset:3072\n"
"ds_read_b64_tr_b16 v[26:27], v3 offset:12288\n"
"buffer_load_dwordx4 v5, s[44:47], s48 offen offset:128 lds\n"
"v_fma_f32 v9, v8, -0.5, -1.0\n"
"v_mfma_f32_32x32x16_bf16 a[48:63], v[28:31], v[32:35], a[48:63]\n"
"ds_read_b64_tr_b16 v[28:29], v3 offset:3584\n"
"ds_read_b64_tr_b16 v[30:31], v3 offset:12800\n"
"v_fma_f32 v11, v10, -0.5, -1.0\n"
"v_fma_f32 v9, v8, v9, -1.0\n"
"s_waitcnt lgkmcnt(4)\n"
"v_mfma_f32_32x32x16_bf16 a[64:79], v[16:19], v[32:35], a[64:79]\n"
"s_add_u32 m0, m0, 896\n"
"ds_read_b64_tr_b16 v[16:17], v3 offset:4096\n"
"ds_read_b64_tr_b16 v[18:19], v3 offset:13312\n"
"buffer_load_dwordx4 v5, s[44:47], s48 offen offset:256 lds\n"
"v_fma_f32 v11, v10, v11, -1.0\n"
"v_mfma_f32_32x32x16_bf16 a[80:95], v[20:23], v[32:35], a[80:95]\n"
"ds_read_b64_tr_b16 v[20:21], v3 offset:4608\n"
"ds_read_b64_tr_b16 v[22:23], v3 offset:13824\n"
"v_rcp_f32 v9, v9\n"
"s_waitcnt lgkmcnt(4)\n"
"v_mfma_f32_32x32x16_bf16 a[96:111], v[24:27], v[32:35], a[96:111]\n"
"s_add_u32 m0, m0, 896\n"
"ds_read_b64_tr_b16 v[24:25], v3 offset:5120\n"
"ds_read_b64_tr_b16 v[26:27], v3 offset:14336\n"
"buffer_load_dwordx4 v5, s[44:47], s48 offen offset:384 lds\n"
"v_rcp_f32 v11, v11\n"
"v_mfma_f32_32x32x16_bf16 a[112:127], v[28:31], v[32:35], a[112:127]\n"
"ds_read_b64_tr_b16 v[28:29], v3 offset:5632\n"
"ds_read_b64_tr_b16 v[30:31], v3 offset:14848\n"
"v_fma_f32 v8, v64, v9, v64\n"
"v_fma_f32 v10, v65, v11, v65\n"
"s_waitcnt lgkmcnt(4)\n"
"v_mfma_f32_32x32x16_bf16 a[128:143], v[16:19], v[32:35], a[128:143]\n"
"s_add_u32 m0, m0, 896\n"
"ds_read_b64_tr_b16 v[16:17], v3 offset:6144\n"
"ds_read_b64_tr_b16 v[18:19], v3 offset:15360\n"
"buffer_load_dwordx4 v5, s[44:47], s48 offen offset:512 lds\n"
"v_cvt_pk_bf16_f32 v40, v8, v10\n"
"v_mfma_f32_32x32x16_bf16 a[144:159], v[20:23], v[32:35], a[144:159]\n"
"ds_read_b64_tr_b16 v[20:21], v3 offset:6656\n"
"ds_read_b64_tr_b16 v[22:23], v3 offset:15872\n"
"v_exp_f32 v12, v66\n"
"s_waitcnt lgkmcnt(4)\n"
"v_mfma_f32_32x32x16_bf16 a[160:175], v[24:27], v[32:35], a[160:175]\n"
"s_add_u32 m0, m0, 896\n"
"ds_read_b64_tr_b16 v[24:25], v3 offset:7168\n"
"ds_read_b64_tr_b16 v[26:27], v3 offset:16384\n"
"buffer_load_dwordx4 v5, s[44:47], s48 offen offset:640 lds\n"
"v_exp_f32 v14, v67\n"
"v_mfma_f32_32x32x16_bf16 a[176:191], v[28:31], v[32:35], a[176:191]\n"
"ds_read_b64_tr_b16 v[28:29], v3 offset:7680\n"
"ds_read_b64_tr_b16 v[30:31], v3 offset:16896\n"
"v_fma_f32 v13, v12, -0.5, -1.0\n"
"s_waitcnt lgkmcnt(4)\n"
"v_mfma_f32_32x32x16_bf16 a[192:207], v[16:19], v[32:35], a[192:207]\n"
"s_add_u32 m0, m0, 896\n"
"ds_read_b64_tr_b16 v[16:17], v3 offset:8192\n"
"ds_read_b64_tr_b16 v[18:19], v3 offset:17408\n"
"buffer_load_dwordx4 v5, s[44:47], s48 offen offset:768 lds\n"
"v_fma_f32 v15, v14, -0.5, -1.0\n"
"v_mfma_f32_32x32x16_bf16 a[208:223], v[20:23], v[32:35], a[208:223]\n"
"ds_read_b64_tr_b16 v[20:21], v3 offset:8704\n"
"ds_read_b64_tr_b16 v[22:23], v3 offset:17920\n"
"v_fma_f32 v13, v12, v13, -1.0\n"
"v_fma_f32 v15, v14, v15, -1.0\n"
"s_waitcnt lgkmcnt(4)\n"
"v_mfma_f32_32x32x16_bf16 a[224:239], v[24:27], v[32:35], a[224:239]\n"
"s_add_u32 m0, m0, 896\n"
"ds_read_b64_tr_b16 v[24:25], v3 offset:18432\n"
"ds_read_b64_tr_b16 v[26:27], v3 offset:27648\n"
"buffer_load_dwordx4 v5, s[44:47], s48 offen offset:896 lds\n"
"v_rcp_f32 v13, v13\n"
"v_mfma_f32_32x32x16_bf16 a[240:255], v[28:31], v[32:35], a[240:255]\n"
"ds_read_b64_tr_b16 v[28:29], v3 offset:18944\n"
"ds_read_b64_tr_b16 v[30:31], v3 offset:28160\n"
"v_rcp_f32 v15, v15\n"
"s_waitcnt lgkmcnt(4)\n"
"v_mfma_f32_32x32x16_bf16 v[224:239], v[16:19], v[32:35], v[224:239]\n"
"s_add_u32 m0, m0, 896\n"
"ds_read_b64_tr_b16 v[16:17], v3 offset:19456\n"
"ds_read_b64_tr_b16 v[18:19], v3 offset:28672\n"
"buffer_load_dwordx4 v5, s[44:47], s48 offen offset:1024 lds\n"
"v_fma_f32 v12, v66, v13, v66\n"
"v_mfma_f32_32x32x16_bf16 v[240:255], v[20:23], v[32:35], v[240:255]\n"
"ds_read_b64_tr_b16 v[20:21], v3 offset:19968\n"
"ds_read_b64_tr_b16 v[22:23], v3 offset:29184\n"
"v_fma_f32 v14, v67, v15, v67\n"
"v_cvt_pk_bf16_f32 v41, v12, v14\n"
"s_waitcnt lgkmcnt(4)\n"
"v_mfma_f32_32x32x16_bf16 a[0:15], v[24:27], v[36:39], a[0:15]\n"
"ds_read_b64_tr_b16 v[24:25], v3 offset:20480\n"
"ds_read_b64_tr_b16 v[26:27], v3 offset:29696\n"
"ds_read_b128 v[64:67], v6 offset:128\n"
"v_exp_f32 v8, v68\n"
"v_mfma_f32_32x32x16_bf16 a[16:31], v[28:31], v[36:39], a[16:31]\n"
"ds_read_b64_tr_b16 v[28:29], v3 offset:20992\n"
"ds_read_b64_tr_b16 v[30:31], v3 offset:30208\n"
"v_exp_f32 v10, v69\n"
"s_waitcnt lgkmcnt(5)\n"
"v_mfma_f32_32x32x16_bf16 a[32:47], v[16:19], v[36:39], a[32:47]\n"
"ds_read_b64_tr_b16 v[16:17], v3 offset:21504\n"
"ds_read_b64_tr_b16 v[18:19], v3 offset:30720\n"
"v_fma_f32 v9, v8, -0.5, -1.0\n"
"v_fma_f32 v11, v10, -0.5, -1.0\n"
"v_mfma_f32_32x32x16_bf16 a[48:63], v[20:23], v[36:39], a[48:63]\n"
"ds_read_b64_tr_b16 v[20:21], v3 offset:22016\n"
"ds_read_b64_tr_b16 v[22:23], v3 offset:31232\n"
"v_fma_f32 v9, v8, v9, -1.0\n"
"v_fma_f32 v11, v10, v11, -1.0\n"
"s_waitcnt lgkmcnt(4)\n"
"v_mfma_f32_32x32x16_bf16 a[64:79], v[24:27], v[36:39], a[64:79]\n"
"ds_read_b64_tr_b16 v[24:25], v3 offset:22528\n"
"ds_read_b64_tr_b16 v[26:27], v3 offset:31744\n"
"v_rcp_f32 v9, v9\n"
"v_mfma_f32_32x32x16_bf16 a[80:95], v[28:31], v[36:39], a[80:95]\n"
"ds_read_b64_tr_b16 v[28:29], v3 offset:23040\n"
"ds_read_b64_tr_b16 v[30:31], v3 offset:32256\n"
"v_rcp_f32 v11, v11\n"
"v_fma_f32 v8, v68, v9, v68\n"
"s_waitcnt lgkmcnt(4)\n"
"v_mfma_f32_32x32x16_bf16 a[96:111], v[16:19], v[36:39], a[96:111]\n"
"ds_read_b64_tr_b16 v[16:17], v3 offset:23552\n"
"ds_read_b64_tr_b16 v[18:19], v3 offset:32768\n"
"v_fma_f32 v10, v69, v11, v69\n"
"v_cvt_pk_bf16_f32 v42, v8, v10\n"
"s_mov_b32 s52, s56\n"
"s_mul_i32 s51, s52, 73728\n"
"s_add_u32 s51, s51, s58\n"
"s_add_u32 s56, s52, 1\n"
"s_cmp_eq_u32 s56, 6\n"
"s_cselect_b32 s56, 0, s56\n"
"s_lshl_b32 s50, s56, 7\n"
"s_add_u32 s50, s50, s57\n"
"s_add_u32 s49, s49, 1\n"
"v_mfma_f32_32x32x16_bf16 a[112:127], v[20:23], v[36:39], a[112:127]\n"
"ds_read_b64_tr_b16 v[20:21], v3 offset:24064\n"
"ds_read_b64_tr_b16 v[22:23], v3 offset:33280\n"
"v_exp_f32 v12, v70\n"
"s_waitcnt lgkmcnt(4)\n"
"v_mfma_f32_32x32x16_bf16 a[128:143], v[24:27], v[36:39], a[128:143]\n"
"ds_read_b64_tr_b16 v[24:25], v3 offset:24576\n"
"ds_read_b64_tr_b16 v[26:27], v3 offset:33792\n"
"v_exp_f32 v14, v71\n"
"v_fma_f32 v13, v12, -0.5, -1.0\n"
"v_mfma_f32_32x32x16_bf16 a[144:159], v[28:31], v[36:39], a[144:159]\n"
"ds_read_b64_tr_b16 v[28:29], v3 offset:25088\n"
"ds_read_b64_tr_b16 v[30:31], v3 offset:34304\n"
"v_fma_f32 v15, v14, -0.5, -1.0\n"
"v_fma_f32 v13, v12, v13, -1.0\n"
"s_waitcnt lgkmcnt(4)\n"
"v_mfma_f32_32x32x16_bf16 a[160:175], v[16:19], v[36:39], a[160:175]\n"
"ds_read_b64_tr_b16 v[16:17], v3 offset:25600\n"
"ds_read_b64_tr_b16 v[18:19], v3 offset:34816\n"
"v_fma_f32 v15, v14, v15, -1.0\n"
"v_rcp_f32 v13, v13\n"
"v_mfma_f32_32x32x16_bf16 a[176:191], v[20:23], v[36:39], a[176:191]\n"
"ds_read_b64_tr_b16 v[20:21], v3 offset:26112\n"
"ds_read_b64_tr_b16 v[22:23], v3 offset:35328\n"
"v_rcp_f32 v15, v15\n"
"s_waitcnt lgkmcnt(4)\n"
"v_mfma_f32_32x32x16_bf16 a[192:207], v[24:27], v[36:39], a[192:207]\n"
"ds_read_b64_tr_b16 v[24:25], v3 offset:26624\n"
"ds_read_b64_tr_b16 v[26:27], v3 offset:35840\n"
"v_fma_f32 v12, v70, v13, v70\n"
"v_fma_f32 v14, v71, v15, v71\n"
"v_mfma_f32_32x32x16_bf16 a[208:223], v[28:31], v[36:39], a[208:223]\n"
"ds_read_b64_tr_b16 v[28:29], v3 offset:27136\n"
"ds_read_b64_tr_b16 v[30:31], v3 offset:36352\n"
"v_cvt_pk_bf16_f32 v43, v12, v14\n"
"ds_read_b128 v[68:71], v6 offset:160\n"
"s_cmp_eq_u32 s49, 6\n"
"s_cbranch_scc1 LAST_%=\n"
"s_waitcnt lgkmcnt(5)\n"
"v_mfma_f32_32x32x16_bf16 a[224:239], v[16:19], v[36:39], a[224:239]\n"
"s_waitcnt vmcnt(9)\n"
"s_barrier\n"
"ds_read_b64_tr_b16 v[16:17], v0 offset:0\n"
"ds_read_b64_tr_b16 v[18:19], v0 offset:256\n"
"v_mfma_f32_32x32x16_bf16 a[240:255], v[20:23], v[36:39], a[240:255]\n"
"ds_read_b64_tr_b16 v[20:21], v0 offset:2048\n"
"ds_read_b64_tr_b16 v[22:23], v0 offset:2304\n"
"s_waitcnt lgkmcnt(5)\n"
"v_mfma_f32_32x32x16_bf16 v[224:239], v[24:27], v[36:39], v[224:239]\n"
"ds_read_b64_tr_b16 v[24:25], v0 offset:4096\n"
"ds_read_b64_tr_b16 v[26:27], v0 offset:4352\n"
"v_mfma_f32_32x32x16_bf16 v[240:255], v[28:31], v[36:39], v[240:255]\n"
"ds_read_b64_tr_b16 v[28:29], v0 offset:6144\n"
"ds_read_b64_tr_b16 v[30:31], v0 offset:6400\n"
"s_branch LOOP_%=\n"
"FIRST_%=:\n"
"s_nop 7\n"
"v_exp_f32 v8, v56\n"
"v_exp_f32 v10, v57\n"
"v_fma_f32 v9, v8, -0.5, -1.0\n"
"v_fma_f32 v11, v10, -0.5, -1.0\n"
"v_fma_f32 v9, v8, v9, -1.0\n"
"s_add_u32 s40, s40, 2654208\n"
"s_addc_u32 s41, s41, 0\n"
"s_add_u32 m0, s53, 36864\n"
"s_nop 0\n"
"buffer_load_dwordx4 v4, s[40:43], s24 offen lds\n"
"v_fma_f32 v11, v10, v11, -1.0\n"
"v_rcp_f32 v9, v9\n"
"v_rcp_f32 v11, v11\n"
"v_fma_f32 v8, v56, v9, v56\n"
"v_fma_f32 v10, v57, v11, v57\n"
"s_add_u32 m0, m0, 1024\n"
"s_nop 0\n"
"buffer_load_dwordx4 v4, s[40:43], s25 offen lds\n"
"v_cvt_pk_bf16_f32 v36, v8, v10\n"
"v_exp_f32 v12, v58\n"
"v_exp_f32 v14, v59\n"
"v_fma_f32 v13, v12, -0.5, -1.0\n"
"v_fma_f32 v15, v14, -0.5, -1.0\n"
"s_add_u32 m0, m0, 1024\n"
"s_nop 0\n"
"buffer_load_dwordx4 v4, s[40:43], s26 offen lds\n"
"v_fma_f32 v13, v12, v13, -1.0\n"
"v_fma_f32 v15, v14, v15, -1.0\n"
"v_rcp_f32 v13, v13\n"
"v_rcp_f32 v15, v15\n"
"v_fma_f32 v12, v58, v13, v58\n"
"s_add_u32 m0, m0, 1024\n"
"s_nop 0\n"
"buffer_load_dwordx4 v4, s[40:43], s27 offen lds\n"
"v_fma_f32 v14, v59, v15, v59\n"
"v_cvt_pk_bf16_f32 v37, v12, v14\n"
"ds_read_b128 v[56:59], v6 offset:64\n"
"v_exp_f32 v8, v60\n"
"v_exp_f32 v10, v61\n"
"s_add_u32 m0, m0, 1024\n"
"s_nop 0\n"
"buffer_load_dwordx4 v4, s[40:43], s28 offen lds\n"
"v_fma_f32 v9, v8, -0.5, -1.0\n"
"v_fma_f32 v11, v10, -0.5, -1.0\n"
"v_fma_f32 v9, v8, v9, -1.0\n"
"v_fma_f32 v11, v10, v11, -1.0\n"
"v_rcp_f32 v9, v9\n"
"s_add_u32 m0, m0, 1024\n"
"s_nop 0\n"
"buffer_load_dwordx4 v4, s[40:43], s29 offen lds\n"
"v_rcp_f32 v11, v11\n"
"v_fma_f32 v8, v60, v9, v60\n"
"v_fma_f32 v10, v61, v11, v61\n"
"v_cvt_pk_bf16_f32 v38, v8, v10\n"
"v_exp_f32 v12, v62\n"
"s_add_u32 m0, m0, 1024\n"
"s_nop 0\n"
"buffer_load_dwordx4 v4, s[40:43], s30 offen lds\n"
"v_exp_f32 v14, v63\n"
"v_fma_f32 v13, v12, -0.5, -1.0\n"
"v_fma_f32 v15, v14, -0.5, -1.0\n"
"v_fma_f32 v13, v12, v13, -1.0\n"
"v_fma_f32 v15, v14, v15, -1.0\n"
"s_add_u32 m0, m0, 1024\n"
"s_nop 0\n"
"buffer_load_dwordx4 v4, s[40:43], s31 offen lds\n"
"v_rcp_f32 v13, v13\n"
"v_rcp_f32 v15, v15\n"
"v_fma_f32 v12, v62, v13, v62\n"
"v_fma_f32 v14, v63, v15, v63\n"
"v_cvt_pk_bf16_f32 v39, v12, v14\n"
"s_add_u32 m0, m0, 1024\n"
"s_nop 0\n"
"buffer_load_dwordx4 v4, s[40:43], s32 offen lds\n"
"ds_read_b128 v[60:63], v6 offset:96\n"
"s_waitcnt vmcnt(18)\n"
"s_barrier\n"
"ds_read_b64_tr_b16 v[16:17], v3 offset:0\n"
"ds_read_b64_tr_b16 v[18:19], v3 offset:9216\n"
"ds_read_b64_tr_b16 v[20:21], v3 offset:512\n"
"ds_read_b64_tr_b16 v[22:23], v3 offset:9728\n"
"ds_read_b64_tr_b16 v[24:25], v3 offset:1024\n"
"ds_read_b64_tr_b16 v[26:27], v3 offset:10240\n"
"ds_read_b64_tr_b16 v[28:29], v3 offset:1536\n"
"ds_read_b64_tr_b16 v[30:31], v3 offset:10752\n"
"s_waitcnt lgkmcnt(0)\n"
"s_branch S3_%=\n"
"LAST_%=:\n"
"s_waitcnt lgkmcnt(5)\n"
"v_mfma_f32_32x32x16_bf16 a[224:239], v[16:19], v[36:39], a[224:239]\n"
"s_waitcnt vmcnt(0)\n"
"s_barrier\n"
"ds_read_b64_tr_b16 v[16:17], v2 offset:0\n"
"ds_read_b64_tr_b16 v[18:19], v2 offset:9216\n"
"v_mfma_f32_32x32x16_bf16 a[240:255], v[20:23], v[36:39], a[240:255]\n"
"ds_read_b64_tr_b16 v[20:21], v2 offset:512\n"
"ds_read_b64_tr_b16 v[22:23], v2 offset:9728\n"
"s_waitcnt lgkmcnt(5)\n"
"v_mfma_f32_32x32x16_bf16 v[224:239], v[24:27], v[36:39], v[224:239]\n"
"ds_read_b64_tr_b16 v[24:25], v2 offset:1024\n"
"ds_read_b64_tr_b16 v[26:27], v2 offset:10240\n"
"v_mfma_f32_32x32x16_bf16 v[240:255], v[28:31], v[36:39], v[240:255]\n"
"ds_read_b64_tr_b16 v[28:29], v2 offset:1536\n"
"ds_read_b64_tr_b16 v[30:31], v2 offset:10752\n"
"s_waitcnt lgkmcnt(4)\n"
"v_mfma_f32_32x32x16_bf16 a[0:15], v[16:19], v[40:43], a[0:15]\n"
"ds_read_b64_tr_b16 v[16:17], v2 offset:2048\n"
"ds_read_b64_tr_b16 v[18:19], v2 offset:11264\n"
"v_exp_f32 v8, v72\n"
"v_exp_f32 v10, v73\n"
"v_mfma_f32_32x32x16_bf16 a[16:31], v[20:23], v[40:43], a[16:31]\n"
"ds_read_b64_tr_b16 v[20:21], v2 offset:2560\n"
"ds_read_b64_tr_b16 v[22:23], v2 offset:11776\n"
"v_fma_f32 v9, v8, -0.5, -1.0\n"
"v_fma_f32 v11, v10, -0.5, -1.0\n"
"v_fma_f32 v9, v8, v9, -1.0\n"
"v_fma_f32 v11, v10, v11, -1.0\n"
"s_waitcnt lgkmcnt(4)\n"
"v_mfma_f32_32x32x16_bf16 a[32:47], v[24:27], v[40:43], a[32:47]\n"
"ds_read_b64_tr_b16 v[24:25], v2 offset:3072\n"
"ds_read_b64_tr_b16 v[26:27], v2 offset:12288\n"
"v_rcp_f32 v9, v9\n"
"v_rcp_f32 v11, v11\n"
"v_mfma_f32_32x32x16_bf16 a[48:63], v[28:31], v[40:43], a[48:63]\n"
"ds_read_b64_tr_b16 v[28:29], v2 offset:3584\n"
"ds_read_b64_tr_b16 v[30:31], v2 offset:12800\n"
"v_fma_f32 v8, v72, v9, v72\n"
"v_fma_f32 v10, v73, v11, v73\n"
"v_cvt_pk_bf16_f32 v44, v8, v10\n"
"s_waitcnt lgkmcnt(4)\n"
"v_mfma_f32_32x32x16_bf16 a[64:79], v[16:19], v[40:43], a[64:79]\n"
"ds_read_b64_tr_b16 v[16:17], v2 offset:4096\n"
"ds_read_b64_tr_b16 v[18:19], v2 offset:13312\n"
"v_exp_f32 v12, v74\n"
"v_exp_f32 v14, v75\n"
"v_mfma_f32_32x32x16_bf16 a[80:95], v[20:23], v[40:43], a[80:95]\n"
"ds_read_b64_tr_b16 v[20:21], v2 offset:4608\n"
"ds_read_b64_tr_b16 v[22:23], v2 offset:13824\n"
"v_fma_f32 v13, v12, -0.5, -1.0\n"
"v_fma_f32 v15, v14, -0.5, -1.0\n"
"v_fma_f32 v13, v12, v13, -1.0\n"
"v_fma_f32 v15, v14, v15, -1.0\n"
"s_waitcnt lgkmcnt(4)\n"
"v_mfma_f32_32x32x16_bf16 a[96:111], v[24:27], v[40:43], a[96:111]\n"
"ds_read_b64_tr_b16 v[24:25], v2 offset:5120\n"
"ds_read_b64_tr_b16 v[26:27], v2 offset:14336\n"
"v_rcp_f32 v13, v13\n"
"v_rcp_f32 v15, v15\n"
"v_mfma_f32_32x32x16_bf16 a[112:127], v[28:31], v[40:43], a[112:127]\n"
"ds_read_b64_tr_b16 v[28:29], v2 offset:5632\n"
"ds_read_b64_tr_b16 v[30:31], v2 offset:14848\n"
"v_fma_f32 v12, v74, v13, v74\n"
"v_fma_f32 v14, v75, v15, v75\n"
"v_cvt_pk_bf16_f32 v45, v12, v14\n"
"s_waitcnt lgkmcnt(4)\n"
"v_mfma_f32_32x32x16_bf16 a[128:143], v[16:19], v[40:43], a[128:143]\n"
"ds_read_b64_tr_b16 v[16:17], v2 offset:6144\n"
"ds_read_b64_tr_b16 v[18:19], v2 offset:15360\n"
"v_exp_f32 v8, v76\n"
"v_exp_f32 v10, v77\n"
"v_mfma_f32_32x32x16_bf16 a[144:159], v[20:23], v[40:43], a[144:159]\n"
"ds_read_b64_tr_b16 v[20:21], v2 offset:6656\n"
"ds_read_b64_tr_b16 v[22:23], v2 offset:15872\n"
"v_fma_f32 v9, v8, -0.5, -1.0\n"
"v_fma_f32 v11, v10, -0.5, -1.0\n"
"v_fma_f32 v9, v8, v9, -1.0\n"
"v_fma_f32 v11, v10, v11, -1.0\n"
"s_waitcnt lgkmcnt(4)\n"
"v_mfma_f32_32x32x16_bf16 a[160:175], v[24:27], v[40:43], a[160:175]\n"
"ds_read_b64_tr_b16 v[24:25], v2 offset:7168\n"
"ds_read_b64_tr_b16 v[26:27], v2 offset:16384\n"
"v_rcp_f32 v9, v9\n"
"v_rcp_f32 v11, v11\n"
"v_mfma_f32_32x32x16_bf16 a[176:191], v[28:31], v[40:43], a[176:191]\n"
"ds_read_b64_tr_b16 v[28:29], v2 offset:7680\n"
"ds_read_b64_tr_b16 v[30:31], v2 offset:16896\n"
"v_fma_f32 v8, v76, v9, v76\n"
"v_fma_f32 v10, v77, v11, v77\n"
"v_cvt_pk_bf16_f32 v46, v8, v10\n"
"s_waitcnt lgkmcnt(4)\n"
"v_mfma_f32_32x32x16_bf16 a[192:207], v[16:19], v[40:43], a[192:207]\n"
"ds_read_b64_tr_b16 v[16:17], v2 offset:8192\n"
"ds_read_b64_tr_b16 v[18:19], v2 offset:17408\n"
"v_exp_f32 v12, v78\n"
"v_exp_f32 v14, v79\n"
"v_mfma_f32_32x32x16_bf16 a[208:223], v[20:23], v[40:43], a[208:223]\n"
"ds_read_b64_tr_b16 v[20:21], v2 offset:8704\n"
"ds_read_b64_tr_b16 v[22:23], v2 offset:17920\n"
"v_fma_f32 v13, v12, -0.5, -1.0\n"
"v_fma_f32 v15, v14, -0.5, -1.0\n"
"v_fma_f32 v13, v12, v13, -1.0\n"
"v_fma_f32 v15, v14, v15, -1.0\n"
"s_waitcnt lgkmcnt(4)\n"
"v_mfma_f32_32x32x16_bf16 a[224:239], v[24:27], v[40:43], a[224:239]\n"
"ds_read_b64_tr_b16 v[24:25], v2 offset:18432\n"
"ds_read_b64_tr_b16 v[26:27], v2 offset:27648\n"
"v_rcp_f32 v13, v13\n"
"v_rcp_f32 v15, v15\n"
"v_mfma_f32_32x32x16_bf16 a[240:255], v[28:31], v[40:43], a[240:255]\n"
"ds_read_b64_tr_b16 v[28:29], v2 offset:18944\n"
"ds_read_b64_tr_b16 v[30:31], v2 offset:28160\n"
"v_fma_f32 v12, v78, v13, v78\n"
"v_fma_f32 v14, v79, v15, v79\n"
"v_cvt_pk_bf16_f32 v47, v12, v14\n"
"s_waitcnt lgkmcnt(4)\n"
"v_mfma_f32_32x32x16_bf16 v[224:239], v[16:19], v[40:43], v[224:239]\n"
"ds_read_b64_tr_b16 v[16:17], v2 offset:19456\n"
"ds_read_b64_tr_b16 v[18:19], v2 offset:28672\n"
"v_mfma_f32_32x32x16_bf16 v[240:255], v[20:23], v[40:43], v[240:255]\n"
"ds_read_b64_tr_b16 v[20:21], v2 offset:19968\n"
"ds_read_b64_tr_b16 v[22:23], v2 offset:29184\n"
"s_waitcnt lgkmcnt(4)\n"
"v_mfma_f32_32x32x16_bf16 a[0:15], v[24:27], v[44:47], a[0:15]\n"
"ds_read_b64_tr_b16 v[24:25], v2 offset:20480\n"
"ds_read_b64_tr_b16 v[26:27], v2 offset:29696\n"
"v_mfma_f32_32x32x16_bf16 a[16:31], v[28:31], v[44:47], a[16:31]\n"
"ds_read_b64_tr_b16 v[28:29], v2 offset:20992\n"
"ds_read_b64_tr_b16 v[30:31], v2 offset:30208\n"
"s_waitcnt lgkmcnt(4)\n"
"v_mfma_f32_32x32x16_bf16 a[32:47], v[16:19], v[44:47], a[32:47]\n"
"ds_read_b64_tr_b16 v[16:17], v2 offset:21504\n"
"ds_read_b64_tr_b16 v[18:19], v2 offset:30720\n"
"v_mfma_f32_32x32x16_bf16 a[48:63], v[20:23], v[44:47], a[48:63]\n"
"ds_read_b64_tr_b16 v[20:21], v2 offset:22016\n"
"ds_read_b64_tr_b16 v[22:23], v2 offset:31232\n"
"s_waitcnt lgkmcnt(4)\n"
"v_mfma_f32_32x32x16_bf16 a[64:79], v[24:27], v[44:47], a[64:79]\n"
"ds_read_b64_tr_b16 v[24:25], v2 offset:22528\n"
"ds_read_b64_tr_b16 v[26:27], v2 offset:31744\n"
"v_mfma_f32_32x32x16_bf16 a[80:95], v[28:31], v[44:47], a[80:95]\n"
"ds_read_b64_tr_b16 v[28:29], v2 offset:23040\n"
"ds_read_b64_tr_b16 v[30:31], v2 offset:32256\n"
"s_waitcnt lgkmcnt(4)\n"
"v_mfma_f32_32x32x16_bf16 a[96:111], v[16:19], v[44:47], a[96:111]\n"
"ds_read_b64_tr_b16 v[16:17], v2 offset:23552\n"
"ds_read_b64_tr_b16 v[18:19], v2 offset:32768\n"
"v_mfma_f32_32x32x16_bf16 a[112:127], v[20:23], v[44:47], a[112:127]\n"
"ds_read_b64_tr_b16 v[20:21], v2 offset:24064\n"
"ds_read_b64_tr_b16 v[22:23], v2 offset:33280\n"
"s_waitcnt lgkmcnt(4)\n"
"v_mfma_f32_32x32x16_bf16 a[128:143], v[24:27], v[44:47], a[128:143]\n"
"ds_read_b64_tr_b16 v[24:25], v2 offset:24576\n"
"ds_read_b64_tr_b16 v[26:27], v2 offset:33792\n"
"v_mfma_f32_32x32x16_bf16 a[144:159], v[28:31], v[44:47], a[144:159]\n"
"ds_read_b64_tr_b16 v[28:29], v2 offset:25088\n"
"ds_read_b64_tr_b16 v[30:31], v2 offset:34304\n"
"s_waitcnt lgkmcnt(4)\n"
"v_mfma_f32_32x32x16_bf16 a[160:175], v[16:19], v[44:47], a[160:175]\n"
"ds_read_b64_tr_b16 v[16:17], v2 offset:25600\n"
"ds_read_b64_tr_b16 v[18:19], v2 offset:34816\n"
"v_mfma_f32_32x32x16_bf16 a[176:191], v[20:23], v[44:47], a[176:191]\n"
"ds_read_b64_tr_b16 v[20:21], v2 offset:26112\n"
"ds_read_b64_tr_b16 v[22:23], v2 offset:35328\n"
"s_waitcnt lgkmcnt(4)\n"
"v_mfma_f32_32x32x16_bf16 a[192:207], v[24:27], v[44:47], a[192:207]\n"
"ds_read_b64_tr_b16 v[24:25], v2 offset:26624\n"
"ds_read_b64_tr_b16 v[26:27], v2 offset:35840\n"
"v_mfma_f32_32x32x16_bf16 a[208:223], v[28:31], v[44:47], a[208:223]\n"
"ds_read_b64_tr_b16 v[28:29], v2 offset:27136\n"
"ds_read_b64_tr_b16 v[30:31], v2 offset:36352\n"
"s_waitcnt lgkmcnt(4)\n"
"v_mfma_f32_32x32x16_bf16 a[224:239], v[16:19], v[44:47], a[224:239]\n"
"v_mfma_f32_32x32x16_bf16 a[240:255], v[20:23], v[44:47], a[240:255]\n"
"s_waitcnt lgkmcnt(0)\n"
"v_mfma_f32_32x32x16_bf16 v[224:239], v[24:27], v[44:47], v[224:239]\n"
"v_mfma_f32_32x32x16_bf16 v[240:255], v[28:31], v[44:47], v[240:255]\n"
"s_mov_b64 s[60:61], %11\n"
"v_accvgpr_read_b32 v80, a0\n"
"v_accvgpr_read_b32 v81, a1\n"
"v_accvgpr_read_b32 v82, a2\n"
"v_accvgpr_read_b32 v83, a3\n"
"v_accvgpr_read_b32 v84, a4\n"
"v_accvgpr_read_b32 v85, a5\n"
"v_accvgpr_read_b32 v86, a6\n"
"v_accvgpr_read_b32 v87, a7\n"
"v_accvgpr_read_b32 v88, a8\n"
"v_accvgpr_read_b32 v89, a9\n"
"v_accvgpr_read_b32 v90, a10\n"
"v_accvgpr_read_b32 v91, a11\n"
"v_accvgpr_read_b32 v92, a12\n"
"v_accvgpr_read_b32 v93, a13\n"
"v_accvgpr_read_b32 v94, a14\n"
"v_accvgpr_read_b32 v95, a15\n"
"v_cvt_pk_bf16_f32 v96, v80, v81\n"
"v_cvt_pk_bf16_f32 v97, v82, v83\n"
"v_cvt_pk_bf16_f32 v98, v84, v85\n"
"v_cvt_pk_bf16_f32 v99, v86, v87\n"
"v_cvt_pk_bf16_f32 v100, v88, v89\n"
"v_cvt_pk_bf16_f32 v101, v90, v91\n"
"v_cvt_pk_bf16_f32 v102, v92, v93\n"
"v_cvt_pk_bf16_f32 v103, v94, v95\n"
"global_store_dwordx4 v7, v[96:99], s[60:61] offset:0 sc1\n"
"global_store_dwordx4 v7, v[100:103], s[60:61] offset:1024 sc1\n"
"v_accvgpr_read_b32 v80, a16\n"
"v_accvgpr_read_b32 v81, a17\n"
"v_accvgpr_read_b32 v82, a18\n"
"v_accvgpr_read_b32 v83, a19\n"
"v_accvgpr_read_b32 v84, a20\n"
"v_accvgpr_read_b32 v85, a21\n"
"v_accvgpr_read_b32 v86, a22\n"
"v_accvgpr_read_b32 v87, a23\n"
"v_accvgpr_read_b32 v88, a24\n"
"v_accvgpr_read_b32 v89, a25\n"
"v_accvgpr_read_b32 v90, a26\n"
"v_accvgpr_read_b32 v91, a27\n"
"v_accvgpr_read_b32 v92, a28\n"
"v_accvgpr_read_b32 v93, a29\n"
"v_accvgpr_read_b32 v94, a30\n"
"v_accvgpr_read_b32 v95, a31\n"
"v_cvt_pk_bf16_f32 v104, v80, v81\n"
"v_cvt_pk_bf16_f32 v105, v82, v83\n"
"v_cvt_pk_bf16_f32 v106, v84, v85\n"
"v_cvt_pk_bf16_f32 v107, v86, v87\n"
"v_cvt_pk_bf16_f32 v108, v88, v89\n"
"v_cvt_pk_bf16_f32 v109, v90, v91\n"
"v_cvt_pk_bf16_f32 v110, v92, v93\n"
"v_cvt_pk_bf16_f32 v111, v94, v95\n"
"global_store_dwordx4 v7, v[104:107], s[60:61] offset:2048 sc1\n"
"global_store_dwordx4 v7, v[108:111], s[60:61] offset:3072 sc1\n"
"s_add_u32 s60, s60, 4096\n"
"s_addc_u32 s61, s61, 0\n"
"v_accvgpr_read_b32 v80, a32\n"
"v_accvgpr_read_b32 v81, a33\n"
"v_accvgpr_read_b32 v82, a34\n"
"v_accvgpr_read_b32 v83, a35\n"
"v_accvgpr_read_b32 v84, a36\n"
"v_accvgpr_read_b32 v85, a37\n"
"v_accvgpr_read_b32 v86, a38\n"
"v_accvgpr_read_b32 v87, a39\n"
"v_accvgpr_read_b32 v88, a40\n"
"v_accvgpr_read_b32 v89, a41\n"
"v_accvgpr_read_b32 v90, a42\n"
"v_accvgpr_read_b32 v91, a43\n"
"v_accvgpr_read_b32 v92, a44\n"
"v_accvgpr_read_b32 v93, a45\n"
"v_accvgpr_read_b32 v94, a46\n"
"v_accvgpr_read_b32 v95, a47\n"
"v_cvt_pk_bf16_f32 v112, v80, v81\n"
"v_cvt_pk_bf16_f32 v113, v82, v83\n"
"v_cvt_pk_bf16_f32 v114, v84, v85\n"
"v_cvt_pk_bf16_f32 v115, v86, v87\n"
"v_cvt_pk_bf16_f32 v116, v88, v89\n"
"v_cvt_pk_bf16_f32 v117, v90, v91\n"
"v_cvt_pk_bf16_f32 v118, v92, v93\n"
"v_cvt_pk_bf16_f32 v119, v94, v95\n"
"global_store_dwordx4 v7, v[112:115], s[60:61] offset:0 sc1\n"
"global_store_dwordx4 v7, v[116:119], s[60:61] offset:1024 sc1\n"
"v_accvgpr_read_b32 v80, a48\n"
"v_accvgpr_read_b32 v81, a49\n"
"v_accvgpr_read_b32 v82, a50\n"
"v_accvgpr_read_b32 v83, a51\n"
"v_accvgpr_read_b32 v84, a52\n"
"v_accvgpr_read_b32 v85, a53\n"
"v_accvgpr_read_b32 v86, a54\n"
"v_accvgpr_read_b32 v87, a55\n"
"v_accvgpr_read_b32 v88, a56\n"
"v_accvgpr_read_b32 v89, a57\n"
"v_accvgpr_read_b32 v90, a58\n"
"v_accvgpr_read_b32 v91, a59\n"
"v_accvgpr_read_b32 v92, a60\n"
"v_accvgpr_read_b32 v93, a61\n"
"v_accvgpr_read_b32 v94, a62\n"
"v_accvgpr_read_b32 v95, a63\n"
"v_cvt_pk_bf16_f32 v120, v80, v81\n"
"v_cvt_pk_bf16_f32 v121, v82, v83\n"
"v_cvt_pk_bf16_f32 v122, v84, v85\n"
"v_cvt_pk_bf16_f32 v123, v86, v87\n"
"v_cvt_pk_bf16_f32 v124, v88, v89\n"
"v_cvt_pk_bf16_f32 v125, v90, v91\n"
"v_cvt_pk_bf16_f32 v126, v92, v93\n"
"v_cvt_pk_bf16_f32 v127, v94, v95\n"
"global_store_dwordx4 v7, v[120:123], s[60:61] offset:2048 sc1\n"
"global_store_dwordx4 v7, v[124:127], s[60:61] offset:3072 sc1\n"
"s_add_u32 s60, s60, 4096\n"
"s_addc_u32 s61, s61, 0\n"
"v_accvgpr_read_b32 v80, a64\n"
"v_accvgpr_read_b32 v81, a65\n"
"v_accvgpr_read_b32 v82, a66\n"
"v_accvgpr_read_b32 v83, a67\n"
"v_accvgpr_read_b32 v84, a68\n"
"v_accvgpr_read_b32 v85, a69\n"
"v_accvgpr_read_b32 v86, a70\n"
"v_accvgpr_read_b32 v87, a71\n"
"v_accvgpr_read_b32 v88, a72\n"
"v_accvgpr_read_b32 v89, a73\n"
"v_accvgpr_read_b32 v90, a74\n"
"v_accvgpr_read_b32 v91, a75\n"
"v_accvgpr_read_b32 v92, a76\n"
"v_accvgpr_read_b32 v93, a77\n"
"v_accvgpr_read_b32 v94, a78\n"
"v_accvgpr_read_b32 v95, a79\n"
"v_cvt_pk_bf16_f32 v128, v80, v81\n"
"v_cvt_pk_bf16_f32 v129, v82, v83\n"
"v_cvt_pk_bf16_f32 v130, v84, v85\n"
"v_cvt_pk_bf16_f32 v131, v86, v87\n"
"v_cvt_pk_bf16_f32 v132, v88, v89\n"
"v_cvt_pk_bf16_f32 v133, v90, v91\n"
"v_cvt_pk_bf16_f32 v134, v92, v93\n"
"v_cvt_pk_bf16_f32 v135, v94, v95\n"
"global_store_dwordx4 v7, v[128:131], s[60:61] offset:0 sc1\n"
"global_store_dwordx4 v7, v[132:135], s[60:61] offset:1024 sc1\n"
"v_accvgpr_read_b32 v80, a80\n"
"v_accvgpr_read_b32 v81, a81\n"
"v_accvgpr_read_b32 v82, a82\n"
"v_accvgpr_read_b32 v83, a83\n"
"v_accvgpr_read_b32 v84, a84\n"
"v_accvgpr_read_b32 v85, a85\n"
"v_accvgpr_read_b32 v86, a86\n"
"v_accvgpr_read_b32 v87, a87\n"
"v_accvgpr_read_b32 v88, a88\n"
"v_accvgpr_read_b32 v89, a89\n"
"v_accvgpr_read_b32 v90, a90\n"
"v_accvgpr_read_b32 v91, a91\n"
"v_accvgpr_read_b32 v92, a92\n"
"v_accvgpr_read_b32 v93, a93\n"
"v_accvgpr_read_b32 v94, a94\n"
"v_accvgpr_read_b32 v95, a95\n"
"v_cvt_pk_bf16_f32 v136, v80, v81\n"
"v_cvt_pk_bf16_f32 v137, v82, v83\n"
"v_cvt_pk_bf16_f32 v138, v84, v85\n"
"v_cvt_pk_bf16_f32 v139, v86, v87\n"
"v_cvt_pk_bf16_f32 v140, v88, v89\n"
"v_cvt_pk_bf16_f32 v141, v90, v91\n"
"v_cvt_pk_bf16_f32 v142, v92, v93\n"
"v_cvt_pk_bf16_f32 v143, v94, v95\n"
"global_store_dwordx4 v7, v[136:139], s[60:61] offset:2048 sc1\n"
"global_store_dwordx4 v7, v[140:143], s[60:61] offset:3072 sc1\n"
"s_add_u32 s60, s60, 4096\n"
"s_addc_u32 s61, s61, 0\n"
"v_accvgpr_read_b32 v80, a96\n"
"v_accvgpr_read_b32 v81, a97\n"
"v_accvgpr_read_b32 v82, a98\n"
"v_accvgpr_read_b32 v83, a99\n"
"v_accvgpr_read_b32 v84, a100\n"
"v_accvgpr_read_b32 v85, a101\n"
"v_accvgpr_read_b32 v86, a102\n"
"v_accvgpr_read_b32 v87, a103\n"
"v_accvgpr_read_b32 v88, a104\n"
"v_accvgpr_read_b32 v89, a105\n"
"v_accvgpr_read_b32 v90, a106\n"
"v_accvgpr_read_b32 v91, a107\n"
"v_accvgpr_read_b32 v92, a108\n"
"v_accvgpr_read_b32 v93, a109\n"
"v_accvgpr_read_b32 v94, a110\n"
"v_accvgpr_read_b32 v95, a111\n"
"v_cvt_pk_bf16_f32 v144, v80, v81\n"
"v_cvt_pk_bf16_f32 v145, v82, v83\n"
"v_cvt_pk_bf16_f32 v146, v84, v85\n"
"v_cvt_pk_bf16_f32 v147, v86, v87\n"
"v_cvt_pk_bf16_f32 v148, v88, v89\n"
"v_cvt_pk_bf16_f32 v149, v90, v91\n"
"v_cvt_pk_bf16_f32 v150, v92, v93\n"
"v_cvt_pk_bf16_f32 v151, v94, v95\n"
"global_store_dwordx4 v7, v[144:147], s[60:61] offset:0 sc1\n"
"global_store_dwordx4 v7, v[148:151], s[60:61] offset:1024 sc1\n"
"v_accvgpr_read_b32 v80, a112\n"
"v_accvgpr_read_b32 v81, a113\n"
"v_accvgpr_read_b32 v82, a114\n"
"v_accvgpr_read_b32 v83, a115\n"
"v_accvgpr_read_b32 v84, a116\n"
"v_accvgpr_read_b32 v85, a117\n"
"v_accvgpr_read_b32 v86, a118\n"
"v_accvgpr_read_b32 v87, a119\n"
"v_accvgpr_read_b32 v88, a120\n"
"v_accvgpr_read_b32 v89, a121\n"
"v_accvgpr_read_b32 v90, a122\n"
"v_accvgpr_read_b32 v91, a123\n"
"v_accvgpr_read_b32 v92, a124\n"
"v_accvgpr_read_b32 v93, a125\n"
"v_accvgpr_read_b32 v94, a126\n"
"v_accvgpr_read_b32 v95, a127\n"
"v_cvt_pk_bf16_f32 v152, v80, v81\n"
"v_cvt_pk_bf16_f32 v153, v82, v83\n"
"v_cvt_pk_bf16_f32 v154, v84, v85\n"
"v_cvt_pk_bf16_f32 v155, v86, v87\n"
"v_cvt_pk_bf16_f32 v156, v88, v89\n"
"v_cvt_pk_bf16_f32 v157, v90, v91\n"
"v_cvt_pk_bf16_f32 v158, v92, v93\n"
"v_cvt_pk_bf16_f32 v159, v94, v95\n"
"global_store_dwordx4 v7, v[152:155], s[60:61] offset:2048 sc1\n"
"global_store_dwordx4 v7, v[156:159], s[60:61] offset:3072 sc1\n"
"s_add_u32 s60, s60, 4096\n"
"s_addc_u32 s61, s61, 0\n"
"v_accvgpr_read_b32 v80, a128\n"
"v_accvgpr_read_b32 v81, a129\n"
"v_accvgpr_read_b32 v82, a130\n"
"v_accvgpr_read_b32 v83, a131\n"
"v_accvgpr_read_b32 v84, a132\n"
"v_accvgpr_read_b32 v85, a133\n"
"v_accvgpr_read_b32 v86, a134\n"
"v_accvgpr_read_b32 v87, a135\n"
"v_accvgpr_read_b32 v88, a136\n"
"v_accvgpr_read_b32 v89, a137\n"
"v_accvgpr_read_b32 v90, a138\n"
"v_accvgpr_read_b32 v91, a139\n"
"v_accvgpr_read_b32 v92, a140\n"
"v_accvgpr_read_b32 v93, a141\n"
"v_accvgpr_read_b32 v94, a142\n"
"v_accvgpr_read_b32 v95, a143\n"
"v_cvt_pk_bf16_f32 v160, v80, v81\n"
"v_cvt_pk_bf16_f32 v161, v82, v83\n"
"v_cvt_pk_bf16_f32 v162, v84, v85\n"
"v_cvt_pk_bf16_f32 v163, v86, v87\n"
"v_cvt_pk_bf16_f32 v164, v88, v89\n"
"v_cvt_pk_bf16_f32 v165, v90, v91\n"
"v_cvt_pk_bf16_f32 v166, v92, v93\n"
"v_cvt_pk_bf16_f32 v167, v94, v95\n"
"global_store_dwordx4 v7, v[160:163], s[60:61] offset:0 sc1\n"
"global_store_dwordx4 v7, v[164:167], s[60:61] offset:1024 sc1\n"
"v_accvgpr_read_b32 v80, a144\n"
"v_accvgpr_read_b32 v81, a145\n"
"v_accvgpr_read_b32 v82, a146\n"
"v_accvgpr_read_b32 v83, a147\n"
"v_accvgpr_read_b32 v84, a148\n"
"v_accvgpr_read_b32 v85, a149\n"
"v_accvgpr_read_b32 v86, a150\n"
"v_accvgpr_read_b32 v87, a151\n"
"v_accvgpr_read_b32 v88, a152\n"
"v_accvgpr_read_b32 v89, a153\n"
"v_accvgpr_read_b32 v90, a154\n"
"v_accvgpr_read_b32 v91, a155\n"
"v_accvgpr_read_b32 v92, a156\n"
"v_accvgpr_read_b32 v93, a157\n"
"v_accvgpr_read_b32 v94, a158\n"
"v_accvgpr_read_b32 v95, a159\n"
"v_cvt_pk_bf16_f32 v168, v80, v81\n"
"v_cvt_pk_bf16_f32 v169, v82, v83\n"
"v_cvt_pk_bf16_f32 v170, v84, v85\n"
"v_cvt_pk_bf16_f32 v171, v86, v87\n"
"v_cvt_pk_bf16_f32 v172, v88, v89\n"
"v_cvt_pk_bf16_f32 v173, v90, v91\n"
"v_cvt_pk_bf16_f32 v174, v92, v93\n"
"v_cvt_pk_bf16_f32 v175, v94, v95\n"
"global_store_dwordx4 v7, v[168:171], s[60:61] offset:2048 sc1\n"
"global_store_dwordx4 v7, v[172:175], s[60:61] offset:3072 sc1\n"
"s_add_u32 s60, s60, 4096\n"
"s_addc_u32 s61, s61, 0\n"
"v_accvgpr_read_b32 v80, a160\n"
"v_accvgpr_read_b32 v81, a161\n"
"v_accvgpr_read_b32 v82, a162\n"
"v_accvgpr_read_b32 v83, a163\n"
"v_accvgpr_read_b32 v84, a164\n"
"v_accvgpr_read_b32 v85, a165\n"
"v_accvgpr_read_b32 v86, a166\n"
"v_accvgpr_read_b32 v87, a167\n"
"v_accvgpr_read_b32 v88, a168\n"
"v_accvgpr_read_b32 v89, a169\n"
"v_accvgpr_read_b32 v90, a170\n"
"v_accvgpr_read_b32 v91, a171\n"
"v_accvgpr_read_b32 v92, a172\n"
"v_accvgpr_read_b32 v93, a173\n"
"v_accvgpr_read_b32 v94, a174\n"
"v_accvgpr_read_b32 v95, a175\n"
"v_cvt_pk_bf16_f32 v176, v80, v81\n"
"v_cvt_pk_bf16_f32 v177, v82, v83\n"
"v_cvt_pk_bf16_f32 v178, v84, v85\n"
"v_cvt_pk_bf16_f32 v179, v86, v87\n"
"v_cvt_pk_bf16_f32 v180, v88, v89\n"
"v_cvt_pk_bf16_f32 v181, v90, v91\n"
"v_cvt_pk_bf16_f32 v182, v92, v93\n"
"v_cvt_pk_bf16_f32 v183, v94, v95\n"
"global_store_dwordx4 v7, v[176:179], s[60:61] offset:0 sc1\n"
"global_store_dwordx4 v7, v[180:183], s[60:61] offset:1024 sc1\n"
"v_accvgpr_read_b32 v80, a176\n"
"v_accvgpr_read_b32 v81, a177\n"
"v_accvgpr_read_b32 v82, a178\n"
"v_accvgpr_read_b32 v83, a179\n"
"v_accvgpr_read_b32 v84, a180\n"
"v_accvgpr_read_b32 v85, a181\n"
"v_accvgpr_read_b32 v86, a182\n"
"v_accvgpr_read_b32 v87, a183\n"
"v_accvgpr_read_b32 v88, a184\n"
"v_accvgpr_read_b32 v89, a185\n"
"v_accvgpr_read_b32 v90, a186\n"
"v_accvgpr_read_b32 v91, a187\n"
"v_accvgpr_read_b32 v92, a188\n"
"v_accvgpr_read_b32 v93, a189\n"
"v_accvgpr_read_b32 v94, a190\n"
"v_accvgpr_read_b32 v95, a191\n"
"v_cvt_pk_bf16_f32 v184, v80, v81\n"
"v_cvt_pk_bf16_f32 v185, v82, v83\n"
"v_cvt_pk_bf16_f32 v186, v84, v85\n"
"v_cvt_pk_bf16_f32 v187, v86, v87\n"
"v_cvt_pk_bf16_f32 v188, v88, v89\n"
"v_cvt_pk_bf16_f32 v189, v90, v91\n"
"v_cvt_pk_bf16_f32 v190, v92, v93\n"
"v_cvt_pk_bf16_f32 v191, v94, v95\n"
"global_store_dwordx4 v7, v[184:187], s[60:61] offset:2048 sc1\n"
"global_store_dwordx4 v7, v[188:191], s[60:61] offset:3072 sc1\n"
"s_add_u32 s60, s60, 4096\n"
"s_addc_u32 s61, s61, 0\n"
"v_accvgpr_read_b32 v80, a192\n"
"v_accvgpr_read_b32 v81, a193\n"
"v_accvgpr_read_b32 v82, a194\n"
"v_accvgpr_read_b32 v83, a195\n"
"v_accvgpr_read_b32 v84, a196\n"
"v_accvgpr_read_b32 v85, a197\n"
"v_accvgpr_read_b32 v86, a198\n"
"v_accvgpr_read_b32 v87, a199\n"
"v_accvgpr_read_b32 v88, a200\n"
"v_accvgpr_read_b32 v89, a201\n"
"v_accvgpr_read_b32 v90, a202\n"
"v_accvgpr_read_b32 v91, a203\n"
"v_accvgpr_read_b32 v92, a204\n"
"v_accvgpr_read_b32 v93, a205\n"
"v_accvgpr_read_b32 v94, a206\n"
"v_accvgpr_read_b32 v95, a207\n"
"v_cvt_pk_bf16_f32 v96, v80, v81\n"
"v_cvt_pk_bf16_f32 v97, v82, v83\n"
"v_cvt_pk_bf16_f32 v98, v84, v85\n"
"v_cvt_pk_bf16_f32 v99, v86, v87\n"
"v_cvt_pk_bf16_f32 v100, v88, v89\n"
"v_cvt_pk_bf16_f32 v101, v90, v91\n"
"v_cvt_pk_bf16_f32 v102, v92, v93\n"
"v_cvt_pk_bf16_f32 v103, v94, v95\n"
"global_store_dwordx4 v7, v[96:99], s[60:61] offset:0 sc1\n"
"global_store_dwordx4 v7, v[100:103], s[60:61] offset:1024 sc1\n"
"v_accvgpr_read_b32 v80, a208\n"
"v_accvgpr_read_b32 v81, a209\n"
"v_accvgpr_read_b32 v82, a210\n"
"v_accvgpr_read_b32 v83, a211\n"
"v_accvgpr_read_b32 v84, a212\n"
"v_accvgpr_read_b32 v85, a213\n"
"v_accvgpr_read_b32 v86, a214\n"
"v_accvgpr_read_b32 v87, a215\n"
"v_accvgpr_read_b32 v88, a216\n"
"v_accvgpr_read_b32 v89, a217\n"
"v_accvgpr_read_b32 v90, a218\n"
"v_accvgpr_read_b32 v91, a219\n"
"v_accvgpr_read_b32 v92, a220\n"
"v_accvgpr_read_b32 v93, a221\n"
"v_accvgpr_read_b32 v94, a222\n"
"v_accvgpr_read_b32 v95, a223\n"
"v_cvt_pk_bf16_f32 v104, v80, v81\n"
"v_cvt_pk_bf16_f32 v105, v82, v83\n"
"v_cvt_pk_bf16_f32 v106, v84, v85\n"
"v_cvt_pk_bf16_f32 v107, v86, v87\n"
"v_cvt_pk_bf16_f32 v108, v88, v89\n"
"v_cvt_pk_bf16_f32 v109, v90, v91\n"
"v_cvt_pk_bf16_f32 v110, v92, v93\n"
"v_cvt_pk_bf16_f32 v111, v94, v95\n"
"global_store_dwordx4 v7, v[104:107], s[60:61] offset:2048 sc1\n"
"global_store_dwordx4 v7, v[108:111], s[60:61] offset:3072 sc1\n"
"s_add_u32 s60, s60, 4096\n"
"s_addc_u32 s61, s61, 0\n"
"v_accvgpr_read_b32 v80, a224\n"
"v_accvgpr_read_b32 v81, a225\n"
"v_accvgpr_read_b32 v82, a226\n"
"v_accvgpr_read_b32 v83, a227\n"
"v_accvgpr_read_b32 v84, a228\n"
"v_accvgpr_read_b32 v85, a229\n"
"v_accvgpr_read_b32 v86, a230\n"
"v_accvgpr_read_b32 v87, a231\n"
"v_accvgpr_read_b32 v88, a232\n"
"v_accvgpr_read_b32 v89, a233\n"
"v_accvgpr_read_b32 v90, a234\n"
"v_accvgpr_read_b32 v91, a235\n"
"v_accvgpr_read_b32 v92, a236\n"
"v_accvgpr_read_b32 v93, a237\n"
"v_accvgpr_read_b32 v94, a238\n"
"v_accvgpr_read_b32 v95, a239\n"
"v_cvt_pk_bf16_f32 v112, v80, v81\n"
"v_cvt_pk_bf16_f32 v113, v82, v83\n"
"v_cvt_pk_bf16_f32 v114, v84, v85\n"
"v_cvt_pk_bf16_f32 v115, v86, v87\n"
"v_cvt_pk_bf16_f32 v116, v88, v89\n"
"v_cvt_pk_bf16_f32 v117, v90, v91\n"
"v_cvt_pk_bf16_f32 v118, v92, v93\n"
"v_cvt_pk_bf16_f32 v119, v94, v95\n"
"global_store_dwordx4 v7, v[112:115], s[60:61] offset:0 sc1\n"
"global_store_dwordx4 v7, v[116:119], s[60:61] offset:1024 sc1\n"
"v_accvgpr_read_b32 v80, a240\n"
"v_accvgpr_read_b32 v81, a241\n"
"v_accvgpr_read_b32 v82, a242\n"
"v_accvgpr_read_b32 v83, a243\n"
"v_accvgpr_read_b32 v84, a244\n"
"v_accvgpr_read_b32 v85, a245\n"
"v_accvgpr_read_b32 v86, a246\n"
"v_accvgpr_read_b32 v87, a247\n"
"v_accvgpr_read_b32 v88, a248\n"
"v_accvgpr_read_b32 v89, a249\n"
"v_accvgpr_read_b32 v90, a250\n"
"v_accvgpr_read_b32 v91, a251\n"
"v_accvgpr_read_b32 v92, a252\n"
"v_accvgpr_read_b32 v93, a253\n"
"v_accvgpr_read_b32 v94, a254\n"
"v_accvgpr_read_b32 v95, a255\n"
"v_cvt_pk_bf16_f32 v120, v80, v81\n"
"v_cvt_pk_bf16_f32 v121, v82, v83\n"
"v_cvt_pk_bf16_f32 v122, v84, v85\n"
"v_cvt_pk_bf16_f32 v123, v86, v87\n"
"v_cvt_pk_bf16_f32 v124, v88, v89\n"
"v_cvt_pk_bf16_f32 v125, v90, v91\n"
"v_cvt_pk_bf16_f32 v126, v92, v93\n"
"v_cvt_pk_bf16_f32 v127, v94, v95\n"
"global_store_dwordx4 v7, v[120:123], s[60:61] offset:2048 sc1\n"
"global_store_dwordx4 v7, v[124:127], s[60:61] offset:3072 sc1\n"
"s_add_u32 s60, s60, 4096\n"
"s_addc_u32 s61, s61, 0\n"
"s_nop 7\n"
"v_cvt_pk_bf16_f32 v128, v224, v225\n"
"v_cvt_pk_bf16_f32 v129, v226, v227\n"
"v_cvt_pk_bf16_f32 v130, v228, v229\n"
"v_cvt_pk_bf16_f32 v131, v230, v231\n"
"v_cvt_pk_bf16_f32 v132, v232, v233\n"
"v_cvt_pk_bf16_f32 v133, v234, v235\n"
"v_cvt_pk_bf16_f32 v134, v236, v237\n"
"v_cvt_pk_bf16_f32 v135, v238, v239\n"
"global_store_dwordx4 v7, v[128:131], s[60:61] offset:0 sc1\n"
"global_store_dwordx4 v7, v[132:135], s[60:61] offset:1024 sc1\n"
"v_cvt_pk_bf16_f32 v136, v240, v241\n"
"v_cvt_pk_bf16_f32 v137, v242, v243\n"
"v_cvt_pk_bf16_f32 v138, v244, v245\n"
"v_cvt_pk_bf16_f32 v139, v246, v247\n"
"v_cvt_pk_bf16_f32 v140, v248, v249\n"
"v_cvt_pk_bf16_f32 v141, v250, v251\n"
"v_cvt_pk_bf16_f32 v142, v252, v253\n"
"v_cvt_pk_bf16_f32 v143, v254, v255\n"
"global_store_dwordx4 v7, v[136:139], s[60:61] offset:2048 sc1\n"
"global_store_dwordx4 v7, v[140:143], s[60:61] offset:3072 sc1\n"
"s_add_u32 s60, s60, 4096\n"
"s_addc_u32 s61, s61, 0\n"
"s_nop 1\n"
"v_mbcnt_lo_u32_b32 v0, -1, 0\n"
"v_mbcnt_hi_u32_b32 v0, -1, v0\n"
"v_and_b32 v1, 31, v0\n"
"v_mul_u32_u24 v1, 1152, v1\n"
"v_and_b32 v2, 32, v0\n"
"v_add_u32 v1, v1, v2\n"
"v_lshlrev_b32 v0, 4, v0\n"
"s_lshl_b32 s44, %19, 2\n"
"s_add_u32 s44, s44, %13\n"
"s_add_u32 s45, s44, 16\n"
"s_add_u32 s46, %19, 32\n"
"s_lshr_b32 s47, s44, 1\n"
"s_lshl_b32 s47, s47, 6\n"
"s_and_b32 s48, s44, 1\n"
"s_lshl_b32 s48, s48, 4\n"
"s_add_u32 s47, s47, s48\n"
"v_add_u32 v2, s47, v1\n"
"s_lshr_b32 s47, s45, 1\n"
"s_lshl_b32 s47, s47, 6\n"
"s_and_b32 s48, s45, 1\n"
"s_lshl_b32 s48, s48, 4\n"
"s_add_u32 s47, s47, s48\n"
"v_add_u32 v3, s47, v1\n"
"s_lshr_b32 s47, s46, 1\n"
"s_lshl_b32 s47, s47, 6\n"
"s_and_b32 s48, s46, 1\n"
"s_lshl_b32 s48, s48, 4\n"
"s_add_u32 s47, s47, s48\n"
"s_lshl_b32 s48, %13, 2\n"
"s_add_u32 s47, s47, s48\n"
"v_add_u32 v4, s47, v1\n"
"s_lshl_b32 s47, s44, 10\n"
"v_add_u32 v5, s47, v0\n"
"v_add_u32 v10, 0x4000, v5\n"
"s_lshl_b32 s47, s46, 10\n"
"s_add_u32 s48, s48, s47\n"
"v_add_u32 v6, s48, v0\n"
"s_waitcnt vmcnt(0)\n"
"v_mov_b32 v7, 0\n"
"v_mov_b32 v8, 1\n"
"s_mov_b64 exec, 1\n"
"global_atomic_add v7, v8, %18 offset:0 sc1\n"
"s_mov_b64 exec, -1\n"
"global_load_dwordx4 v[160:163], v2, %16\n"
"global_load_dwordx4 v[164:167], v3, %16\n"
"global_load_dword v168, v4, %16\n"
"s_mov_b32 s44, 0x3f80\n"
"s_mov_b32 s45, 0x3f800000\n"
"s_mov_b32 s46, 0x3ef5fdf0\n"
"s_cmp_lg_u32 %13, 0\n"
"s_cbranch_scc1 TGO0_%=\n"
"TSP0_%=:\n"
"global_load_dword v9, v7, %18 offset:0 sc1\n"
"s_waitcnt vmcnt(0)\n"
"v_readfirstlane_b32 s47, v9\n"
"s_cmp_ge_u32 s47, 64\n"
"s_cbranch_scc1 TGO0_%=\n"
"s_sleep 2\n"
"s_branch TSP0_%=\n"
"TGO0_%=:\n"
"s_barrier\n"
"s_mov_b64 s[40:41], %15\n"
"global_load_dwordx4 v[16:19], v5, s[40:41] sc1\n"
"s_add_u32 s40, s40, 2359296\n"
"s_addc_u32 s41, s41, 0\n"
"global_load_dwordx4 v[20:23], v5, s[40:41] sc1\n"
"s_add_u32 s40, s40, 2359296\n"
"s_addc_u32 s41, s41, 0\n"
"global_load_dwordx4 v[24:27], v5, s[40:41] sc1\n"
"s_add_u32 s40, s40, 2359296\n"
"s_addc_u32 s41, s41, 0\n"
"global_load_dwordx4 v[28:31], v5, s[40:41] sc1\n"
"s_add_u32 s40, s40, 2359296\n"
"s_addc_u32 s41, s41, 0\n"
"global_load_dwordx4 v[32:35], v5, s[40:41] sc1\n"
"s_add_u32 s40, s40, 2359296\n"
"s_addc_u32 s41, s41, 0\n"
"global_load_dwordx4 v[36:39], v5, s[40:41] sc1\n"
"s_add_u32 s40, s40, 2359296\n"
"s_addc_u32 s41, s41, 0\n"
"global_load_dwordx4 v[40:43], v5, s[40:41] sc1\n"
"s_add_u32 s40, s40, 2359296\n"
"s_addc_u32 s41, s41, 0\n"
"global_load_dwordx4 v[44:47], v5, s[40:41] sc1\n"
"s_add_u32 s40, s40, 2359296\n"
"s_addc_u32 s41, s41, 0\n"
"global_load_dwordx4 v[48:51], v5, s[40:41] sc1\n"
"s_add_u32 s40, s40, 2359296\n"
"s_addc_u32 s41, s41, 0\n"
"global_load_dwordx4 v[52:55], v5, s[40:41] sc1\n"
"s_add_u32 s40, s40, 2359296\n"
"s_addc_u32 s41, s41, 0\n"
"global_load_dwordx4 v[56:59], v5, s[40:41] sc1\n"
"s_add_u32 s40, s40, 2359296\n"
"s_addc_u32 s41, s41, 0\n"
"global_load_dwordx4 v[60:63], v5, s[40:41] sc1\n"
"s_add_u32 s40, s40, 2359296\n"
"s_addc_u32 s41, s41, 0\n"
"global_load_dwordx4 v[64:67], v5, s[40:41] sc1\n"
"s_add_u32 s40, s40, 2359296\n"
"s_addc_u32 s41, s41, 0\n"
"global_load_dwordx4 v[68:71], v5, s[40:41] sc1\n"
"s_add_u32 s40, s40, 2359296\n"
"s_addc_u32 s41, s41, 0\n"
"global_load_dwordx4 v[72:75], v5, s[40:41] sc1\n"
"s_add_u32 s40, s40, 2359296\n"
"s_addc_u32 s41, s41, 0\n"
"global_load_dwordx4 v[76:79], v5, s[40:41] sc1\n"
"s_mov_b64 s[40:41], %15\n"
"global_load_dwordx4 v[80:83], v10, s[40:41] sc1\n"
"s_add_u32 s40, s40, 2359296\n"
"s_addc_u32 s41, s41, 0\n"
"global_load_dwordx4 v[84:87], v10, s[40:41] sc1\n"
"s_add_u32 s40, s40, 2359296\n"
"s_addc_u32 s41, s41, 0\n"
"global_load_dwordx4 v[88:91], v10, s[40:41] sc1\n"
"s_add_u32 s40, s40, 2359296\n"
"s_addc_u32 s41, s41, 0\n"
"global_load_dwordx4 v[92:95], v10, s[40:41] sc1\n"
"s_add_u32 s40, s40, 2359296\n"
"s_addc_u32 s41, s41, 0\n"
"global_load_dwordx4 v[96:99], v10, s[40:41] sc1\n"
"s_add_u32 s40, s40, 2359296\n"
"s_addc_u32 s41, s41, 0\n"
"global_load_dwordx4 v[100:103], v10, s[40:41] sc1\n"
"s_add_u32 s40, s40, 2359296\n"
"s_addc_u32 s41, s41, 0\n"
"global_load_dwordx4 v[104:107], v10, s[40:41] sc1\n"
"s_add_u32 s40, s40, 2359296\n"
"s_addc_u32 s41, s41, 0\n"
"global_load_dwordx4 v[108:111], v10, s[40:41] sc1\n"
"s_add_u32 s40, s40, 2359296\n"
"s_addc_u32 s41, s41, 0\n"
"global_load_dwordx4 v[112:115], v10, s[40:41] sc1\n"
"s_add_u32 s40, s40, 2359296\n"
"s_addc_u32 s41, s41, 0\n"
"global_load_dwordx4 v[116:119], v10, s[40:41] sc1\n"
"s_add_u32 s40, s40, 2359296\n"
"s_addc_u32 s41, s41, 0\n"
"global_load_dwordx4 v[120:123], v10, s[40:41] sc1\n"
"s_add_u32 s40, s40, 2359296\n"
"s_addc_u32 s41, s41, 0\n"
"global_load_dwordx4 v[124:127], v10, s[40:41] sc1\n"
"s_add_u32 s40, s40, 2359296\n"
"s_addc_u32 s41, s41, 0\n"
"global_load_dwordx4 v[128:131], v10, s[40:41] sc1\n"
"s_add_u32 s40, s40, 2359296\n"
"s_addc_u32 s41, s41, 0\n"
"global_load_dwordx4 v[132:135], v10, s[40:41] sc1\n"
"s_add_u32 s40, s40, 2359296\n"
"s_addc_u32 s41, s41, 0\n"
"global_load_dwordx4 v[136:139], v10, s[40:41] sc1\n"
"s_add_u32 s40, s40, 2359296\n"
"s_addc_u32 s41, s41, 0\n"
"global_load_dwordx4 v[140:143], v10, s[40:41] sc1\n"
"s_mov_b64 s[40:41], %15\n"
"global_load_dword v144, v6, s[40:41] sc1\n"
"s_add_u32 s40, s40, 2359296\n"
"s_addc_u32 s41, s41, 0\n"
"global_load_dword v145, v6, s[40:41] sc1\n"
"s_add_u32 s40, s40, 2359296\n"
"s_addc_u32 s41, s41, 0\n"
"global_load_dword v146, v6, s[40:41] sc1\n"
"s_add_u32 s40, s40, 2359296\n"
"s_addc_u32 s41, s41, 0\n"
"global_load_dword v147, v6, s[40:41] sc1\n"
"s_add_u32 s40, s40, 2359296\n"
"s_addc_u32 s41, s41, 0\n"
"global_load_dword v148, v6, s[40:41] sc1\n"
"s_add_u32 s40, s40, 2359296\n"
"s_addc_u32 s41, s41, 0\n"
"global_load_dword v149, v6, s[40:41] sc1\n"
"s_add_u32 s40, s40, 2359296\n"
"s_addc_u32 s41, s41, 0\n"
"global_load_dword v150, v6, s[40:41] sc1\n"
"s_add_u32 s40, s40, 2359296\n"
"s_addc_u32 s41, s41, 0\n"
"global_load_dword v151, v6, s[40:41] sc1\n"
"s_add_u32 s40, s40, 2359296\n"
"s_addc_u32 s41, s41, 0\n"
"global_load_dword v152, v6, s[40:41] sc1\n"
"s_add_u32 s40, s40, 2359296\n"
"s_addc_u32 s41, s41, 0\n"
"global_load_dword v153, v6, s[40:41] sc1\n"
"s_add_u32 s40, s40, 2359296\n"
"s_addc_u32 s41, s41, 0\n"
"global_load_dword v154, v6, s[40:41] sc1\n"
"s_add_u32 s40, s40, 2359296\n"
"s_addc_u32 s41, s41, 0\n"
"global_load_dword v155, v6, s[40:41] sc1\n"
"s_add_u32 s40, s40, 2359296\n"
"s_addc_u32 s41, s41, 0\n"
"global_load_dword v156, v6, s[40:41] sc1\n"
"s_add_u32 s40, s40, 2359296\n"
"s_addc_u32 s41, s41, 0\n"
"global_load_dword v157, v6, s[40:41] sc1\n"
"s_add_u32 s40, s40, 2359296\n"
"s_addc_u32 s41, s41, 0\n"
"global_load_dword v158, v6, s[40:41] sc1\n"
"s_add_u32 s40, s40, 2359296\n"
"s_addc_u32 s41, s41, 0\n"
"global_load_dword v159, v6, s[40:41] sc1\n"
"s_waitcnt vmcnt(46)\n"
"v_dot2_f32_bf16 v208, v16, s44, 0\n"
"v_dot2_f32_bf16 v209, v16, s45, 0\n"
"v_dot2_f32_bf16 v210, v17, s44, 0\n"
"v_dot2_f32_bf16 v211, v17, s45, 0\n"
"v_dot2_f32_bf16 v212, v18, s44, 0\n"
"v_dot2_f32_bf16 v213, v18, s45, 0\n"
"v_dot2_f32_bf16 v214, v19, s44, 0\n"
"v_dot2_f32_bf16 v215, v19, s45, 0\n"
"v_dot2_f32_bf16 v208, v20, s44, v208\n"
"v_dot2_f32_bf16 v209, v20, s45, v209\n"
"v_dot2_f32_bf16 v210, v21, s44, v210\n"
"v_dot2_f32_bf16 v211, v21, s45, v211\n"
"v_dot2_f32_bf16 v212, v22, s44, v212\n"
"v_dot2_f32_bf16 v213, v22, s45, v213\n"
"v_dot2_f32_bf16 v214, v23, s44, v214\n"
"v_dot2_f32_bf16 v215, v23, s45, v215\n"
"s_waitcnt vmcnt(44)\n"
"v_dot2_f32_bf16 v208, v24, s44, v208\n"
"v_dot2_f32_bf16 v209, v24, s45, v209\n"
"v_dot2_f32_bf16 v210, v25, s44, v210\n"
"v_dot2_f32_bf16 v211, v25, s45, v211\n"
"v_dot2_f32_bf16 v212, v26, s44, v212\n"
"v_dot2_f32_bf16 v213, v26, s45, v213\n"
"v_dot2_f32_bf16 v214, v27, s44, v214\n"
"v_dot2_f32_bf16 v215, v27, s45, v215\n"
"v_dot2_f32_bf16 v208, v28, s44, v208\n"
"v_dot2_f32_bf16 v209, v28, s45, v209\n"
"v_dot2_f32_bf16 v210, v29, s44, v210\n"
"v_dot2_f32_bf16 v211, v29, s45, v211\n"
"v_dot2_f32_bf16 v212, v30, s44, v212\n"
"v_dot2_f32_bf16 v213, v30, s45, v213\n"
"v_dot2_f32_bf16 v214, v31, s44, v214\n"
"v_dot2_f32_bf16 v215, v31, s45, v215\n"
"s_waitcnt vmcnt(42)\n"
"v_dot2_f32_bf16 v208, v32, s44, v208\n"
"v_dot2_f32_bf16 v209, v32, s45, v209\n"
"v_dot2_f32_bf16 v210, v33, s44, v210\n"
"v_dot2_f32_bf16 v211, v33, s45, v211\n"
"v_dot2_f32_bf16 v212, v34, s44, v212\n"
"v_dot2_f32_bf16 v213, v34, s45, v213\n"
"v_dot2_f32_bf16 v214, v35, s44, v214\n"
"v_dot2_f32_bf16 v215, v35, s45, v215\n"
"v_dot2_f32_bf16 v208, v36, s44, v208\n"
"v_dot2_f32_bf16 v209, v36, s45, v209\n"
"v_dot2_f32_bf16 v210, v37, s44, v210\n"
"v_dot2_f32_bf16 v211, v37, s45, v211\n"
"v_dot2_f32_bf16 v212, v38, s44, v212\n"
"v_dot2_f32_bf16 v213, v38, s45, v213\n"
"v_dot2_f32_bf16 v214, v39, s44, v214\n"
"v_dot2_f32_bf16 v215, v39, s45, v215\n"
"s_waitcnt vmcnt(40)\n"
"v_dot2_f32_bf16 v208, v40, s44, v208\n"
"v_dot2_f32_bf16 v209, v40, s45, v209\n"
"v_dot2_f32_bf16 v210, v41, s44, v210\n"
"v_dot2_f32_bf16 v211, v41, s45, v211\n"
"v_dot2_f32_bf16 v212, v42, s44, v212\n"
"v_dot2_f32_bf16 v213, v42, s45, v213\n"
"v_dot2_f32_bf16 v214, v43, s44, v214\n"
"v_dot2_f32_bf16 v215, v43, s45, v215\n"
"v_dot2_f32_bf16 v208, v44, s44, v208\n"
"v_dot2_f32_bf16 v209, v44, s45, v209\n"
"v_dot2_f32_bf16 v210, v45, s44, v210\n"
"v_dot2_f32_bf16 v211, v45, s45, v211\n"
"v_dot2_f32_bf16 v212, v46, s44, v212\n"
"v_dot2_f32_bf16 v213, v46, s45, v213\n"
"v_dot2_f32_bf16 v214, v47, s44, v214\n"
"v_dot2_f32_bf16 v215, v47, s45, v215\n"
"s_waitcnt vmcnt(38)\n"
"v_dot2_f32_bf16 v208, v48, s44, v208\n"
"v_dot2_f32_bf16 v209, v48, s45, v209\n"
"v_dot2_f32_bf16 v210, v49, s44, v210\n"
"v_dot2_f32_bf16 v211, v49, s45, v211\n"
"v_dot2_f32_bf16 v212, v50, s44, v212\n"
"v_dot2_f32_bf16 v213, v50, s45, v213\n"
"v_dot2_f32_bf16 v214, v51, s44, v214\n"
"v_dot2_f32_bf16 v215, v51, s45, v215\n"
"v_dot2_f32_bf16 v208, v52, s44, v208\n"
"v_dot2_f32_bf16 v209, v52, s45, v209\n"
"v_dot2_f32_bf16 v210, v53, s44, v210\n"
"v_dot2_f32_bf16 v211, v53, s45, v211\n"
"v_dot2_f32_bf16 v212, v54, s44, v212\n"
"v_dot2_f32_bf16 v213, v54, s45, v213\n"
"v_dot2_f32_bf16 v214, v55, s44, v214\n"
"v_dot2_f32_bf16 v215, v55, s45, v215\n"
"s_waitcnt vmcnt(36)\n"
"v_dot2_f32_bf16 v208, v56, s44, v208\n"
"v_dot2_f32_bf16 v209, v56, s45, v209\n"
"v_dot2_f32_bf16 v210, v57, s44, v210\n"
"v_dot2_f32_bf16 v211, v57, s45, v211\n"
"v_dot2_f32_bf16 v212, v58, s44, v212\n"
"v_dot2_f32_bf16 v213, v58, s45, v213\n"
"v_dot2_f32_bf16 v214, v59, s44, v214\n"
"v_dot2_f32_bf16 v215, v59, s45, v215\n"
"v_dot2_f32_bf16 v208, v60, s44, v208\n"
"v_dot2_f32_bf16 v209, v60, s45, v209\n"
"v_dot2_f32_bf16 v210, v61, s44, v210\n"
"v_dot2_f32_bf16 v211, v61, s45, v211\n"
"v_dot2_f32_bf16 v212, v62, s44, v212\n"
"v_dot2_f32_bf16 v213, v62, s45, v213\n"
"v_dot2_f32_bf16 v214, v63, s44, v214\n"
"v_dot2_f32_bf16 v215, v63, s45, v215\n"
"s_waitcnt vmcnt(34)\n"
"v_dot2_f32_bf16 v180, v64, s44, 0\n"
"v_dot2_f32_bf16 v181, v64, s45, 0\n"
"v_dot2_f32_bf16 v182, v65, s44, 0\n"
"v_dot2_f32_bf16 v183, v65, s45, 0\n"
"v_dot2_f32_bf16 v184, v66, s44, 0\n"
"v_dot2_f32_bf16 v185, v66, s45, 0\n"
"v_dot2_f32_bf16 v186, v67, s44, 0\n"
"v_dot2_f32_bf16 v187, v67, s45, 0\n"
"v_dot2_f32_bf16 v180, v68, s44, v180\n"
"v_dot2_f32_bf16 v181, v68, s45, v181\n"
"v_dot2_f32_bf16 v182, v69, s44, v182\n"
"v_dot2_f32_bf16 v183, v69, s45, v183\n"
"v_dot2_f32_bf16 v184, v70, s44, v184\n"
"v_dot2_f32_bf16 v185, v70, s45, v185\n"
"v_dot2_f32_bf16 v186, v71, s44, v186\n"
"v_dot2_f32_bf16 v187, v71, s45, v187\n"
"s_waitcnt vmcnt(32)\n"
"v_dot2_f32_bf16 v180, v72, s44, v180\n"
"v_dot2_f32_bf16 v181, v72, s45, v181\n"
"v_dot2_f32_bf16 v182, v73, s44, v182\n"
"v_dot2_f32_bf16 v183, v73, s45, v183\n"
"v_dot2_f32_bf16 v184, v74, s44, v184\n"
"v_dot2_f32_bf16 v185, v74, s45, v185\n"
"v_dot2_f32_bf16 v186, v75, s44, v186\n"
"v_dot2_f32_bf16 v187, v75, s45, v187\n"
"v_dot2_f32_bf16 v180, v76, s44, v180\n"
"v_dot2_f32_bf16 v181, v76, s45, v181\n"
"v_dot2_f32_bf16 v182, v77, s44, v182\n"
"v_dot2_f32_bf16 v183, v77, s45, v183\n"
"v_dot2_f32_bf16 v184, v78, s44, v184\n"
"v_dot2_f32_bf16 v185, v78, s45, v185\n"
"v_dot2_f32_bf16 v186, v79, s44, v186\n"
"v_dot2_f32_bf16 v187, v79, s45, v187\n"
"s_nop 3\n"
"v_exp_f32 v188, -v208\n"
"v_exp_f32 v189, -v209\n"
"v_exp_f32 v190, -v210\n"
"v_exp_f32 v191, -v211\n"
"v_exp_f32 v192, -v212\n"
"v_exp_f32 v193, -v213\n"
"v_exp_f32 v194, -v214\n"
"v_exp_f32 v195, -v215\n"
"v_mul_f32 v208, v208, v180\n"
"v_mul_f32 v209, v209, v181\n"
"v_mul_f32 v210, v210, v182\n"
"v_mul_f32 v211, v211, v183\n"
"v_mul_f32 v212, v212, v184\n"
"v_mul_f32 v213, v213, v185\n"
"v_mul_f32 v214, v214, v186\n"
"v_mul_f32 v215, v215, v187\n"
"v_add_f32 v188, 1.0, v188\n"
"v_add_f32 v189, 1.0, v189\n"
"v_add_f32 v190, 1.0, v190\n"
"v_add_f32 v191, 1.0, v191\n"
"v_add_f32 v192, 1.0, v192\n"
"v_add_f32 v193, 1.0, v193\n"
"v_add_f32 v194, 1.0, v194\n"
"v_add_f32 v195, 1.0, v195\n"
"v_rcp_f32 v188, v188\n"
"v_rcp_f32 v189, v189\n"
"v_rcp_f32 v190, v190\n"
"v_rcp_f32 v191, v191\n"
"v_rcp_f32 v192, v192\n"
"v_rcp_f32 v193, v193\n"
"v_rcp_f32 v194, v194\n"
"v_rcp_f32 v195, v195\n"
"v_lshlrev_b32 v180, 16, v160\n"
"v_and_b32 v181, 0xffff0000, v160\n"
"v_lshlrev_b32 v182, 16, v161\n"
"v_and_b32 v183, 0xffff0000, v161\n"
"v_lshlrev_b32 v184, 16, v162\n"
"v_and_b32 v185, 0xffff0000, v162\n"
"v_lshlrev_b32 v186, 16, v163\n"
"v_and_b32 v187, 0xffff0000, v163\n"
"v_mul_f32 v208, v208, v188\n"
"v_mul_f32 v209, v209, v189\n"
"v_mul_f32 v210, v210, v190\n"
"v_mul_f32 v211, v211, v191\n"
"v_mul_f32 v212, v212, v192\n"
"v_mul_f32 v213, v213, v193\n"
"v_mul_f32 v214, v214, v194\n"
"v_mul_f32 v215, v215, v195\n"
"v_fma_f32 v208, v208, s46, v180\n"
"v_fma_f32 v209, v209, s46, v181\n"
"v_fma_f32 v210, v210, s46, v182\n"
"v_fma_f32 v211, v211, s46, v183\n"
"v_fma_f32 v212, v212, s46, v184\n"
"v_fma_f32 v213, v213, s46, v185\n"
"v_fma_f32 v214, v214, s46, v186\n"
"v_fma_f32 v215, v215, s46, v187\n"
"v_cvt_pk_bf16_f32 v196, v208, v209\n"
"v_cvt_pk_bf16_f32 v197, v210, v211\n"
"v_cvt_pk_bf16_f32 v198, v212, v213\n"
"v_cvt_pk_bf16_f32 v199, v214, v215\n"
"s_waitcnt vmcnt(30)\n"
"v_dot2_f32_bf16 v216, v80, s44, 0\n"
"v_dot2_f32_bf16 v217, v80, s45, 0\n"
"v_dot2_f32_bf16 v218, v81, s44, 0\n"
"v_dot2_f32_bf16 v219, v81, s45, 0\n"
"v_dot2_f32_bf16 v220, v82, s44, 0\n"
"v_dot2_f32_bf16 v221, v82, s45, 0\n"
"v_dot2_f32_bf16 v222, v83, s44, 0\n"
"v_dot2_f32_bf16 v223, v83, s45, 0\n"
"v_dot2_f32_bf16 v216, v84, s44, v216\n"
"v_dot2_f32_bf16 v217, v84, s45, v217\n"
"v_dot2_f32_bf16 v218, v85, s44, v218\n"
"v_dot2_f32_bf16 v219, v85, s45, v219\n"
"v_dot2_f32_bf16 v220, v86, s44, v220\n"
"v_dot2_f32_bf16 v221, v86, s45, v221\n"
"v_dot2_f32_bf16 v222, v87, s44, v222\n"
"v_dot2_f32_bf16 v223, v87, s45, v223\n"
"s_waitcnt vmcnt(28)\n"
"v_dot2_f32_bf16 v216, v88, s44, v216\n"
"v_dot2_f32_bf16 v217, v88, s45, v217\n"
"v_dot2_f32_bf16 v218, v89, s44, v218\n"
"v_dot2_f32_bf16 v219, v89, s45, v219\n"
"v_dot2_f32_bf16 v220, v90, s44, v220\n"
"v_dot2_f32_bf16 v221, v90, s45, v221\n"
"v_dot2_f32_bf16 v222, v91, s44, v222\n"
"v_dot2_f32_bf16 v223, v91, s45, v223\n"
"v_dot2_f32_bf16 v216, v92, s44, v216\n"
"v_dot2_f32_bf16 v217, v92, s45, v217\n"
"v_dot2_f32_bf16 v218, v93, s44, v218\n"
"v_dot2_f32_bf16 v219, v93, s45, v219\n"
"v_dot2_f32_bf16 v220, v94, s44, v220\n"
"v_dot2_f32_bf16 v221, v94, s45, v221\n"
"v_dot2_f32_bf16 v222, v95, s44, v222\n"
"v_dot2_f32_bf16 v223, v95, s45, v223\n"
"s_waitcnt vmcnt(26)\n"
"v_dot2_f32_bf16 v216, v96, s44, v216\n"
"v_dot2_f32_bf16 v217, v96, s45, v217\n"
"v_dot2_f32_bf16 v218, v97, s44, v218\n"
"v_dot2_f32_bf16 v219, v97, s45, v219\n"
"v_dot2_f32_bf16 v220, v98, s44, v220\n"
"v_dot2_f32_bf16 v221, v98, s45, v221\n"
"v_dot2_f32_bf16 v222, v99, s44, v222\n"
"v_dot2_f32_bf16 v223, v99, s45, v223\n"
"v_dot2_f32_bf16 v216, v100, s44, v216\n"
"v_dot2_f32_bf16 v217, v100, s45, v217\n"
"v_dot2_f32_bf16 v218, v101, s44, v218\n"
"v_dot2_f32_bf16 v219, v101, s45, v219\n"
"v_dot2_f32_bf16 v220, v102, s44, v220\n"
"v_dot2_f32_bf16 v221, v102, s45, v221\n"
"v_dot2_f32_bf16 v222, v103, s44, v222\n"
"v_dot2_f32_bf16 v223, v103, s45, v223\n"
"s_waitcnt vmcnt(24)\n"
"v_dot2_f32_bf16 v216, v104, s44, v216\n"
"v_dot2_f32_bf16 v217, v104, s45, v217\n"
"v_dot2_f32_bf16 v218, v105, s44, v218\n"
"v_dot2_f32_bf16 v219, v105, s45, v219\n"
"v_dot2_f32_bf16 v220, v106, s44, v220\n"
"v_dot2_f32_bf16 v221, v106, s45, v221\n"
"v_dot2_f32_bf16 v222, v107, s44, v222\n"
"v_dot2_f32_bf16 v223, v107, s45, v223\n"
"v_dot2_f32_bf16 v216, v108, s44, v216\n"
"v_dot2_f32_bf16 v217, v108, s45, v217\n"
"v_dot2_f32_bf16 v218, v109, s44, v218\n"
"v_dot2_f32_bf16 v219, v109, s45, v219\n"
"v_dot2_f32_bf16 v220, v110, s44, v220\n"
"v_dot2_f32_bf16 v221, v110, s45, v221\n"
"v_dot2_f32_bf16 v222, v111, s44, v222\n"
"v_dot2_f32_bf16 v223, v111, s45, v223\n"
"s_waitcnt vmcnt(22)\n"
"v_dot2_f32_bf16 v216, v112, s44, v216\n"
"v_dot2_f32_bf16 v217, v112, s45, v217\n"
"v_dot2_f32_bf16 v218, v113, s44, v218\n"
"v_dot2_f32_bf16 v219, v113, s45, v219\n"
"v_dot2_f32_bf16 v220, v114, s44, v220\n"
"v_dot2_f32_bf16 v221, v114, s45, v221\n"
"v_dot2_f32_bf16 v222, v115, s44, v222\n"
"v_dot2_f32_bf16 v223, v115, s45, v223\n"
"v_dot2_f32_bf16 v216, v116, s44, v216\n"
"v_dot2_f32_bf16 v217, v116, s45, v217\n"
"v_dot2_f32_bf16 v218, v117, s44, v218\n"
"v_dot2_f32_bf16 v219, v117, s45, v219\n"
"v_dot2_f32_bf16 v220, v118, s44, v220\n"
"v_dot2_f32_bf16 v221, v118, s45, v221\n"
"v_dot2_f32_bf16 v222, v119, s44, v222\n"
"v_dot2_f32_bf16 v223, v119, s45, v223\n"
"s_waitcnt vmcnt(20)\n"
"v_dot2_f32_bf16 v216, v120, s44, v216\n"
"v_dot2_f32_bf16 v217, v120, s45, v217\n"
"v_dot2_f32_bf16 v218, v121, s44, v218\n"
"v_dot2_f32_bf16 v219, v121, s45, v219\n"
"v_dot2_f32_bf16 v220, v122, s44, v220\n"
"v_dot2_f32_bf16 v221, v122, s45, v221\n"
"v_dot2_f32_bf16 v222, v123, s44, v222\n"
"v_dot2_f32_bf16 v223, v123, s45, v223\n"
"v_dot2_f32_bf16 v216, v124, s44, v216\n"
"v_dot2_f32_bf16 v217, v124, s45, v217\n"
"v_dot2_f32_bf16 v218, v125, s44, v218\n"
"v_dot2_f32_bf16 v219, v125, s45, v219\n"
"v_dot2_f32_bf16 v220, v126, s44, v220\n"
"v_dot2_f32_bf16 v221, v126, s45, v221\n"
"v_dot2_f32_bf16 v222, v127, s44, v222\n"
"v_dot2_f32_bf16 v223, v127, s45, v223\n"
"s_waitcnt vmcnt(18)\n"
"v_dot2_f32_bf16 v180, v128, s44, 0\n"
"v_dot2_f32_bf16 v181, v128, s45, 0\n"
"v_dot2_f32_bf16 v182, v129, s44, 0\n"
"v_dot2_f32_bf16 v183, v129, s45, 0\n"
"v_dot2_f32_bf16 v184, v130, s44, 0\n"
"v_dot2_f32_bf16 v185, v130, s45, 0\n"
"v_dot2_f32_bf16 v186, v131, s44, 0\n"
"v_dot2_f32_bf16 v187, v131, s45, 0\n"
"v_dot2_f32_bf16 v180, v132, s44, v180\n"
"v_dot2_f32_bf16 v181, v132, s45, v181\n"
"v_dot2_f32_bf16 v182, v133, s44, v182\n"
"v_dot2_f32_bf16 v183, v133, s45, v183\n"
"v_dot2_f32_bf16 v184, v134, s44, v184\n"
"v_dot2_f32_bf16 v185, v134, s45, v185\n"
"v_dot2_f32_bf16 v186, v135, s44, v186\n"
"v_dot2_f32_bf16 v187, v135, s45, v187\n"
"s_waitcnt vmcnt(16)\n"
"v_dot2_f32_bf16 v180, v136, s44, v180\n"
"v_dot2_f32_bf16 v181, v136, s45, v181\n"
"v_dot2_f32_bf16 v182, v137, s44, v182\n"
"v_dot2_f32_bf16 v183, v137, s45, v183\n"
"v_dot2_f32_bf16 v184, v138, s44, v184\n"
"v_dot2_f32_bf16 v185, v138, s45, v185\n"
"v_dot2_f32_bf16 v186, v139, s44, v186\n"
"v_dot2_f32_bf16 v187, v139, s45, v187\n"
"v_dot2_f32_bf16 v180, v140, s44, v180\n"
"v_dot2_f32_bf16 v181, v140, s45, v181\n"
"v_dot2_f32_bf16 v182, v141, s44, v182\n"
"v_dot2_f32_bf16 v183, v141, s45, v183\n"
"v_dot2_f32_bf16 v184, v142, s44, v184\n"
"v_dot2_f32_bf16 v185, v142, s45, v185\n"
"v_dot2_f32_bf16 v186, v143, s44, v186\n"
"v_dot2_f32_bf16 v187, v143, s45, v187\n"
"s_nop 3\n"
"v_exp_f32 v188, -v216\n"
"v_exp_f32 v189, -v217\n"
"v_exp_f32 v190, -v218\n"
"v_exp_f32 v191, -v219\n"
"v_exp_f32 v192, -v220\n"
"v_exp_f32 v193, -v221\n"
"v_exp_f32 v194, -v222\n"
"v_exp_f32 v195, -v223\n"
"v_mul_f32 v216, v216, v180\n"
"v_mul_f32 v217, v217, v181\n"
"v_mul_f32 v218, v218, v182\n"
"v_mul_f32 v219, v219, v183\n"
"v_mul_f32 v220, v220, v184\n"
"v_mul_f32 v221, v221, v185\n"
"v_mul_f32 v222, v222, v186\n"
"v_mul_f32 v223, v223, v187\n"
"v_add_f32 v188, 1.0, v188\n"
"v_add_f32 v189, 1.0, v189\n"
"v_add_f32 v190, 1.0, v190\n"
"v_add_f32 v191, 1.0, v191\n"
"v_add_f32 v192, 1.0, v192\n"
"v_add_f32 v193, 1.0, v193\n"
"v_add_f32 v194, 1.0, v194\n"
"v_add_f32 v195, 1.0, v195\n"
"v_rcp_f32 v188, v188\n"
"v_rcp_f32 v189, v189\n"
"v_rcp_f32 v190, v190\n"
"v_rcp_f32 v191, v191\n"
"v_rcp_f32 v192, v192\n"
"v_rcp_f32 v193, v193\n"
"v_rcp_f32 v194, v194\n"
"v_rcp_f32 v195, v195\n"
"v_lshlrev_b32 v180, 16, v164\n"
"v_and_b32 v181, 0xffff0000, v164\n"
"v_lshlrev_b32 v182, 16, v165\n"
"v_and_b32 v183, 0xffff0000, v165\n"
"v_lshlrev_b32 v184, 16, v166\n"
"v_and_b32 v185, 0xffff0000, v166\n"
"v_lshlrev_b32 v186, 16, v167\n"
"v_and_b32 v187, 0xffff0000, v167\n"
"v_mul_f32 v216, v216, v188\n"
"v_mul_f32 v217, v217, v189\n"
"v_mul_f32 v218, v218, v190\n"
"v_mul_f32 v219, v219, v191\n"
"v_mul_f32 v220, v220, v192\n"
"v_mul_f32 v221, v221, v193\n"
"v_mul_f32 v222, v222, v194\n"
"v_mul_f32 v223, v223, v195\n"
"v_fma_f32 v216, v216, s46, v180\n"
"v_fma_f32 v217, v217, s46, v181\n"
"v_fma_f32 v218, v218, s46, v182\n"
"v_fma_f32 v219, v219, s46, v183\n"
"v_fma_f32 v220, v220, s46, v184\n"
"v_fma_f32 v221, v221, s46, v185\n"
"v_fma_f32 v222, v222, s46, v186\n"
"v_fma_f32 v223, v223, s46, v187\n"
"v_cvt_pk_bf16_f32 v200, v216, v217\n"
"v_cvt_pk_bf16_f32 v201, v218, v219\n"
"v_cvt_pk_bf16_f32 v202, v220, v221\n"
"v_cvt_pk_bf16_f32 v203, v222, v223\n"
"s_waitcnt vmcnt(14)\n"
"v_dot2_f32_bf16 v224, v144, s44, 0\n"
"v_dot2_f32_bf16 v225, v144, s45, 0\n"
"s_nop 1\n"
"v_dot2_f32_bf16 v224, v145, s44, v224\n"
"v_dot2_f32_bf16 v225, v145, s45, v225\n"
"s_nop 1\n"
"s_waitcnt vmcnt(12)\n"
"v_dot2_f32_bf16 v224, v146, s44, v224\n"
"v_dot2_f32_bf16 v225, v146, s45, v225\n"
"s_nop 1\n"
"v_dot2_f32_bf16 v224, v147, s44, v224\n"
"v_dot2_f32_bf16 v225, v147, s45, v225\n"
"s_nop 1\n"
"s_waitcnt vmcnt(10)\n"
"v_dot2_f32_bf16 v224, v148, s44, v224\n"
"v_dot2_f32_bf16 v225, v148, s45, v225\n"
"s_nop 1\n"
"v_dot2_f32_bf16 v224, v149, s44, v224\n"
"v_dot2_f32_bf16 v225, v149, s45, v225\n"
"s_nop 1\n"
"s_waitcnt vmcnt(8)\n"
"v_dot2_f32_bf16 v224, v150, s44, v224\n"
"v_dot2_f32_bf16 v225, v150, s45, v225\n"
"s_nop 1\n"
"v_dot2_f32_bf16 v224, v151, s44, v224\n"
"v_dot2_f32_bf16 v225, v151, s45, v225\n"
"s_nop 1\n"
"s_waitcnt vmcnt(6)\n"
"v_dot2_f32_bf16 v224, v152, s44, v224\n"
"v_dot2_f32_bf16 v225, v152, s45, v225\n"
"s_nop 1\n"
"v_dot2_f32_bf16 v224, v153, s44, v224\n"
"v_dot2_f32_bf16 v225, v153, s45, v225\n"
"s_nop 1\n"
"s_waitcnt vmcnt(4)\n"
"v_dot2_f32_bf16 v224, v154, s44, v224\n"
"v_dot2_f32_bf16 v225, v154, s45, v225\n"
"s_nop 1\n"
"v_dot2_f32_bf16 v224, v155, s44, v224\n"
"v_dot2_f32_bf16 v225, v155, s45, v225\n"
"s_nop 1\n"
"s_waitcnt vmcnt(2)\n"
"v_dot2_f32_bf16 v180, v156, s44, 0\n"
"v_dot2_f32_bf16 v181, v156, s45, 0\n"
"s_nop 1\n"
"v_dot2_f32_bf16 v180, v157, s44, v180\n"
"v_dot2_f32_bf16 v181, v157, s45, v181\n"
"s_nop 1\n"
"s_waitcnt vmcnt(0)\n"
"v_dot2_f32_bf16 v180, v158, s44, v180\n"
"v_dot2_f32_bf16 v181, v158, s45, v181\n"
"s_nop 1\n"
"v_dot2_f32_bf16 v180, v159, s44, v180\n"
"v_dot2_f32_bf16 v181, v159, s45, v181\n"
"s_nop 1\n"
"s_nop 3\n"
"v_exp_f32 v188, -v224\n"
"v_exp_f32 v189, -v225\n"
"v_mul_f32 v224, v224, v180\n"
"v_mul_f32 v225, v225, v181\n"
"v_add_f32 v188, 1.0, v188\n"
"v_add_f32 v189, 1.0, v189\n"
"v_rcp_f32 v188, v188\n"
"v_rcp_f32 v189, v189\n"
"v_lshlrev_b32 v180, 16, v168\n"
"v_and_b32 v181, 0xffff0000, v168\n"
"v_mul_f32 v224, v224, v188\n"
"v_mul_f32 v225, v225, v189\n"
"v_fma_f32 v224, v224, s46, v180\n"
"v_fma_f32 v225, v225, s46, v181\n"
"v_cvt_pk_bf16_f32 v204, v224, v225\n"
"global_store_dwordx4 v2, v[196:199], %17\n"
"global_store_dwordx4 v3, v[200:203], %17\n"
"global_store_dword v4, v204, %17\n"
"s_nop 1\n"
"CEN_%=:\n"
      "s_endpgm\n"
      :: "v"(g1), "v"(g2), "v"(dw1), "v"(dw2), "v"(brd), "v"(l * 16), "v"(bi), "s"(b1 + role * 384), "s"(w1), "s"(w2), "s"(xp), "s"(op),
         "s"(c0), "s"(wv), "s"(role), TAIL_OPS
      :
"v8","v9","v10","v11","v12","v13","v14","v15","v16","v17","v18","v19","v20","v21","v22","v23",
"v24","v25","v26","v27","v28","v29","v30","v31","v32","v33","v34","v35","v36","v37","v38","v39",
"v40","v41","v42","v43","v44","v45","v46","v47","v48","v49","v50","v51","v52","v53","v54","v55",
"v56","v57","v58","v59","v60","v61","v62","v63","v64","v65","v66","v67","v68","v69","v70","v71",
"v72","v73","v74","v75","v76","v77","v78","v79","v80","v81","v82","v83","v84","v85","v86","v87",
"v88","v89","v90","v91","v92","v93","v94","v95","v96","v97","v98","v99","v100","v101","v102","v103",
"v104","v105","v106","v107","v108","v109","v110","v111","v112","v113","v114","v115","v116","v117","v118","v119",
"v120","v121","v122","v123","v124","v125","v126","v127","v128","v129","v130","v131","v132","v133","v134","v135",
"v136","v137","v138","v139","v140","v141","v142","v143","v144","v145","v146","v147","v148","v149","v150","v151",
"v152","v153","v154","v155","v156","v157","v158","v159","v160","v161","v162","v163","v164","v165","v166","v167",
"v168","v169","v170","v171","v172","v173","v174","v175","v176","v177","v178","v179","v180","v181","v182","v183",
"v184","v185","v186","v187","v188","v189","v190","v191","v192","v193","v194","v195","v196","v197","v198","v199",
"v200","v201","v202","v203","v204","v205","v206","v207","v208","v209","v210","v211","v212","v213","v214","v215",
"v216","v217","v218","v219","v220","v221","v222","v223","v224","v225","v226","v227","v228","v229","v230","v231",
"v232","v233","v234","v235","v236","v237","v238","v239","v240","v241","v242","v243","v244","v245","v246","v247",
"v248","v249","v250","v251","v252","v253","v254","v255","a0","a1","a2","a3","a4","a5","a6","a7",
"a8","a9","a10","a11","a12","a13","a14","a15","a16","a17","a18","a19","a20","a21","a22","a23",
"a24","a25","a26","a27","a28","a29","a30","a31","a32","a33","a34","a35","a36","a37","a38","a39",
"a40","a41","a42","a43","a44","a45","a46","a47","a48","a49","a50","a51","a52","a53","a54","a55",
"a56","a57","a58","a59","a60","a61","a62","a63","a64","a65","a66","a67","a68","a69","a70","a71",
"a72","a73","a74","a75","a76","a77","a78","a79","a80","a81","a82","a83","a84","a85","a86","a87",
"a88","a89","a90","a91","a92","a93","a94","a95","a96","a97","a98","a99","a100","a101","a102","a103",
"a104","a105","a106","a107","a108","a109","a110","a111","a112","a113","a114","a115","a116","a117","a118","a119",
"a120","a121","a122","a123","a124","a125","a126","a127","a128","a129","a130","a131","a132","a133","a134","a135",
"a136","a137","a138","a139","a140","a141","a142","a143","a144","a145","a146","a147","a148","a149","a150","a151",
"a152","a153","a154","a155","a156","a157","a158","a159","a160","a161","a162","a163","a164","a165","a166","a167",
"a168","a169","a170","a171","a172","a173","a174","a175","a176","a177","a178","a179","a180","a181","a182","a183",
"a184","a185","a186","a187","a188","a189","a190","a191","a192","a193","a194","a195","a196","a197","a198","a199",
"a200","a201","a202","a203","a204","a205","a206","a207","a208","a209","a210","a211","a212","a213","a214","a215",
"a216","a217","a218","a219","a220","a221","a222","a223","a224","a225","a226","a227","a228","a229","a230","a231",
"a232","a233","a234","a235","a236","a237","a238","a239","a240","a241","a242","a243","a244","a245","a246","a247",
"a248","a249","a250","a251","a252","a253","a254","a255","s24","s25","s26","s27","s28","s29","s30","s31",
"s32","s33","s34","s35","s40","s41","s42","s43","s44","s45","s46","s47","s48","s49","s50","s51",
"s52","s53","s54","s55","s56","s57","s58","s59","s60","s61","s62","scc","memory"
  );
  __builtin_unreachable();
}

extern "C" void kernel_launch(void* const* d_in, const int* in_sizes, int n_in, void* d_out, int out_size, void* d_ws, size_t ws_size,
                              hipStream_t stream) {
  const u16 *x = (const u16*)d_in[0], *wqkv = (const u16*)d_in[1], *wphi = (const u16*)d_in[2], *wo = (const u16*)d_in[3];
  const u16 *w1 = (const u16*)d_in[5], *b1 = (const u16*)d_in[6], *w2 = (const u16*)d_in[7];
  char* ws = (char*)d_ws;
  (void)hipFuncSetAttribute((const void*)main_kernel, hipFuncAttributeMaxDynamicSharedMemorySize, LDS_B);
  prep_kernel<<<NB_FOLD + NB_LN + NB_WO + 6 + NB_PF, 320, 0, stream>>>(x, wqkv, wphi, wo, ws, (const char*)w1, (const char*)w2, (const char*)d_out);
  main_kernel<<<256, 256, LDS_B, stream>>>(w1, b1, w2, ws, x, (u16*)d_out);
}
```
